```python
import torch

def _softmax(x, axis=-1):
    return torch.softmax(x, dim=axis)
def _gelu(x):
    return torch.nn.functional.gelu(x, approximate="tanh")
def _silu(x):
    return torch.nn.functional.silu(x)
def _sigmoid(x):
    return torch.sigmoid(x)
def _layernorm(x, axis=-1, eps=1e-5):
    m = x.mean(dim=axis, keepdim=True)
    v = x.var(dim=axis, keepdim=True, unbiased=False)
    return (x - m) / torch.sqrt(v + eps)
def _scatter_add(tgt, idx, src, axis=0):
    return tgt.clone().index_add_(axis, idx, src)
def _segment_sum(src, seg, n):
    out = torch.zeros((n,) + src.shape[1:], dtype=src.dtype)
    return out.index_add_(0, seg, src)
def _pad(x, pw):
    flat = tuple(v for pair in reversed(pw) for v in pair)
    return torch.nn.functional.pad(x, flat)

kernel_name = 'parallel_sum__linear_attn_block__dense_activation_relu__prompt'

def setup_inputs():

    _w = (lambda a: (lambda t: t.to(torch.bfloat16) if t.is_floating_point() else t)(torch.as_tensor(a)))
    def _A_setup():
        B, S, D = 1, 2048, 512
        return {
            "x": _w((lambda *s: torch.randn(*s, dtype=torch.bfloat16) * 0.05)(B, S, D)),
            "wqkv": _w((lambda *s: torch.randn(*s, dtype=torch.bfloat16) * 0.2)(D, 3 * D)),
            "wo": _w((lambda *s: torch.randn(*s, dtype=torch.bfloat16) * 0.2)(D, D)),
        }
    _A_in = _A_setup()

    def _B_setup():
        B, S, D, Dff = 1, 2048, 512, 1376
        return {
            "x": _w((lambda *s: torch.randn(*s, dtype=torch.bfloat16) * 0.05)(B, S, D)),
            "w1": _w((lambda *s: torch.randn(*s, dtype=torch.bfloat16) * 0.05)(D, Dff)),
            "w2": _w((lambda *s: torch.randn(*s, dtype=torch.bfloat16) * 0.05)(Dff, D)),
        }
    _B_in = _B_setup()

    return {
        "A_x": _A_in["x"],
        "A_wqkv": _A_in["wqkv"],
        "A_wo": _A_in["wo"],
        "B_x": _B_in["x"],
        "B_w1": _B_in["w1"],
        "B_w2": _B_in["w2"],
    }

def reference(A_x, A_wqkv, A_wo, B_x, B_w1, B_w2):
    def _A_ref(x, wqkv, wo):
        B, S, D = x.shape
        H = 32; Dh = D // H
        qkv = (lambda x, *d: x.permute(*d))((x @ wqkv).reshape(B, S, 3, H, Dh), 2, 0, 3, 1, 4)
        q, k, v = qkv[0], qkv[1], qkv[2]
        phi = lambda t: torch.where(t > 0, t + 1, torch.exp(t)) + 1e-6
        pq, pk = phi(q), phi(k)
        kv = torch.einsum("bhsd,bhse->bhde", pk, v)
        z = torch.einsum("bhsd->bhd", pk)
        num = torch.einsum("bhqd,bhde->bhqe", pq, kv)
        den = torch.einsum("bhqd,bhd->bhq", pq, z)[..., None] + 1e-6
        o = num / den
        return (lambda x, *d: x.permute(*d))(o, 0, 2, 1, 3).reshape(B, S, D) @ wo

    def _B_ref(x, w1, w2):
        h = torch.einsum("bsd,df->bsf", x, w1)
        zero = torch.zeros_like(h)
        h = torch.maximum(h, zero)
        return torch.einsum("bsf,fd->bsd", h, w2)

    x = A_x
    xn = _layernorm(x, axis=-1)
    return x + _A_ref(xn, A_wqkv, A_wo) + _B_ref(xn, B_w1, B_w2)
```

```cpp
#include <hip/hip_runtime.h>
#include <hip/hip_bf16.h>
typedef unsigned short u16;
__device__ __forceinline__ float bf2f(u16 v){ return __uint_as_float(((unsigned)v)<<16); }
__device__ __forceinline__ u16 f2bf(float f){ __hip_bfloat16 b = __float2bfloat16(f); return *(u16*)&b; }
constexpr int S=2048, D=512, F=1376, H=32;

__global__ void k_ln(const u16* x, float* xn){
  int r = blockIdx.x; int t = threadIdx.x;
  float v[8]; float s=0;
  for(int i=0;i<8;i++){ v[i]=bf2f(x[r*D+t*8+i]); s+=v[i]; }
  for(int o=32;o;o>>=1) s+=__shfl_xor(s,o);
  float m=s/D; float q=0;
  for(int i=0;i<8;i++){ float d=v[i]-m; q+=d*d; }
  for(int o=32;o;o>>=1) q+=__shfl_xor(q,o);
  float rs=1.0f/sqrtf(q/D+1e-5f);
  for(int i=0;i<8;i++) xn[r*D+t*8+i]=(v[i]-m)*rs;
}
__global__ void k_gemm(const float* A, const u16* B, float* C, int N, int K, int lda, int ldc, int mode){
  __shared__ float As[16][17], Bs[16][17];
  int tx=threadIdx.x&15, ty=threadIdx.x>>4;
  int row=blockIdx.y*16+ty, col=blockIdx.x*16+tx;
  float acc=0;
  for(int k=0;k<K;k+=16){
    As[ty][tx]=A[row*lda+k+tx];
    Bs[ty][tx]=bf2f(B[(k+ty)*N+col]);
    __syncthreads();
    for(int j=0;j<16;j++) acc+=As[ty][j]*Bs[j][tx];
    __syncthreads();
  }
  if(mode==1) acc=fmaxf(acc,0.f);
  C[row*ldc+col]=acc;
}
__device__ __forceinline__ float phi(float t){ return (t>0? t+1.f: __expf(t))+1e-6f; }
__global__ void k_kv(const float* qkv, float* kv, float* z){
  int h=blockIdx.x; int d=threadIdx.x>>4, e=threadIdx.x&15;
  float a=0, zz=0;
  for(int s=0;s<S;s++){ float pk=phi(qkv[s*1536+512+h*16+d]); a+=pk*qkv[s*1536+1024+h*16+e]; zz+=pk; }
  kv[h*256+d*16+e]=a; if(e==0) z[h*16+d]=zz;
}
__global__ void k_o(const float* qkv, const float* kv, const float* z, float* o){
  int s=blockIdx.x; int c=threadIdx.x; int h=c>>4, e=c&15;
  float num=0, den=0;
  for(int d=0;d<16;d++){ float pq=phi(qkv[s*1536+h*16+d]); num+=pq*kv[h*256+d*16+e]; den+=pq*z[h*16+d]; }
  o[s*D+c]=num/(den+1e-6f);
}
__global__ void k_fin(const u16* x, const float* a, const float* b, u16* out){
  int i=blockIdx.x*256+threadIdx.x;
  out[i]=f2bf(bf2f(x[i])+a[i]+b[i]);
}
extern "C" void kernel_launch(void* const* d_in, const int* in_sizes, int n_in, void* d_out, int out_size, void* d_ws, size_t ws_size, hipStream_t stream){
  const u16* x=(const u16*)d_in[0]; const u16* wqkv=(const u16*)d_in[1]; const u16* wo=(const u16*)d_in[2];
  const u16* w1=(const u16*)d_in[4]; const u16* w2=(const u16*)d_in[5];
  float* ws=(float*)d_ws;
  float* xn=ws; float* qkv=xn+S*D; float* h=qkv+S*1536; float* kv=h+S*F; float* z=kv+H*256; float* o=z+H*16; float* a=o+S*D; float* b=a+S*D;
  k_ln<<<S,64,0,stream>>>(x,xn);
  k_gemm<<<dim3(1536/16,S/16),256,0,stream>>>(xn,wqkv,qkv,1536,D,D,1536,0);
  k_gemm<<<dim3(F/16,S/16),256,0,stream>>>(xn,w1,h,F,D,D,F,1);
  k_kv<<<H,256,0,stream>>>(qkv,kv,z);
  k_o<<<S,512,0,stream>>>(qkv,kv,z,o);
  k_gemm<<<dim3(D/16,S/16),256,0,stream>>>(o,wo,a,D,D,D,D,0);
  k_gemm<<<dim3(D/16,S/16),256,0,stream>>>(h,w2,b,D,F,F,D,0);
  k_fin<<<S*D/256,256,0,stream>>>(x,a,b,(u16*)d_out);
}
```

```cpp
#include <hip/hip_runtime.h>

typedef unsigned u32;
typedef unsigned long long u64;
typedef u32 u32x4 __attribute__((ext_vector_type(4)));
typedef u32 u32x2 __attribute__((ext_vector_type(2)));
typedef short s16x4 __attribute__((ext_vector_type(4)));
typedef short bf16x8 __attribute__((ext_vector_type(8)));
typedef float f32x16 __attribute__((ext_vector_type(16)));
typedef float f32x4 __attribute__((ext_vector_type(4)));
typedef float f32x2 __attribute__((ext_vector_type(2)));
typedef __bf16 bf2 __attribute__((ext_vector_type(2)));
#define LAS __attribute__((address_space(3)))
#define DI __device__ __forceinline__

namespace {
constexpr int SLOT = 32768;
constexpr u32 WS_E = 0, WS_ARR = 4096, WS_F = 8192, WS_KVF = 24576, WS_Z = 32768, WS_KV = 65536,
              WS_A2 = 1u << 20, A2_TILE = 118 * 1024, WS_END = WS_A2 + 64 * A2_TILE;
constexpr u32 SPIN_CAP = 1u << 17;

DI u32x4 mk_srd(const void* p, u32 bytes) {
  const u64 a = (u64)p;
  return u32x4{(u32)a, (u32)(a >> 32) & 0xffffu, bytes, 0x00020000u};
}
DI void dma16(u32 lds, u32 voff, u32x4 srd, u32 soff) {
  asm volatile("s_mov_b32 m0, %0\n\ts_nop 0\n\tbuffer_load_dwordx4 %1, %2, %3 offen lds" ::"s"(lds), "v"(voff), "s"(srd), "s"(soff) : "memory");
}
DI u32x4 ld16(u32 voff, u32x4 srd, u32 soff) {
  u32x4 r;
  asm volatile("buffer_load_dwordx4 %0, %1, %2, %3 offen" : "=v"(r) : "v"(voff), "s"(srd), "s"(soff) : "memory");
  return r;
}
DI u32x4 ld16_sc1(u32 voff, u32x4 srd, u32 soff) {
  u32x4 r;
  asm volatile("buffer_load_dwordx4 %0, %1, %2, %3 offen sc1" : "=v"(r) : "v"(voff), "s"(srd), "s"(soff) : "memory");
  return r;
}
DI u32x2 ld8_sc1(u32 voff, u32x4 srd, u32 soff) {
  u32x2 r;
  asm volatile("buffer_load_dwordx2 %0, %1, %2, %3 offen sc1" : "=v"(r) : "v"(voff), "s"(srd), "s"(soff) : "memory");
  return r;
}
DI u32 ld4_sc1(u32 voff, u32x4 srd, u32 soff) {
  u32 r;
  asm volatile("buffer_load_dword %0, %1, %2, %3 offen sc1" : "=v"(r) : "v"(voff), "s"(srd), "s"(soff) : "memory");
  return r;
}
DI void st16_sc1(u32x4 v, u32 voff, u32x4 srd, u32 soff) {
  asm volatile("buffer_store_dwordx4 %0, %1, %2, %3 offen sc1\n\ts_nop 1" ::"v"(v), "v"(voff), "s"(srd), "s"(soff) : "memory");
}
DI void st16(u32x4 v, u32 voff, u32x4 srd, u32 soff) {
  asm volatile("buffer_store_dwordx4 %0, %1, %2, %3 offen\n\ts_nop 1" ::"v"(v), "v"(voff), "s"(srd), "s"(soff) : "memory");
}
DI void st8_sc1(u32x2 v, u32 voff, u32x4 srd, u32 soff) {
  asm volatile("buffer_store_dwordx2 %0, %1, %2, %3 offen sc1" ::"v"(v), "v"(voff), "s"(srd), "s"(soff) : "memory");
}
DI void st4_sc1(u32 v, u32 voff, u32x4 srd, u32 soff) {
  asm volatile("buffer_store_dword %0, %1, %2, %3 offen sc1" ::"v"(v), "v"(voff), "s"(srd), "s"(soff) : "memory");
}
#define WAITV(n) asm volatile("s_waitcnt vmcnt(" #n ")" ::: "memory")
#define WAITV1(n, a) asm volatile("s_waitcnt vmcnt(" #n ")" : "+v"(a)::"memory")
#define WAITV4(n, a, b, c, d) asm volatile("s_waitcnt vmcnt(" #n ")" : "+v"(a), "+v"(b), "+v"(c), "+v"(d)::"memory")
#define SB() __builtin_amdgcn_sched_barrier(0)
#define BAR() do { SB(); asm volatile("s_waitcnt lgkmcnt(0)\n\ts_barrier" ::: "memory"); SB(); } while (0)
#define BARV(n) do { SB(); asm volatile("s_waitcnt vmcnt(" #n ") lgkmcnt(0)\n\ts_barrier" ::: "memory"); SB(); } while (0)

DI u32 pk2(float a, float b) { return __builtin_bit_cast(u32, __builtin_convertvector((f32x2{a, b}), bf2)); }
DI float lo16(u32 x) { return __uint_as_float(x << 16); }
DI float hi16(u32 x) { return __uint_as_float(x & 0xffff0000u); }
DI f32x16 mfma(u32x4 a, u32x4 b, f32x16 c) {
  return __builtin_amdgcn_mfma_f32_32x32x16_bf16(__builtin_bit_cast(bf16x8, a), __builtin_bit_cast(bf16x8, b), c, 0, 0, 0);
}
DI u32x4 trfrag(u32 a0, u32 a1) {
  const s16x4 v = __builtin_amdgcn_ds_read_tr16_b64_v4i16((LAS s16x4*)a0);
  const s16x4 w = __builtin_amdgcn_ds_read_tr16_b64_v4i16((LAS s16x4*)a1);
  return __builtin_bit_cast(u32x4, __builtin_shufflevector(v, w, 0, 1, 2, 3, 4, 5, 6, 7));
}
DI u32x4 lds_rd16(u32 a) { return *(LAS u32x4*)a; }
DI void lds_wr16(u32 a, u32x4 v) { *(LAS u32x4*)a = v; }
DI float phi(float t) { return (t > 0.f ? t + 1.f : __expf(t)) + 1e-6f; }
DI float dpp_add(float v, const int ctrl_sel) {
  int t;
  if (ctrl_sel == 0) t = __builtin_amdgcn_update_dpp(0, __float_as_int(v), 0xB1, 0xf, 0xf, true);
  else if (ctrl_sel == 1) t = __builtin_amdgcn_update_dpp(0, __float_as_int(v), 0x4E, 0xf, 0xf, true);
  else t = __builtin_amdgcn_update_dpp(0, __float_as_int(v), 0x141, 0xf, 0xf, true);
  return v + __int_as_float(t);
}
DI f32x16 zero16() {
  f32x16 z;
#pragma unroll
  for (int i = 0; i < 16; ++i) z[i] = 0.f;
  return z;
}
DI void poll(u32x2 v, u64 mask, u32 tlo, u32 thi, u32 voff, u32x4 srd, u32 soff) {
  for (u32 spins = 0; spins < SPIN_CAP; ++spins) {
    const u64 ok = __builtin_amdgcn_ballot_w64(v.x == tlo && v.y == thi);
    if ((ok & mask) == mask) break;
    __builtin_amdgcn_s_sleep(2);
    v = ld8_sc1(voff, srd, soff);
    WAITV1(0, v);
  }
}
}

#define STAMP(i)

template <bool TA>
DI void kloop(f32x16& acc, const u32x4 (&xf)[32], u32 aE, u32 aO, const int f0, const int f1, const bool xfirst) {
  u32x4 wf[2][4];
#pragma unroll
  for (int j = 0; j < 4; ++j) wf[0][j] = trfrag(aE + 1024 * (f0 + j), aO + 1024 * (f0 + j));
#pragma unroll
  for (int f = f0; f < f1; f += 4) {
    const int cur = ((f - f0) >> 2) & 1;
    SB();
#pragma unroll
    for (int j = 0; j < 4; ++j) {
      if (f + 4 < f1) wf[cur ^ 1][j] = trfrag(aE + 1024 * (f + 4 + j), aO + 1024 * (f + 4 + j));
      acc = xfirst ? mfma(xf[f + j], wf[cur][j], acc) : mfma(wf[cur][j], xf[f + j], acc);
      asm volatile("" : "+v"(acc));
      SB();
    }
  }
}

__global__ __launch_bounds__(512, 2) void fused_block(const void* __restrict__ px, const void* __restrict__ pwqkv,
                                                      const void* __restrict__ pwo, const void* __restrict__ pw1,
                                                      const void* __restrict__ pw2, void* __restrict__ pout,
                                                      void* __restrict__ pws) {
  __shared__ __attribute__((aligned(1024))) char smem[5 * SLOT];
  const u32 L0 = (u32)(size_t)(LAS char*)smem;
  STAMP(0);
  asm volatile("" ::"s"(px), "s"(pwqkv), "s"(pwo), "s"(pw1), "s"(pw2), "s"(pout), "s"(pws));
  const u32 tid = threadIdx.x, lane = tid & 63;
  const u32 w = __builtin_amdgcn_readfirstlane(tid >> 6);
  const u32 s = w & 3, g = w >> 2;
  const u32 b = blockIdx.x, xcd = b & 7, r = b >> 3;
  const u32 p = (xcd >> 1) * 4 + (r >> 3);
  const u32 cl = (r & 3) + 4 * (xcd & 1);
  const bool typeA = ((r >> 2) & 1) == 0;
  const u32 rot = ((r & 7) + 2 * (r >> 3)) & 7;
  const u32 rt = xcd * 4 + (r >> 3), ct = r & 7;

  const u32x4 sx = mk_srd(px, 2048 * 1024), sws = mk_srd(pws, WS_END);
  const u32x4 sw = typeA ? mk_srd(pwqkv, 512 * 3072) : mk_srd(pw1, 512 * 2752);
  const u32 ldw = typeA ? 3072 : 2752;
  const u32 lr = lane >> 3, lc = lane & 7, h = lane >> 5, q = (lane & 15) >> 2;

  u32x2 ep = ld8_sc1(0, sws, WS_E);
  u32x4 xr[16];
  {
    const u32 vo = (16 * w + lr) * 1024 + lc * 16;
#pragma unroll
    for (int i = 0; i < 8; ++i)
#pragma unroll
      for (int g2 = 0; g2 < 2; ++g2) xr[2 * i + g2] = ld16(vo, sx, p * 131072 + g2 * 8192 + ((i + rot) & 7) * 128);
  }
  const u32 s0 = (cl * 43 + 5) >> 3;
  const u32 nhu = (((cl + 1) * 43 + 5) >> 3) - s0;
  const u32 pa = s0 & 1;
  const u32 vw = 2 * lr * ldw + ((lc ^ (((lane >> 4) & 1) << 2)) << 4);
  auto wblock = [&](u32 colb, u32 par, u32 slot, int t) {
    const u32 n = w + 8 * t;
    const u32 k = (((n >> 2) + rot) & 7) * 64 + (n & 3) * 16 + par;
    dma16(L0 + slot * SLOT + n * 1024, vw, sw, k * ldw + colb);
  };
  const u32 cK = 1024 + 128 * cl, cV = 2048 + 128 * cl, cQ = 128 * cl;
  const u32 cA = 64 * s0, cM = 64 * s0 - 64;
  const u32 bc0 = typeA ? cK : cA, bp0 = typeA ? 0 : pa;
  const u32 bc1 = typeA ? cK : cM, bp1 = typeA ? 1 : pa ^ 1;
  const u32 bc2 = typeA ? cV : cM + 128, bp2 = typeA ? 0 : pa ^ 1;
  const u32 bc3 = typeA ? cV : cA + 128, bp3 = typeA ? 1 : pa;
  const u32 bc4 = typeA ? cQ : cM + 256, bp4 = typeA ? 0 : pa ^ 1;
  const u32 bc5 = typeA ? cQ : cA + 256, bp5 = typeA ? 1 : pa;
  const u32 bc6 = cM + 384, bp6 = pa ^ 1;
#pragma unroll
  for (int t = 0; t < 4; ++t) {
    wblock(bc0, bp0, 0, t);
    wblock(bc1, bp1, 1, t);
    wblock(bc2, bp2, 2, t);
  }
  STAMP(1);
  const u32 trb = (4 * h + q) * 128 + ((32 * ((lane >> 4) & 1) + 8 * (lane & 3)) ^ (64 * (q >> 1)));
  u32 aE[3], aO[3];
  {
    const u32 sA[3] = {0, 3, 0}, sM0[3] = {1, 2, 4}, sM1[3] = {2, 4, 1}, sE[3] = {0, 2, 4}, sOd[3] = {1, 3, 0};
#pragma unroll
    for (int j = 0; j < 3; ++j) {
      const u32 al = sA[j] * SLOT + 64 * g, mi = (g ? sM1[j] : sM0[j]) * SLOT + 64 * (g ^ 1);
      const u32 e = typeA ? sE[j] * SLOT + 64 * g : (pa ? mi : al);
      const u32 o = typeA ? sOd[j] * SLOT + 64 * g : (pa ? al : mi);
      aE[j] = L0 + (trb ^ e);
      aO[j] = L0 + (trb ^ o);
    }
  }

  float sum[2] = {0.f, 0.f}, sq[2] = {0.f, 0.f};
  const bf2 ones2 = __builtin_bit_cast(bf2, 0x3f803f80u);
  auto stat = [&](int i) {
#pragma unroll
    for (int d = 0; d < 4; ++d) {
      const u32 t_ = xr[i][d];
      const bf2 v = __builtin_bit_cast(bf2, t_);
      sum[i & 1] = __builtin_amdgcn_fdot2_f32_bf16(v, ones2, sum[i & 1], false);
      sq[i & 1] = __builtin_amdgcn_fdot2_f32_bf16(v, v, sq[i & 1], false);
    }
  };
  WAITV1(28, ep);
  const u64 tag64 = (((u64)(u32)__builtin_amdgcn_readfirstlane(ep.y) << 32) | (u32)__builtin_amdgcn_readfirstlane(ep.x)) + 1;
  const u32 tlo = (u32)tag64, thi = (u32)(tag64 >> 32);
  WAITV4(24, xr[0], xr[1], xr[2], xr[3]);
  stat(0); stat(1); stat(2); stat(3);
  WAITV4(20, xr[4], xr[5], xr[6], xr[7]);
  stat(4); stat(5); stat(6); stat(7);
  WAITV4(16, xr[8], xr[9], xr[10], xr[11]);
  stat(8); stat(9); stat(10); stat(11);
  WAITV4(12, xr[12], xr[13], xr[14], xr[15]);
  STAMP(2);
  stat(12); stat(13); stat(14); stat(15);
  float rs[2], nb[2];
#pragma unroll
  for (int g2 = 0; g2 < 2; ++g2) {
    float a = sum[g2], c2 = sq[g2];
    a = dpp_add(a, 0); c2 = dpp_add(c2, 0);
    a = dpp_add(a, 1); c2 = dpp_add(c2, 1);
    a = dpp_add(a, 2); c2 = dpp_add(c2, 2);
    const float m = a * (1.f / 512.f);
    const float var = fmaxf(c2 * (1.f / 512.f) - m * m, 0.f);
    rs[g2] = __builtin_amdgcn_rsqf(var + 1e-5f);
    nb[g2] = -m * rs[g2];
  }
  const u32 stw = (16 * w + lr) * 256 + ((lc ^ lr) << 4);
  auto stage = [&](int rd) {
    const u32 base = L0 + (3 + (rd & 1)) * SLOT;
#pragma unroll
    for (int ii = 0; ii < 2; ++ii)
#pragma unroll
      for (int g2 = 0; g2 < 2; ++g2) {
        const u32x4 v = xr[2 * (2 * rd + ii) + g2];
        u32x4 o;
        o.x = pk2(fmaf(lo16(v.x), rs[g2], nb[g2]), fmaf(lo16(v.y), rs[g2], nb[g2]));
        o.y = pk2(fmaf(lo16(v.z), rs[g2], nb[g2]), fmaf(lo16(v.w), rs[g2], nb[g2]));
        o.z = pk2(fmaf(hi16(v.x), rs[g2], nb[g2]), fmaf(hi16(v.y), rs[g2], nb[g2]));
        o.w = pk2(fmaf(hi16(v.z), rs[g2], nb[g2]), fmaf(hi16(v.w), rs[g2], nb[g2]));
        lds_wr16(base + stw + g2 * 2048 + ((ii ^ g2) << 7), o);
      }
  };
  u32x4 xf[32];
  const u32 str = (32 * s + (lane & 31)) * 256 + ((h ^ (lane & 15)) << 4);
  auto unstage = [&](int rd) {
    const u32 base = L0 + (3 + (rd & 1)) * SLOT;
#pragma unroll
    for (int f = 0; f < 8; ++f) xf[8 * rd + f] = lds_rd16(base + (str ^ (32 * f)));
  };

  const u32 T = 4 * p + s;
  const u32 vlin = lane * 16;
  const u32 a2w = WS_A2 + T * A2_TILE;
  const u32x4 sw2 = mk_srd(pw2, 1376 * 1024), swo = mk_srd(pwo, 512 * 1024);
  const u32 vb2 = lr * 1024 + ((lc ^ (((lane >> 4) & 1) << 2)) << 4);
  const u32 rot2 = (ct * 11 + (r >> 3) * 22) % 88, rot3 = (ct * 4 + (r >> 3) * 8) & 31;
  auto kap_h = [&](u32 pos) { const u32 k = pos + rot2; return k >= 88 ? k - 88 : k; };
  auto b2block = [&](const int blk) {
#pragma unroll
    for (int t = 0; t < 4; ++t) {
      const u32 n = w + 8 * t;
      const int pos0 = 16 * blk + 4 * t;
      if (pos0 >= 120) continue;
      const u32 pos = pos0 + (w >> 1);
      const u32 dst = L0 + ((blk + 2) % 5) * SLOT + n * 1024;
      if (pos0 < 88) dma16(dst, vb2, sw2, kap_h(pos) * 16384 + (w & 1) * 8192 + ct * 128);
      else dma16(dst, vb2, swo, ((pos - 88 + rot3) & 31) * 16384 + (w & 1) * 8192 + ct * 128);
    }
  };

  auto step1 = [&](f32x16& acc, const bool xfirst) {
    stage(0);
    BARV(9);
    STAMP(3);
    if (w == 0 && lane == 0) st8_sc1(u32x2{tlo, thi}, 0, sws, WS_ARR + b * 8);
    unstage(0);
    stage(1);
    kloop<true>(acc, xf, aE[0], aO[0], 0, 8, xfirst);
    BARV(6);
    unstage(1);
    stage(2);
    kloop<true>(acc, xf, aE[0], aO[0], 8, 16, xfirst);
    BARV(3);
    unstage(2);
    stage(3);
    kloop<true>(acc, xf, aE[0], aO[0], 16, 24, xfirst);
    BARV(0);
#pragma unroll
    for (int t = 0; t < 4; ++t) wblock(bc3, bp3, 3, t);
    unstage(3);
    BAR();
#pragma unroll
    for (int t = 0; t < 4; ++t) wblock(bc4, bp4, 4, t);
    kloop<true>(acc, xf, aE[0], aO[0], 24, 32, xfirst);
  };

  if (typeA) {
    f32x16 acc = zero16();
    step1(acc, true);
    STAMP(4);
    BARV(4);
#pragma unroll
    for (int t = 0; t < 4; ++t) wblock(bc5, bp5, 0, t);
    u32x4 pk[2];
#pragma unroll
    for (int i = 0; i < 8; ++i) pk[i >> 2][i & 3] = pk2(phi(acc[2 * i]), phi(acc[2 * i + 1]));
    acc = zero16();
    kloop<true>(acc, xf, aE[1], aO[1], 0, 32, true);
    STAMP(5);
    BARV(0);
    b2block(0);
    b2block(1);
    {
      u32x4 vv[2];
#pragma unroll
      for (int i = 0; i < 8; ++i) vv[i >> 2][i & 3] = pk2(acc[2 * i], acc[2 * i + 1]);
      f32x16 kv = mfma(pk[0], vv[0], zero16());
      kv = mfma(pk[1], vv[1], kv);
      const u32x4 ones = {0x3f803f80u, 0x3f803f80u, 0x3f803f80u, 0x3f803f80u};
      f32x16 zz = mfma(ones, pk[0], zero16());
      zz = mfma(ones, pk[1], zz);
      const u32 hm = 0u - ((lane >> 4) & 1);
      u32x4 u0, u1;
#pragma unroll
      for (int j = 0; j < 4; ++j) {
        u0[j] = (__float_as_uint(kv[8 + j]) & hm) | (__float_as_uint(kv[j]) & ~hm);
        u1[j] = (__float_as_uint(kv[12 + j]) & hm) | (__float_as_uint(kv[4 + j]) & ~hm);
      }
      lds_wr16(L0 + SLOT + w * 2048 + vlin, u0);
      lds_wr16(L0 + SLOT + w * 2048 + 1024 + vlin, u1);
      *(LAS float*)(L0 + SLOT + 16384 + w * 256 + lane * 4) = zz[0];
    }
    BAR();
    if (s == 0) {
      f32x4 a0 = {0, 0, 0, 0}, a1 = {0, 0, 0, 0};
      float z = 0.f;
#pragma unroll
      for (int j = 0; j < 4; ++j) {
        a0 += __builtin_bit_cast(f32x4, lds_rd16(L0 + SLOT + (w + j) * 2048 + vlin));
        a1 += __builtin_bit_cast(f32x4, lds_rd16(L0 + SLOT + (w + j) * 2048 + 1024 + vlin));
        z += *(LAS float*)(L0 + SLOT + 16384 + (w + j) * 256 + lane * 4);
      }
      const u32 u = (cl * 2 + g) * 16 + p;
      st16_sc1(__builtin_bit_cast(u32x4, a0), vlin, sws, WS_KV + u * 2048);
      st16_sc1(__builtin_bit_cast(u32x4, a1), vlin, sws, WS_KV + u * 2048 + 1024);
      if (lane < 32) st4_sc1(__float_as_uint(z), lane * 4, sws, WS_Z + u * 128);
    }
    acc = zero16();
    kloop<true>(acc, xf, aE[2], aO[2], 0, 16, false);
    if (s == 0) {
      WAITV(0);
      if (lane == 0) st8_sc1(u32x2{tlo, thi}, 0, sws, WS_KVF + ((cl * 2 + g) * 16 + p) * 8);
    }
    kloop<true>(acc, xf, aE[2], aO[2], 16, 32, false);
    STAMP(6);
    const u32 ug = (cl * 2 + g) * 16 + 4 * s;
    u32x2 fl = ld8_sc1((lane & 3) * 8, sws, WS_KVF + ug * 8);
    u32x4 pq[2];
#pragma unroll
    for (int i = 0; i < 8; ++i) pq[i >> 2][i & 3] = pk2(phi(acc[2 * i]), phi(acc[2 * i + 1]));
    BAR();
    WAITV1(0, fl);
    poll(fl, 0xfull, tlo, thi, (lane & 3) * 8, sws, WS_KVF + ug * 8);
    STAMP(7);
    {
      u32x4 t[8];
      u32 zt[2];
#pragma unroll
      for (int j = 0; j < 8; ++j) t[j] = ld16_sc1(vlin, sws, WS_KV + ug * 2048 + j * 1024);
      zt[0] = ld4_sc1(lane * 4, sws, WS_Z + ug * 128);
      zt[1] = ld4_sc1(lane * 4, sws, WS_Z + ug * 128 + 256);
      b2block(2);
      b2block(3);
      WAITV4(8, t[0], t[1], t[2], t[3]);
      WAITV4(8, t[4], t[5], t[6], t[7]);
      asm volatile("" : "+v"(zt[0]), "+v"(zt[1]));
      f32x4 a0 = (__builtin_bit_cast(f32x4, t[0]) + __builtin_bit_cast(f32x4, t[2])) + (__builtin_bit_cast(f32x4, t[4]) + __builtin_bit_cast(f32x4, t[6]));
      f32x4 a1 = (__builtin_bit_cast(f32x4, t[1]) + __builtin_bit_cast(f32x4, t[3])) + (__builtin_bit_cast(f32x4, t[5]) + __builtin_bit_cast(f32x4, t[7]));
      lds_wr16(L0 + SLOT + w * 2048 + vlin, __builtin_bit_cast(u32x4, a0));
      lds_wr16(L0 + SLOT + w * 2048 + 1024 + vlin, __builtin_bit_cast(u32x4, a1));
      *(LAS float*)(L0 + SLOT + 16384 + w * 256 + lane * 4) = __uint_as_float(zt[0]) + __uint_as_float(zt[1]);
    }
    BAR();
    {
      f32x4 a0 = {0, 0, 0, 0}, a1 = {0, 0, 0, 0}, z0 = {0, 0, 0, 0}, z1 = {0, 0, 0, 0};
      const u32 hd = (lane >> 4) & 1;
      const u32 za = L0 + SLOT + 16384 + g * 1024 + hd * 64 + h * 16;
#pragma unroll
      for (int j = 0; j < 4; ++j) {
        a0 += __builtin_bit_cast(f32x4, lds_rd16(L0 + SLOT + (4 * g + j) * 2048 + vlin));
        a1 += __builtin_bit_cast(f32x4, lds_rd16(L0 + SLOT + (4 * g + j) * 2048 + 1024 + vlin));
      }
#pragma unroll
      for (int j = 0; j < 8; ++j) {
        z0 += __builtin_bit_cast(f32x4, lds_rd16(za + j * 128));
        z1 += __builtin_bit_cast(f32x4, lds_rd16(za + j * 128 + 32));
      }
      const u32x4 kf = {pk2(a0[0], a0[1]), pk2(a0[2], a0[3]), pk2(a1[0], a1[1]), pk2(a1[2], a1[3])};
      const u32x4 zf = {pk2(z0[0], z0[1]), pk2(z0[2], z0[3]), pk2(z1[0], z1[1]), pk2(z1[2], z1[3])};
      const u32x4 zr = {0, 0, 0, 0};
      f32x16 num = mfma(hd ? zr : kf, pq[0], zero16());
      num = mfma(hd ? kf : zr, pq[1], num);
      f32x16 den = mfma(hd ? zr : zf, pq[0], zero16());
      den = mfma(hd ? zf : zr, pq[1], den);
      u32x4 o[2];
#pragma unroll
      for (int i = 0; i < 8; ++i)
        o[i >> 2][i & 3] = pk2(num[2 * i] * __builtin_amdgcn_rcpf(den[2 * i] + 1e-6f), num[2 * i + 1] * __builtin_amdgcn_rcpf(den[2 * i + 1] + 1e-6f));
      st16_sc1(o[0], vlin, sws, a2w + (86 + 4 * cl + 2 * g) * 1024);
      st16_sc1(o[1], vlin, sws, a2w + (87 + 4 * cl + 2 * g) * 1024);
    }
    BAR();
    b2block(4);
    WAITV(4);
    if (lane == 0) st8_sc1(u32x2{tlo, thi}, 0, sws, WS_F + T * 256 + 128 + (cl * 2 + g) * 8);
  } else {
    auto hstore = [&](const f32x16& acc, u32 u) {
      u32x4 o[2];
#pragma unroll
      for (int i = 0; i < 8; ++i) o[i >> 2][i & 3] = pk2(fmaxf(acc[2 * i], 0.f), fmaxf(acc[2 * i + 1], 0.f));
      st16_sc1(o[0], vlin, sws, a2w + u * 2048);
      st16_sc1(o[1], vlin, sws, a2w + u * 2048 + 1024);
    };
    f32x16 acc = zero16();
    step1(acc, false);
    STAMP(4);
    BARV(0);
#pragma unroll
    for (int t = 0; t < 4; ++t) wblock(bc5, bp5, 0, t);
    if (nhu == 6) {
#pragma unroll
      for (int t = 0; t < 4; ++t) wblock(bc6, bp6, 1, t);
    }
    hstore(acc, s0 + g);
    acc = zero16();
    kloop<false>(acc, xf, aE[1], aO[1], 0, 32, false);
    STAMP(5);
    BARV(0);
    b2block(0);
    b2block(1);
    hstore(acc, s0 + 2 + g);
    if (4 + g < nhu) {
      acc = zero16();
      kloop<false>(acc, xf, aE[2], aO[2], 0, 32, false);
      hstore(acc, s0 + 4 + g);
    }
    STAMP(6);
    BAR();
    b2block(2);
    b2block(3);
    b2block(4);
    WAITV(12);
    STAMP(7);
    if (lane == 0) st8_sc1(u32x2{tlo, thi}, 0, sws, WS_F + T * 256 + (cl * 2 + g) * 8);
  }

  const u32 kq = s, mh = g;
  const u32 T2 = 2 * rt + mh;
  const u32 fo = WS_F + T2 * 256, vf = (lane & 31) * 8;
  STAMP(8);
  {
    u32x2 fl = ld8_sc1(vf, sws, fo);
    WAITV1(0, fl);
    poll(fl, 0xffffull, tlo, thi, vf, sws, fo);
  }
  STAMP(9);
  u32x4 a2[30];
  const u32 a2r = WS_A2 + T2 * A2_TILE;
#pragma unroll
  for (int m = 0; m < 22; ++m) {
    const u32 k = kap_h(4 * m + kq);
    a2[m] = ld16_sc1(vlin, sws, k < 86 ? a2r + k * 1024 : WS_END);
  }
  u32x2 flo = ld8_sc1(vf, sws, fo);
  u32x4 xres = ld16((64 * rt + 8 * w + lr) * 1024 + lc * 16, sx, ct * 128);
  BAR();
  f32x16 acc2[2] = {zero16(), zero16()};
  const u32 tb0 = L0 + kq * 2048 + trb, tb1 = L0 + kq * 2048 + (trb ^ 64);
  auto pstep = [&](const int m) {
    const u32 o = ((m / 4 + 2) % 5) * SLOT + (m & 3) * 8192;
    acc2[0] = mfma(trfrag(tb0 + o, tb0 + o + 1024), a2[m], acc2[0]);
    acc2[1] = mfma(trfrag(tb1 + o, tb1 + o + 1024), a2[m], acc2[1]);
    asm volatile("" : "+v"(acc2[0]), "+v"(acc2[1]));
  };
  WAITV4(20, a2[0], a2[1], a2[2], a2[3]);
  STAMP(10);
  pstep(0); pstep(1); pstep(2); pstep(3);
  BAR();
  b2block(5);
  WAITV4(20, a2[4], a2[5], a2[6], a2[7]);
  pstep(4); pstep(5); pstep(6); pstep(7);
  BAR();
  b2block(6);
  WAITV4(20, a2[8], a2[9], a2[10], a2[11]);
  pstep(8); pstep(9); pstep(10); pstep(11);
  BAR();
  b2block(7);
  WAITV4(18, a2[12], a2[13], a2[14], a2[15]);
  pstep(12); pstep(13); pstep(14); pstep(15);
  WAITV4(14, a2[16], a2[17], a2[18], a2[19]);
  pstep(16); pstep(17); pstep(18); pstep(19);
  WAITV4(10, a2[20], a2[21], flo, xres);
  STAMP(11);
  poll(flo, 0xffff0000ull, tlo, thi, vf, sws, fo);
  STAMP(12);
#pragma unroll
  for (int m = 22; m < 30; ++m) a2[m] = ld16_sc1(vlin, sws, a2r + (86 + ((4 * (m - 22) + kq + rot3) & 31)) * 1024);
  BARV(8);
  pstep(20); pstep(21);
  WAITV4(4, a2[22], a2[23], a2[24], a2[25]);
  pstep(22); pstep(23); pstep(24); pstep(25);
  WAITV4(0, a2[26], a2[27], a2[28], a2[29]);
  STAMP(13);
  pstep(26); pstep(27); pstep(28); pstep(29);
  {
    const u32 wb = L0 + (kq * 2 + mh) * 8192 + (lane & 31) * 256;
#pragma unroll
    for (int nt = 0; nt < 2; ++nt)
#pragma unroll
      for (int rq = 0; rq < 4; ++rq) {
        const u32x4 v = {__float_as_uint(acc2[nt][4 * rq]), __float_as_uint(acc2[nt][4 * rq + 1]), __float_as_uint(acc2[nt][4 * rq + 2]), __float_as_uint(acc2[nt][4 * rq + 3])};
        lds_wr16(wb + (((8 * nt + 2 * rq + h) ^ (lane & 7)) << 4), v);
      }
  }
  BAR();
  {
    f32x4 r0 = {lo16(xres.x), hi16(xres.x), lo16(xres.y), hi16(xres.y)}, r1 = {lo16(xres.z), hi16(xres.z), lo16(xres.w), hi16(xres.w)};
    const u32 rb = L0 + (8 * w + lr) * 256;
#pragma unroll
    for (int j = 0; j < 4; ++j) {
      r0 += __builtin_bit_cast(f32x4, lds_rd16(rb + j * 16384 + (((2 * lc) ^ lr) << 4)));
      r1 += __builtin_bit_cast(f32x4, lds_rd16(rb + j * 16384 + (((2 * lc + 1) ^ lr) << 4)));
    }
    const u32x4 o = {pk2(r0[0], r0[1]), pk2(r0[2], r0[3]), pk2(r1[0], r1[1]), pk2(r1[2], r1[3])};
    const u32x4 so = mk_srd(pout, 2048 * 1024);
    st16(o, (64 * rt + 8 * w + lr) * 1024 + lc * 16, so, ct * 128);
  }
  STAMP(14);
  if (b == 0 && w == 0) {
#pragma unroll 1
    for (u32 j = 0; j < 4; ++j) {
      u32x2 fl = ld8_sc1(lane * 8, sws, WS_ARR + j * 512);
      WAITV1(0, fl);
      poll(fl, ~0ull, tlo, thi, lane * 8, sws, WS_ARR + j * 512);
    }
    if (lane == 0) st8_sc1(u32x2{tlo, thi}, 0, sws, WS_E);
  }
}

extern "C" void kernel_launch(void* const* d_in, const int* in_sizes, int n_in, void* d_out, int out_size, void* d_ws,
                              size_t ws_size, hipStream_t stream) {
  fused_block<<<256, 512, 0, stream>>>(d_in[0], d_in[1], d_in[2], d_in[4], d_in[5], d_out, d_ws);
}
```

```cpp
#define DIAG 0
#include <hip/hip_runtime.h>

typedef unsigned u32;
typedef unsigned long long u64;
typedef u32 u32x4 __attribute__((ext_vector_type(4)));
typedef u32 u32x2 __attribute__((ext_vector_type(2)));
typedef short s16x4 __attribute__((ext_vector_type(4)));
typedef short bf16x8 __attribute__((ext_vector_type(8)));
typedef float f32x16 __attribute__((ext_vector_type(16)));
typedef float f32x4 __attribute__((ext_vector_type(4)));
typedef float f32x2 __attribute__((ext_vector_type(2)));
typedef __bf16 bf2 __attribute__((ext_vector_type(2)));
#define LAS __attribute__((address_space(3)))
#define DI __device__ __forceinline__

namespace {
constexpr int SLOT = 32768;
constexpr u32 WS_E = 0, WS_ARR = 4096, WS_F = 8192, WS_KVF = 24576, WS_Z = 32768, WS_KV = 65536,
              WS_A2 = 1u << 20, A2_TILE = 118 * 1024, WS_END = WS_A2 + 64 * A2_TILE;
constexpr u32 SPIN_CAP = 1u << 17;

DI u32x4 mk_srd(const void* p, u32 bytes) {
  const u64 a = (u64)p;
  return u32x4{(u32)a, (u32)(a >> 32) & 0xffffu, bytes, 0x00020000u};
}
DI void dma16(u32 lds, u32 voff, u32x4 srd, u32 soff) {
  asm volatile("s_mov_b32 m0, %0\n\ts_nop 0\n\tbuffer_load_dwordx4 %1, %2, %3 offen lds" ::"s"(lds), "v"(voff), "s"(srd), "s"(soff) : "memory");
}
DI u32x4 ld16(u32 voff, u32x4 srd, u32 soff) {
  u32x4 r;
  asm volatile("buffer_load_dwordx4 %0, %1, %2, %3 offen" : "=v"(r) : "v"(voff), "s"(srd), "s"(soff) : "memory");
  return r;
}
DI u32x4 ld16_sc1(u32 voff, u32x4 srd, u32 soff) {
  u32x4 r;
  asm volatile("buffer_load_dwordx4 %0, %1, %2, %3 offen sc1" : "=v"(r) : "v"(voff), "s"(srd), "s"(soff) : "memory");
  return r;
}
DI u32x2 ld8_sc1(u32 voff, u32x4 srd, u32 soff) {
  u32x2 r;
  asm volatile("buffer_load_dwordx2 %0, %1, %2, %3 offen sc1" : "=v"(r) : "v"(voff), "s"(srd), "s"(soff) : "memory");
  return r;
}
DI u32 ld4_sc1(u32 voff, u32x4 srd, u32 soff) {
  u32 r;
  asm volatile("buffer_load_dword %0, %1, %2, %3 offen sc1" : "=v"(r) : "v"(voff), "s"(srd), "s"(soff) : "memory");
  return r;
}
DI void st16_sc1(u32x4 v, u32 voff, u32x4 srd, u32 soff) {
  asm volatile("buffer_store_dwordx4 %0, %1, %2, %3 offen sc1\n\ts_nop 1" ::"v"(v), "v"(voff), "s"(srd), "s"(soff) : "memory");
}
DI void st16(u32x4 v, u32 voff, u32x4 srd, u32 soff) {
  asm volatile("buffer_store_dwordx4 %0, %1, %2, %3 offen\n\ts_nop 1" ::"v"(v), "v"(voff), "s"(srd), "s"(soff) : "memory");
}
DI void st8_sc1(u32x2 v, u32 voff, u32x4 srd, u32 soff) {
  asm volatile("buffer_store_dwordx2 %0, %1, %2, %3 offen sc1" ::"v"(v), "v"(voff), "s"(srd), "s"(soff) : "memory");
}
DI void st4_sc1(u32 v, u32 voff, u32x4 srd, u32 soff) {
  asm volatile("buffer_store_dword %0, %1, %2, %3 offen sc1" ::"v"(v), "v"(voff), "s"(srd), "s"(soff) : "memory");
}
#define WAITV(n) asm volatile("s_waitcnt vmcnt(" #n ")" ::: "memory")
#define WAITV1(n, a) asm volatile("s_waitcnt vmcnt(" #n ")" : "+v"(a)::"memory")
#define WAITV4(n, a, b, c, d) asm volatile("s_waitcnt vmcnt(" #n ")" : "+v"(a), "+v"(b), "+v"(c), "+v"(d)::"memory")
#define SB() __builtin_amdgcn_sched_barrier(0)
#define BAR() do { SB(); asm volatile("s_waitcnt lgkmcnt(0)\n\ts_barrier" ::: "memory"); SB(); } while (0)
#define BARV(n) do { SB(); asm volatile("s_waitcnt vmcnt(" #n ") lgkmcnt(0)\n\ts_barrier" ::: "memory"); SB(); } while (0)

DI u32 pk2(float a, float b) { return __builtin_bit_cast(u32, __builtin_convertvector((f32x2{a, b}), bf2)); }
DI float lo16(u32 x) { return __uint_as_float(x << 16); }
DI float hi16(u32 x) { return __uint_as_float(x & 0xffff0000u); }
DI f32x16 mfma(u32x4 a, u32x4 b, f32x16 c) {
  return __builtin_amdgcn_mfma_f32_32x32x16_bf16(__builtin_bit_cast(bf16x8, a), __builtin_bit_cast(bf16x8, b), c, 0, 0, 0);
}
DI u32x4 trfrag(u32 a0, u32 a1) {
  const s16x4 v = __builtin_amdgcn_ds_read_tr16_b64_v4i16((LAS s16x4*)a0);
  const s16x4 w = __builtin_amdgcn_ds_read_tr16_b64_v4i16((LAS s16x4*)a1);
  return __builtin_bit_cast(u32x4, __builtin_shufflevector(v, w, 0, 1, 2, 3, 4, 5, 6, 7));
}
DI u32x4 lds_rd16(u32 a) { return *(LAS u32x4*)a; }
DI void lds_wr16(u32 a, u32x4 v) { *(LAS u32x4*)a = v; }
DI float phi(float t) { return (t > 0.f ? t + 1.f : __expf(t)) + 1e-6f; }
DI float dpp_add(float v, const int ctrl_sel) {
  int t;
  if (ctrl_sel == 0) t = __builtin_amdgcn_update_dpp(0, __float_as_int(v), 0xB1, 0xf, 0xf, true);
  else if (ctrl_sel == 1) t = __builtin_amdgcn_update_dpp(0, __float_as_int(v), 0x4E, 0xf, 0xf, true);
  else t = __builtin_amdgcn_update_dpp(0, __float_as_int(v), 0x141, 0xf, 0xf, true);
  return v + __int_as_float(t);
}
DI f32x16 zero16() {
  f32x16 z;
#pragma unroll
  for (int i = 0; i < 16; ++i) z[i] = 0.f;
  return z;
}
DI void poll(u32x2 v, u64 mask, u32 tlo, u32 thi, u32 voff, u32x4 srd, u32 soff) {
  for (u32 spins = 0; spins < SPIN_CAP; ++spins) {
    const u64 ok = __builtin_amdgcn_ballot_w64(v.x == tlo && v.y == thi);
    if ((ok & mask) == mask) break;
    __builtin_amdgcn_s_sleep(2);
    v = ld8_sc1(voff, srd, soff);
    WAITV1(0, v);
  }
}
}

#define STAMP(i) do { __builtin_amdgcn_sched_barrier(0); asm volatile("s_memrealtime %0\n\ts_waitcnt lgkmcnt(0)" : "=s"(stamp[i])::"memory"); __builtin_amdgcn_sched_barrier(0); } while (0)

template <bool TA>
DI void kloop(f32x16& acc, const u32x4 (&xf)[32], u32 aE, u32 aO, const int f0, const int f1, const bool xfirst) {
  u32x4 wf[2][4];
#pragma unroll
  for (int j = 0; j < 4; ++j) wf[0][j] = trfrag(aE + 1024 * (f0 + j), aO + 1024 * (f0 + j));
#pragma unroll
  for (int f = f0; f < f1; f += 4) {
    const int cur = ((f - f0) >> 2) & 1;
    SB();
#pragma unroll
    for (int j = 0; j < 4; ++j) {
      if (f + 4 < f1) wf[cur ^ 1][j] = trfrag(aE + 1024 * (f + 4 + j), aO + 1024 * (f + 4 + j));
      acc = xfirst ? mfma(xf[f + j], wf[cur][j], acc) : mfma(wf[cur][j], xf[f + j], acc);
      asm volatile("" : "+v"(acc));
      SB();
    }
  }
}

__global__ __launch_bounds__(512, 2) void fused_block(const void* __restrict__ px, const void* __restrict__ pwqkv,
                                                      const void* __restrict__ pwo, const void* __restrict__ pw1,
                                                      const void* __restrict__ pw2, void* __restrict__ pout,
                                                      void* __restrict__ pws) {
  __shared__ __attribute__((aligned(1024))) char smem[5 * SLOT];
  const u32 L0 = (u32)(size_t)(LAS char*)smem;
  u64 stamp[16];
  u64 cy0 = __builtin_amdgcn_s_memtime();
  STAMP(0);
  asm volatile("" ::"s"(px), "s"(pwqkv), "s"(pwo), "s"(pw1), "s"(pw2), "s"(pout), "s"(pws));
  const u32 tid = threadIdx.x, lane = tid & 63;
  const u32 w = __builtin_amdgcn_readfirstlane(tid >> 6);
  const u32 s = w & 3, g = w >> 2;
  const u32 b = blockIdx.x, xcd = b & 7, r = b >> 3;
  const u32 p = (xcd >> 1) * 4 + (r >> 3);
  const u32 cl = (r & 3) + 4 * (xcd & 1);
  const bool typeA = ((r >> 2) & 1) == 0;
  const u32 rot = ((r & 7) + 2 * (r >> 3)) & 7;
  const u32 rt = xcd * 4 + (r >> 3), ct = r & 7;

  const u32x4 sx = mk_srd(px, 2048 * 1024), sws = mk_srd(pws, WS_END);
  const u32x4 sw = typeA ? mk_srd(pwqkv, 512 * 3072) : mk_srd(pw1, 512 * 2752);
  const u32 ldw = typeA ? 3072 : 2752;
  const u32 lr = lane >> 3, lc = lane & 7, h = lane >> 5, q = (lane & 15) >> 2;

  u32x2 ep = ld8_sc1(0, sws, WS_E);
  u32x4 xr[16];
  {
    const u32 vo = (16 * w + lr) * 1024 + lc * 16;
#pragma unroll
    for (int i = 0; i < 8; ++i)
#pragma unroll
      for (int g2 = 0; g2 < 2; ++g2) xr[2 * i + g2] = ld16(vo, sx, p * 131072 + g2 * 8192 + ((i + rot) & 7) * 128);
  }
  const u32 s0 = (cl * 43 + 5) >> 3;
  const u32 nhu = (((cl + 1) * 43 + 5) >> 3) - s0;
  const u32 pa = s0 & 1;
  const u32 vw = 2 * lr * ldw + ((lc ^ (((lane >> 4) & 1) << 2)) << 4);
  auto wblock = [&](u32 colb, u32 par, u32 slot, int t) {
    const u32 n = w + 8 * t;
    const u32 k = (((n >> 2) + rot) & 7) * 64 + (n & 3) * 16 + par;
    dma16(L0 + slot * SLOT + n * 1024, vw, sw, k * ldw + colb);
  };
  const u32 cK = 1024 + 128 * cl, cV = 2048 + 128 * cl, cQ = 128 * cl;
  const u32 cA = 64 * s0, cM = 64 * s0 - 64;
  const u32 bc0 = typeA ? cK : cA, bp0 = typeA ? 0 : pa;
  const u32 bc1 = typeA ? cK : cM, bp1 = typeA ? 1 : pa ^ 1;
  const u32 bc2 = typeA ? cV : cM + 128, bp2 = typeA ? 0 : pa ^ 1;
  const u32 bc3 = typeA ? cV : cA + 128, bp3 = typeA ? 1 : pa;
  const u32 bc4 = typeA ? cQ : cM + 256, bp4 = typeA ? 0 : pa ^ 1;
  const u32 bc5 = typeA ? cQ : cA + 256, bp5 = typeA ? 1 : pa;
  const u32 bc6 = cM + 384, bp6 = pa ^ 1;
#pragma unroll
  for (int t = 0; t < 4; ++t) {
    wblock(bc0, bp0, 0, t);
    wblock(bc1, bp1, 1, t);
    wblock(bc2, bp2, 2, t);
  }
  STAMP(1);
  const u32 trb = (4 * h + q) * 128 + ((32 * ((lane >> 4) & 1) + 8 * (lane & 3)) ^ (64 * (q >> 1)));
  u32 aE[3], aO[3];
  {
    const u32 sA[3] = {0, 3, 0}, sM0[3] = {1, 2, 4}, sM1[3] = {2, 4, 1}, sE[3] = {0, 2, 4}, sOd[3] = {1, 3, 0};
#pragma unroll
    for (int j = 0; j < 3; ++j) {
      const u32 al = sA[j] * SLOT + 64 * g, mi = (g ? sM1[j] : sM0[j]) * SLOT + 64 * (g ^ 1);
      const u32 e = typeA ? sE[j] * SLOT + 64 * g : (pa ? mi : al);
      const u32 o = typeA ? sOd[j] * SLOT + 64 * g : (pa ? al : mi);
      aE[j] = L0 + (trb ^ e);
      aO[j] = L0 + (trb ^ o);
    }
  }

  float sum[2] = {0.f, 0.f}, sq[2] = {0.f, 0.f};
  const bf2 ones2 = __builtin_bit_cast(bf2, 0x3f803f80u);
  auto stat = [&](int i) {
#pragma unroll
    for (int d = 0; d < 4; ++d) {
      const u32 t_ = xr[i][d];
      const bf2 v = __builtin_bit_cast(bf2, t_);
      sum[i & 1] = __builtin_amdgcn_fdot2_f32_bf16(v, ones2, sum[i & 1], false);
      sq[i & 1] = __builtin_amdgcn_fdot2_f32_bf16(v, v, sq[i & 1], false);
    }
  };
  WAITV1(28, ep);
  const u64 tag64 = (((u64)(u32)__builtin_amdgcn_readfirstlane(ep.y) << 32) | (u32)__builtin_amdgcn_readfirstlane(ep.x)) + 1;
  const u32 tlo = (u32)tag64, thi = (u32)(tag64 >> 32);
  WAITV4(24, xr[0], xr[1], xr[2], xr[3]);
  stat(0); stat(1); stat(2); stat(3);
  WAITV4(20, xr[4], xr[5], xr[6], xr[7]);
  stat(4); stat(5); stat(6); stat(7);
  WAITV4(16, xr[8], xr[9], xr[10], xr[11]);
  stat(8); stat(9); stat(10); stat(11);
  WAITV4(12, xr[12], xr[13], xr[14], xr[15]);
  STAMP(2);
  stat(12); stat(13); stat(14); stat(15);
  float rs[2], nb[2];
#pragma unroll
  for (int g2 = 0; g2 < 2; ++g2) {
    float a = sum[g2], c2 = sq[g2];
    a = dpp_add(a, 0); c2 = dpp_add(c2, 0);
    a = dpp_add(a, 1); c2 = dpp_add(c2, 1);
    a = dpp_add(a, 2); c2 = dpp_add(c2, 2);
    const float m = a * (1.f / 512.f);
    const float var = fmaxf(c2 * (1.f / 512.f) - m * m, 0.f);
    rs[g2] = __builtin_amdgcn_rsqf(var + 1e-5f);
    nb[g2] = -m * rs[g2];
  }
  const u32 stw = (16 * w + lr) * 256 + ((lc ^ lr) << 4);
  auto stage = [&](int rd) {
    const u32 base = L0 + (3 + (rd & 1)) * SLOT;
#pragma unroll
    for (int ii = 0; ii < 2; ++ii)
#pragma unroll
      for (int g2 = 0; g2 < 2; ++g2) {
        const u32x4 v = xr[2 * (2 * rd + ii) + g2];
        u32x4 o;
        o.x = pk2(fmaf(lo16(v.x), rs[g2], nb[g2]), fmaf(lo16(v.y), rs[g2], nb[g2]));
        o.y = pk2(fmaf(lo16(v.z), rs[g2], nb[g2]), fmaf(lo16(v.w), rs[g2], nb[g2]));
        o.z = pk2(fmaf(hi16(v.x), rs[g2], nb[g2]), fmaf(hi16(v.y), rs[g2], nb[g2]));
        o.w = pk2(fmaf(hi16(v.z), rs[g2], nb[g2]), fmaf(hi16(v.w), rs[g2], nb[g2]));
        lds_wr16(base + stw + g2 * 2048 + ((ii ^ g2) << 7), o);
      }
  };
  u32x4 xf[32];
  const u32 str = (32 * s + (lane & 31)) * 256 + ((h ^ (lane & 15)) << 4);
  auto unstage = [&](int rd) {
    const u32 base = L0 + (3 + (rd & 1)) * SLOT;
#pragma unroll
    for (int f = 0; f < 8; ++f) xf[8 * rd + f] = lds_rd16(base + (str ^ (32 * f)));
  };

  const u32 T = 4 * p + s;
  const u32 vlin = lane * 16;
  const u32 a2w = WS_A2 + T * A2_TILE;
  const u32x4 sw2 = mk_srd(pw2, 1376 * 1024), swo = mk_srd(pwo, 512 * 1024);
  const u32 vb2 = lr * 1024 + ((lc ^ (((lane >> 4) & 1) << 2)) << 4);
  const u32 rot2 = (ct * 11 + (r >> 3) * 22) % 88, rot3 = (ct * 4 + (r >> 3) * 8) & 31;
  auto kap_h = [&](u32 pos) { const u32 k = pos + rot2; return k >= 88 ? k - 88 : k; };
  auto b2block = [&](const int blk) {
#pragma unroll
    for (int t = 0; t < 4; ++t) {
      const u32 n = w + 8 * t;
      const int pos0 = 16 * blk + 4 * t;
      if (pos0 >= 120) continue;
      const u32 pos = pos0 + (w >> 1);
      const u32 dst = L0 + ((blk + 2) % 5) * SLOT + n * 1024;
      if (pos0 < 88) dma16(dst, vb2, sw2, kap_h(pos) * 16384 + (w & 1) * 8192 + ct * 128);
      else dma16(dst, vb2, swo, ((pos - 88 + rot3) & 31) * 16384 + (w & 1) * 8192 + ct * 128);
    }
  };

  auto step1 = [&](f32x16& acc, const bool xfirst) {
    stage(0);
    BARV(9);
    STAMP(3);
    if (w == 0 && lane == 0) { st4_sc1((u32)stamp[0], 0, sws, 2048 + b * 4); WAITV(0); }
    if (w == 0 && lane == 0) st8_sc1(u32x2{tlo, thi}, 0, sws, WS_ARR + b * 8);
    unstage(0);
    stage(1);
    kloop<true>(acc, xf, aE[0], aO[0], 0, 8, xfirst);
    BARV(6);
    unstage(1);
    stage(2);
    kloop<true>(acc, xf, aE[0], aO[0], 8, 16, xfirst);
    BARV(3);
    unstage(2);
    stage(3);
    kloop<true>(acc, xf, aE[0], aO[0], 16, 24, xfirst);
    BARV(0);
#pragma unroll
    for (int t = 0; t < 4; ++t) wblock(bc3, bp3, 3, t);
    unstage(3);
    BAR();
#pragma unroll
    for (int t = 0; t < 4; ++t) wblock(bc4, bp4, 4, t);
    kloop<true>(acc, xf, aE[0], aO[0], 24, 32, xfirst);
  };

  if (typeA) {
    f32x16 acc = zero16();
    step1(acc, true);
    STAMP(4);
    BARV(4);
#pragma unroll
    for (int t = 0; t < 4; ++t) wblock(bc5, bp5, 0, t);
    u32x4 pk[2];
#pragma unroll
    for (int i = 0; i < 8; ++i) pk[i >> 2][i & 3] = pk2(phi(acc[2 * i]), phi(acc[2 * i + 1]));
    acc = zero16();
    kloop<true>(acc, xf, aE[1], aO[1], 0, 32, true);
    STAMP(5);
    BARV(0);
    b2block(0);
    b2block(1);
    {
      u32x4 vv[2];
#pragma unroll
      for (int i = 0; i < 8; ++i) vv[i >> 2][i & 3] = pk2(acc[2 * i], acc[2 * i + 1]);
      f32x16 kv = mfma(pk[0], vv[0], zero16());
      kv = mfma(pk[1], vv[1], kv);
      const u32x4 ones = {0x3f803f80u, 0x3f803f80u, 0x3f803f80u, 0x3f803f80u};
      f32x16 zz = mfma(ones, pk[0], zero16());
      zz = mfma(ones, pk[1], zz);
      const u32 hm = 0u - ((lane >> 4) & 1);
      u32x4 u0, u1;
#pragma unroll
      for (int j = 0; j < 4; ++j) {
        u0[j] = (__float_as_uint(kv[8 + j]) & hm) | (__float_as_uint(kv[j]) & ~hm);
        u1[j] = (__float_as_uint(kv[12 + j]) & hm) | (__float_as_uint(kv[4 + j]) & ~hm);
      }
      lds_wr16(L0 + SLOT + w * 2048 + vlin, u0);
      lds_wr16(L0 + SLOT + w * 2048 + 1024 + vlin, u1);
      *(LAS float*)(L0 + SLOT + 16384 + w * 256 + lane * 4) = zz[0];
    }
    BAR();
    if (s == 0) {
      f32x4 a0 = {0, 0, 0, 0}, a1 = {0, 0, 0, 0};
      float z = 0.f;
#pragma unroll
      for (int j = 0; j < 4; ++j) {
        a0 += __builtin_bit_cast(f32x4, lds_rd16(L0 + SLOT + (w + j) * 2048 + vlin));
        a1 += __builtin_bit_cast(f32x4, lds_rd16(L0 + SLOT + (w + j) * 2048 + 1024 + vlin));
        z += *(LAS float*)(L0 + SLOT + 16384 + (w + j) * 256 + lane * 4);
      }
      const u32 u = (cl * 2 + g) * 16 + p;
      st16_sc1(__builtin_bit_cast(u32x4, a0), vlin, sws, WS_KV + u * 2048);
      st16_sc1(__builtin_bit_cast(u32x4, a1), vlin, sws, WS_KV + u * 2048 + 1024);
      if (lane < 32) st4_sc1(__float_as_uint(z), lane * 4, sws, WS_Z + u * 128);
    }
    acc = zero16();
    kloop<true>(acc, xf, aE[2], aO[2], 0, 16, false);
    if (s == 0) {
      WAITV(0);
      if (lane == 0) st8_sc1(u32x2{tlo, thi}, 0, sws, WS_KVF + ((cl * 2 + g) * 16 + p) * 8);
    }
    kloop<true>(acc, xf, aE[2], aO[2], 16, 32, false);
    STAMP(6);
    const u32 ug = (cl * 2 + g) * 16 + 4 * s;
    u32x2 fl = ld8_sc1((lane & 3) * 8, sws, WS_KVF + ug * 8);
    u32x4 pq[2];
#pragma unroll
    for (int i = 0; i < 8; ++i) pq[i >> 2][i & 3] = pk2(phi(acc[2 * i]), phi(acc[2 * i + 1]));
    BAR();
    WAITV1(0, fl);
    poll(fl, 0xfull, tlo, thi, (lane & 3) * 8, sws, WS_KVF + ug * 8);
    STAMP(7);
    {
      u32x4 t[8];
      u32 zt[2];
#pragma unroll
      for (int j = 0; j < 8; ++j) t[j] = ld16_sc1(vlin, sws, WS_KV + ug * 2048 + j * 1024);
      zt[0] = ld4_sc1(lane * 4, sws, WS_Z + ug * 128);
      zt[1] = ld4_sc1(lane * 4, sws, WS_Z + ug * 128 + 256);
      b2block(2);
      b2block(3);
      WAITV4(8, t[0], t[1], t[2], t[3]);
      WAITV4(8, t[4], t[5], t[6], t[7]);
      asm volatile("" : "+v"(zt[0]), "+v"(zt[1]));
      f32x4 a0 = (__builtin_bit_cast(f32x4, t[0]) + __builtin_bit_cast(f32x4, t[2])) + (__builtin_bit_cast(f32x4, t[4]) + __builtin_bit_cast(f32x4, t[6]));
      f32x4 a1 = (__builtin_bit_cast(f32x4, t[1]) + __builtin_bit_cast(f32x4, t[3])) + (__builtin_bit_cast(f32x4, t[5]) + __builtin_bit_cast(f32x4, t[7]));
      lds_wr16(L0 + SLOT + w * 2048 + vlin, __builtin_bit_cast(u32x4, a0));
      lds_wr16(L0 + SLOT + w * 2048 + 1024 + vlin, __builtin_bit_cast(u32x4, a1));
      *(LAS float*)(L0 + SLOT + 16384 + w * 256 + lane * 4) = __uint_as_float(zt[0]) + __uint_as_float(zt[1]);
    }
    BAR();
    {
      f32x4 a0 = {0, 0, 0, 0}, a1 = {0, 0, 0, 0}, z0 = {0, 0, 0, 0}, z1 = {0, 0, 0, 0};
      const u32 hd = (lane >> 4) & 1;
      const u32 za = L0 + SLOT + 16384 + g * 1024 + hd * 64 + h * 16;
#pragma unroll
      for (int j = 0; j < 4; ++j) {
        a0 += __builtin_bit_cast(f32x4, lds_rd16(L0 + SLOT + (4 * g + j) * 2048 + vlin));
        a1 += __builtin_bit_cast(f32x4, lds_rd16(L0 + SLOT + (4 * g + j) * 2048 + 1024 + vlin));
      }
#pragma unroll
      for (int j = 0; j < 8; ++j) {
        z0 += __builtin_bit_cast(f32x4, lds_rd16(za + j * 128));
        z1 += __builtin_bit_cast(f32x4, lds_rd16(za + j * 128 + 32));
      }
      const u32x4 kf = {pk2(a0[0], a0[1]), pk2(a0[2], a0[3]), pk2(a1[0], a1[1]), pk2(a1[2], a1[3])};
      const u32x4 zf = {pk2(z0[0], z0[1]), pk2(z0[2], z0[3]), pk2(z1[0], z1[1]), pk2(z1[2], z1[3])};
      const u32x4 zr = {0, 0, 0, 0};
      f32x16 num = mfma(hd ? zr : kf, pq[0], zero16());
      num = mfma(hd ? kf : zr, pq[1], num);
      f32x16 den = mfma(hd ? zr : zf, pq[0], zero16());
      den = mfma(hd ? zf : zr, pq[1], den);
      u32x4 o[2];
#pragma unroll
      for (int i = 0; i < 8; ++i)
        o[i >> 2][i & 3] = pk2(num[2 * i] * __builtin_amdgcn_rcpf(den[2 * i] + 1e-6f), num[2 * i + 1] * __builtin_amdgcn_rcpf(den[2 * i + 1] + 1e-6f));
      st16_sc1(o[0], vlin, sws, a2w + (86 + 4 * cl + 2 * g) * 1024);
      st16_sc1(o[1], vlin, sws, a2w + (87 + 4 * cl + 2 * g) * 1024);
    }
    BAR();
    b2block(4);
    WAITV(4);
    if (lane == 0) st8_sc1(u32x2{tlo, thi}, 0, sws, WS_F + T * 256 + 128 + (cl * 2 + g) * 8);
  } else {
    auto hstore = [&](const f32x16& acc, u32 u) {
      u32x4 o[2];
#pragma unroll
      for (int i = 0; i < 8; ++i) o[i >> 2][i & 3] = pk2(fmaxf(acc[2 * i], 0.f), fmaxf(acc[2 * i + 1], 0.f));
      st16_sc1(o[0], vlin, sws, a2w + u * 2048);
      st16_sc1(o[1], vlin, sws, a2w + u * 2048 + 1024);
    };
    f32x16 acc = zero16();
    step1(acc, false);
    STAMP(4);
    BARV(0);
#pragma unroll
    for (int t = 0; t < 4; ++t) wblock(bc5, bp5, 0, t);
    if (nhu == 6) {
#pragma unroll
      for (int t = 0; t < 4; ++t) wblock(bc6, bp6, 1, t);
    }
    hstore(acc, s0 + g);
    acc = zero16();
    kloop<false>(acc, xf, aE[1], aO[1], 0, 32, false);
    STAMP(5);
    BARV(0);
    b2block(0);
    b2block(1);
    hstore(acc, s0 + 2 + g);
    if (4 + g < nhu) {
      acc = zero16();
      kloop<false>(acc, xf, aE[2], aO[2], 0, 32, false);
      hstore(acc, s0 + 4 + g);
    }
    STAMP(6);
    BAR();
    b2block(2);
    b2block(3);
    b2block(4);
    WAITV(12);
    STAMP(7);
    if (lane == 0) st8_sc1(u32x2{tlo, thi}, 0, sws, WS_F + T * 256 + (cl * 2 + g) * 8);
  }

  const u32 kq = s, mh = g;
  const u32 T2 = 2 * rt + mh;
  const u32 fo = WS_F + T2 * 256, vf = (lane & 31) * 8;
  STAMP(8);
  {
    u32x2 fl = ld8_sc1(vf, sws, fo);
    WAITV1(0, fl);
    poll(fl, 0xffffull, tlo, thi, vf, sws, fo);
  }
  STAMP(9);
  u32x4 a2[30];
  const u32 a2r = WS_A2 + T2 * A2_TILE;
#pragma unroll
  for (int m = 0; m < 22; ++m) {
    const u32 k = kap_h(4 * m + kq);
    a2[m] = ld16_sc1(vlin, sws, k < 86 ? a2r + k * 1024 : WS_END);
  }
  u32x2 flo = ld8_sc1(vf, sws, fo);
  u32x4 xres = ld16((64 * rt + 8 * w + lr) * 1024 + lc * 16, sx, ct * 128);
  BAR();
  f32x16 acc2[2] = {zero16(), zero16()};
  const u32 tb0 = L0 + kq * 2048 + trb, tb1 = L0 + kq * 2048 + (trb ^ 64);
  auto pstep = [&](const int m) {
    const u32 o = ((m / 4 + 2) % 5) * SLOT + (m & 3) * 8192;
    acc2[0] = mfma(trfrag(tb0 + o, tb0 + o + 1024), a2[m], acc2[0]);
    acc2[1] = mfma(trfrag(tb1 + o, tb1 + o + 1024), a2[m], acc2[1]);
    asm volatile("" : "+v"(acc2[0]), "+v"(acc2[1]));
  };
  WAITV4(20, a2[0], a2[1], a2[2], a2[3]);
  STAMP(10);
  pstep(0); pstep(1); pstep(2); pstep(3);
  BAR();
  b2block(5);
  WAITV4(20, a2[4], a2[5], a2[6], a2[7]);
  pstep(4); pstep(5); pstep(6); pstep(7);
  BAR();
  b2block(6);
  WAITV4(20, a2[8], a2[9], a2[10], a2[11]);
  pstep(8); pstep(9); pstep(10); pstep(11);
  BAR();
  b2block(7);
  WAITV4(18, a2[12], a2[13], a2[14], a2[15]);
  pstep(12); pstep(13); pstep(14); pstep(15);
  WAITV4(14, a2[16], a2[17], a2[18], a2[19]);
  pstep(16); pstep(17); pstep(18); pstep(19);
  WAITV4(10, a2[20], a2[21], flo, xres);
  STAMP(11);
  poll(flo, 0xffff0000ull, tlo, thi, vf, sws, fo);
  STAMP(12);
#pragma unroll
  for (int m = 22; m < 30; ++m) a2[m] = ld16_sc1(vlin, sws, a2r + (86 + ((4 * (m - 22) + kq + rot3) & 31)) * 1024);
  BARV(8);
  pstep(20); pstep(21);
  WAITV4(4, a2[22], a2[23], a2[24], a2[25]);
  pstep(22); pstep(23); pstep(24); pstep(25);
  WAITV4(0, a2[26], a2[27], a2[28], a2[29]);
  STAMP(13);
  pstep(26); pstep(27); pstep(28); pstep(29);
  {
    const u32 wb = L0 + (kq * 2 + mh) * 8192 + (lane & 31) * 256;
#pragma unroll
    for (int nt = 0; nt < 2; ++nt)
#pragma unroll
      for (int rq = 0; rq < 4; ++rq) {
        const u32x4 v = {__float_as_uint(acc2[nt][4 * rq]), __float_as_uint(acc2[nt][4 * rq + 1]), __float_as_uint(acc2[nt][4 * rq + 2]), __float_as_uint(acc2[nt][4 * rq + 3])};
        lds_wr16(wb + (((8 * nt + 2 * rq + h) ^ (lane & 7)) << 4), v);
      }
  }
  BAR();
  {
    f32x4 r0 = {lo16(xres.x), hi16(xres.x), lo16(xres.y), hi16(xres.y)}, r1 = {lo16(xres.z), hi16(xres.z), lo16(xres.w), hi16(xres.w)};
    const u32 rb = L0 + (8 * w + lr) * 256;
#pragma unroll
    for (int j = 0; j < 4; ++j) {
      r0 += __builtin_bit_cast(f32x4, lds_rd16(rb + j * 16384 + (((2 * lc) ^ lr) << 4)));
      r1 += __builtin_bit_cast(f32x4, lds_rd16(rb + j * 16384 + (((2 * lc + 1) ^ lr) << 4)));
    }
    const u32x4 o = {pk2(r0[0], r0[1]), pk2(r0[2], r0[3]), pk2(r1[0], r1[1]), pk2(r1[2], r1[3])};
    const u32x4 so = mk_srd(pout, 2048 * 1024);
    st16(o, (64 * rt + 8 * w + lr) * 1024 + lc * 16, so, ct * 128);
  }
  STAMP(14);
  if (b == 0 && w == 0) {
#pragma unroll 1
    for (u32 j = 0; j < 4; ++j) {
      u32x2 fl = ld8_sc1(lane * 8, sws, WS_ARR + j * 512);
      WAITV1(0, fl);
      poll(fl, ~0ull, tlo, thi, lane * 8, sws, WS_ARR + j * 512);
    }
    if (lane == 0) st8_sc1(u32x2{tlo, thi}, 0, sws, WS_E);
    u64 cy1 = __builtin_amdgcn_s_memtime();
    STAMP(15);
    u32 X = 0;
    if (DIAG == 1) X = (u32)(stamp[15] - stamp[0]);
    if (DIAG == 2) X = (u32)(cy1 - cy0) / 24;
    if (DIAG == 3) X = (u32)(stamp[2] - stamp[0]);
    if (DIAG == 4) {
      u32x4 v = ld16_sc1(vlin, sws, 2048);
      WAITV1(0, v);
      int mn = 1 << 30, mx = -(1 << 30);
      for (int j = 0; j < 4; ++j) { int d = (int)(v[j] - (u32)stamp[0]); mn = min(mn, d); mx = max(mx, d); }
      for (int o = 32; o; o >>= 1) { mn = min(mn, __shfl_xor(mn, o)); mx = max(mx, __shfl_xor(mx, o)); }
      X = __builtin_amdgcn_readfirstlane(mx - mn);
    }
    if (DIAG == 5) X = (u32)(stamp[6] - stamp[0]);
    if (X > 10000) X = 10000;
    const u64 until = stamp[15] + 4 * X;
    while (__builtin_amdgcn_s_memrealtime() < until) __builtin_amdgcn_s_sleep(1);
  }
}

extern "C" void kernel_launch(void* const* d_in, const int* in_sizes, int n_in, void* d_out, int out_size, void* d_ws,
                              size_t ws_size, hipStream_t stream) {
  fused_block<<<256, 512, 0, stream>>>(d_in[0], d_in[1], d_in[2], d_in[4], d_in[5], d_out, d_ws);
}
```

```cpp
#define DIAG 7
#include <hip/hip_runtime.h>

typedef unsigned u32;
typedef unsigned long long u64;
typedef u32 u32x4 __attribute__((ext_vector_type(4)));
typedef u32 u32x2 __attribute__((ext_vector_type(2)));
typedef short s16x4 __attribute__((ext_vector_type(4)));
typedef short bf16x8 __attribute__((ext_vector_type(8)));
typedef float f32x16 __attribute__((ext_vector_type(16)));
typedef float f32x4 __attribute__((ext_vector_type(4)));
typedef float f32x2 __attribute__((ext_vector_type(2)));
typedef __bf16 bf2 __attribute__((ext_vector_type(2)));
#define LAS __attribute__((address_space(3)))
#define DI __device__ __forceinline__

namespace {
constexpr int SLOT = 32768;
constexpr u32 WS_E = 0, WS_ARR = 4096, WS_F = 8192, WS_KVF = 24576, WS_Z = 32768, WS_KV = 65536,
              WS_A2 = 1u << 20, A2_TILE = 118 * 1024, WS_END = WS_A2 + 64 * A2_TILE;
constexpr u32 SPIN_CAP = 1u << 17;

DI u32x4 mk_srd(const void* p, u32 bytes) {
  const u64 a = (u64)p;
  return u32x4{(u32)a, (u32)(a >> 32) & 0xffffu, bytes, 0x00020000u};
}
DI void dma16(u32 lds, u32 voff, u32x4 srd, u32 soff) {
  asm volatile("s_mov_b32 m0, %0\n\ts_nop 0\n\tbuffer_load_dwordx4 %1, %2, %3 offen lds" ::"s"(lds), "v"(voff), "s"(srd), "s"(soff) : "memory");
}
DI u32x4 ld16(u32 voff, u32x4 srd, u32 soff) {
  u32x4 r;
  asm volatile("buffer_load_dwordx4 %0, %1, %2, %3 offen" : "=v"(r) : "v"(voff), "s"(srd), "s"(soff) : "memory");
  return r;
}
DI u32x4 ld16_sc1(u32 voff, u32x4 srd, u32 soff) {
  u32x4 r;
  asm volatile("buffer_load_dwordx4 %0, %1, %2, %3 offen sc1" : "=v"(r) : "v"(voff), "s"(srd), "s"(soff) : "memory");
  return r;
}
DI u32x2 ld8_sc1(u32 voff, u32x4 srd, u32 soff) {
  u32x2 r;
  asm volatile("buffer_load_dwordx2 %0, %1, %2, %3 offen sc1" : "=v"(r) : "v"(voff), "s"(srd), "s"(soff) : "memory");
  return r;
}
DI u32 ld4_sc1(u32 voff, u32x4 srd, u32 soff) {
  u32 r;
  asm volatile("buffer_load_dword %0, %1, %2, %3 offen sc1" : "=v"(r) : "v"(voff), "s"(srd), "s"(soff) : "memory");
  return r;
}
DI void st16_sc1(u32x4 v, u32 voff, u32x4 srd, u32 soff) {
  asm volatile("buffer_store_dwordx4 %0, %1, %2, %3 offen sc1\n\ts_nop 1" ::"v"(v), "v"(voff), "s"(srd), "s"(soff) : "memory");
}
DI void st16(u32x4 v, u32 voff, u32x4 srd, u32 soff) {
  asm volatile("buffer_store_dwordx4 %0, %1, %2, %3 offen\n\ts_nop 1" ::"v"(v), "v"(voff), "s"(srd), "s"(soff) : "memory");
}
DI void st8_sc1(u32x2 v, u32 voff, u32x4 srd, u32 soff) {
  asm volatile("buffer_store_dwordx2 %0, %1, %2, %3 offen sc1" ::"v"(v), "v"(voff), "s"(srd), "s"(soff) : "memory");
}
DI void st4_sc1(u32 v, u32 voff, u32x4 srd, u32 soff) {
  asm volatile("buffer_store_dword %0, %1, %2, %3 offen sc1" ::"v"(v), "v"(voff), "s"(srd), "s"(soff) : "memory");
}
#define WAITV(n) asm volatile("s_waitcnt vmcnt(" #n ")" ::: "memory")
#define WAITV1(n, a) asm volatile("s_waitcnt vmcnt(" #n ")" : "+v"(a)::"memory")
#define WAITV4(n, a, b, c, d) asm volatile("s_waitcnt vmcnt(" #n ")" : "+v"(a), "+v"(b), "+v"(c), "+v"(d)::"memory")
#define SB() __builtin_amdgcn_sched_barrier(0)
#define BAR() do { SB(); asm volatile("s_waitcnt lgkmcnt(0)\n\ts_barrier" ::: "memory"); SB(); } while (0)
#define BARV(n) do { SB(); asm volatile("s_waitcnt vmcnt(" #n ") lgkmcnt(0)\n\ts_barrier" ::: "memory"); SB(); } while (0)

DI u32 pk2(float a, float b) { return __builtin_bit_cast(u32, __builtin_convertvector((f32x2{a, b}), bf2)); }
DI float lo16(u32 x) { return __uint_as_float(x << 16); }
DI float hi16(u32 x) { return __uint_as_float(x & 0xffff0000u); }
DI f32x16 mfma(u32x4 a, u32x4 b, f32x16 c) {
  return __builtin_amdgcn_mfma_f32_32x32x16_bf16(__builtin_bit_cast(bf16x8, a), __builtin_bit_cast(bf16x8, b), c, 0, 0, 0);
}
DI u32x4 trfrag(u32 a0, u32 a1) {
  const s16x4 v = __builtin_amdgcn_ds_read_tr16_b64_v4i16((LAS s16x4*)a0);
  const s16x4 w = __builtin_amdgcn_ds_read_tr16_b64_v4i16((LAS s16x4*)a1);
  return __builtin_bit_cast(u32x4, __builtin_shufflevector(v, w, 0, 1, 2, 3, 4, 5, 6, 7));
}
DI u32x4 lds_rd16(u32 a) { return *(LAS u32x4*)a; }
DI void lds_wr16(u32 a, u32x4 v) { *(LAS u32x4*)a = v; }
DI float phi(float t) { return (t > 0.f ? t + 1.f : __expf(t)) + 1e-6f; }
DI float dpp_add(float v, const int ctrl_sel) {
  int t;
  if (ctrl_sel == 0) t = __builtin_amdgcn_update_dpp(0, __float_as_int(v), 0xB1, 0xf, 0xf, true);
  else if (ctrl_sel == 1) t = __builtin_amdgcn_update_dpp(0, __float_as_int(v), 0x4E, 0xf, 0xf, true);
  else t = __builtin_amdgcn_update_dpp(0, __float_as_int(v), 0x141, 0xf, 0xf, true);
  return v + __int_as_float(t);
}
DI f32x16 zero16() {
  f32x16 z;
#pragma unroll
  for (int i = 0; i < 16; ++i) z[i] = 0.f;
  return z;
}
DI void poll(u32x2 v, u64 mask, u32 tlo, u32 thi, u32 voff, u32x4 srd, u32 soff) {
  for (u32 spins = 0; spins < SPIN_CAP; ++spins) {
    const u64 ok = __builtin_amdgcn_ballot_w64(v.x == tlo && v.y == thi);
    if ((ok & mask) == mask) break;
    __builtin_amdgcn_s_sleep(2);
    v = ld8_sc1(voff, srd, soff);
    WAITV1(0, v);
  }
}
}

#define STAMP(i) do { __builtin_amdgcn_sched_barrier(0); asm volatile("s_memrealtime %0\n\ts_waitcnt lgkmcnt(0)" : "=s"(stamp[i])::"memory"); __builtin_amdgcn_sched_barrier(0); } while (0)

template <bool TA>
DI void kloop(f32x16& acc, const u32x4 (&xf)[32], u32 aE, u32 aO, const int f0, const int f1, const bool xfirst) {
  u32x4 wf[2][4];
#pragma unroll
  for (int j = 0; j < 4; ++j) wf[0][j] = trfrag(aE + 1024 * (f0 + j), aO + 1024 * (f0 + j));
#pragma unroll
  for (int f = f0; f < f1; f += 4) {
    const int cur = ((f - f0) >> 2) & 1;
    SB();
#pragma unroll
    for (int j = 0; j < 4; ++j) {
      if (f + 4 < f1) wf[cur ^ 1][j] = trfrag(aE + 1024 * (f + 4 + j), aO + 1024 * (f + 4 + j));
      acc = xfirst ? mfma(xf[f + j], wf[cur][j], acc) : mfma(wf[cur][j], xf[f + j], acc);
      asm volatile("" : "+v"(acc));
      SB();
    }
  }
}

__global__ __launch_bounds__(512, 2) void fused_block(const void* __restrict__ px, const void* __restrict__ pwqkv,
                                                      const void* __restrict__ pwo, const void* __restrict__ pw1,
                                                      const void* __restrict__ pw2, void* __restrict__ pout,
                                                      void* __restrict__ pws) {
  __shared__ __attribute__((aligned(1024))) char smem[5 * SLOT];
  const u32 L0 = (u32)(size_t)(LAS char*)smem;
  u64 stamp[16];
  u64 cy0 = __builtin_amdgcn_s_memtime();
  STAMP(0);
  asm volatile("" ::"s"(px), "s"(pwqkv), "s"(pwo), "s"(pw1), "s"(pw2), "s"(pout), "s"(pws));
  const u32 tid = threadIdx.x, lane = tid & 63;
  const u32 w = __builtin_amdgcn_readfirstlane(tid >> 6);
  const u32 s = w & 3, g = w >> 2;
  const u32 b = blockIdx.x, xcd = b & 7, r = b >> 3;
  const u32 p = (xcd >> 1) * 4 + (r >> 3);
  const u32 cl = (r & 3) + 4 * (xcd & 1);
  const bool typeA = ((r >> 2) & 1) == 0;
  const u32 rot = ((r & 7) + 2 * (r >> 3)) & 7;
  const u32 rt = xcd * 4 + (r >> 3), ct = r & 7;

  const u32x4 sx = mk_srd(px, 2048 * 1024), sws = mk_srd(pws, WS_END);
  const u32x4 sw = typeA ? mk_srd(pwqkv, 512 * 3072) : mk_srd(pw1, 512 * 2752);
  const u32 ldw = typeA ? 3072 : 2752;
  const u32 lr = lane >> 3, lc = lane & 7, h = lane >> 5, q = (lane & 15) >> 2;

  u32x2 ep = ld8_sc1(0, sws, WS_E);
  u32x4 xr[16];
  {
    const u32 vo = (16 * w + lr) * 1024 + lc * 16;
#pragma unroll
    for (int i = 0; i < 8; ++i)
#pragma unroll
      for (int g2 = 0; g2 < 2; ++g2) xr[2 * i + g2] = ld16(vo, sx, p * 131072 + g2 * 8192 + ((i + rot) & 7) * 128);
  }
  const u32 s0 = (cl * 43 + 5) >> 3;
  const u32 nhu = (((cl + 1) * 43 + 5) >> 3) - s0;
  const u32 pa = s0 & 1;
  const u32 vw = 2 * lr * ldw + ((lc ^ (((lane >> 4) & 1) << 2)) << 4);
  auto wblock = [&](u32 colb, u32 par, u32 slot, int t) {
    const u32 n = w + 8 * t;
    const u32 k = (((n >> 2) + rot) & 7) * 64 + (n & 3) * 16 + par;
    dma16(L0 + slot * SLOT + n * 1024, vw, sw, k * ldw + colb);
  };
  const u32 cK = 1024 + 128 * cl, cV = 2048 + 128 * cl, cQ = 128 * cl;
  const u32 cA = 64 * s0, cM = 64 * s0 - 64;
  const u32 bc0 = typeA ? cK : cA, bp0 = typeA ? 0 : pa;
  const u32 bc1 = typeA ? cK : cM, bp1 = typeA ? 1 : pa ^ 1;
  const u32 bc2 = typeA ? cV : cM + 128, bp2 = typeA ? 0 : pa ^ 1;
  const u32 bc3 = typeA ? cV : cA + 128, bp3 = typeA ? 1 : pa;
  const u32 bc4 = typeA ? cQ : cM + 256, bp4 = typeA ? 0 : pa ^ 1;
  const u32 bc5 = typeA ? cQ : cA + 256, bp5 = typeA ? 1 : pa;
  const u32 bc6 = cM + 384, bp6 = pa ^ 1;
#pragma unroll
  for (int t = 0; t < 4; ++t) {
    wblock(bc0, bp0, 0, t);
    wblock(bc1, bp1, 1, t);
    wblock(bc2, bp2, 2, t);
  }
  STAMP(1);
  const u32 trb = (4 * h + q) * 128 + ((32 * ((lane >> 4) & 1) + 8 * (lane & 3)) ^ (64 * (q >> 1)));
  u32 aE[3], aO[3];
  {
    const u32 sA[3] = {0, 3, 0}, sM0[3] = {1, 2, 4}, sM1[3] = {2, 4, 1}, sE[3] = {0, 2, 4}, sOd[3] = {1, 3, 0};
#pragma unroll
    for (int j = 0; j < 3; ++j) {
      const u32 al = sA[j] * SLOT + 64 * g, mi = (g ? sM1[j] : sM0[j]) * SLOT + 64 * (g ^ 1);
      const u32 e = typeA ? sE[j] * SLOT + 64 * g : (pa ? mi : al);
      const u32 o = typeA ? sOd[j] * SLOT + 64 * g : (pa ? al : mi);
      aE[j] = L0 + (trb ^ e);
      aO[j] = L0 + (trb ^ o);
    }
  }

  float sum[2] = {0.f, 0.f}, sq[2] = {0.f, 0.f};
  const bf2 ones2 = __builtin_bit_cast(bf2, 0x3f803f80u);
  auto stat = [&](int i) {
#pragma unroll
    for (int d = 0; d < 4; ++d) {
      const u32 t_ = xr[i][d];
      const bf2 v = __builtin_bit_cast(bf2, t_);
      sum[i & 1] = __builtin_amdgcn_fdot2_f32_bf16(v, ones2, sum[i & 1], false);
      sq[i & 1] = __builtin_amdgcn_fdot2_f32_bf16(v, v, sq[i & 1], false);
    }
  };
  WAITV1(28, ep);
  const u64 tag64 = (((u64)(u32)__builtin_amdgcn_readfirstlane(ep.y) << 32) | (u32)__builtin_amdgcn_readfirstlane(ep.x)) + 1;
  const u32 tlo = (u32)tag64, thi = (u32)(tag64 >> 32);
  WAITV4(24, xr[0], xr[1], xr[2], xr[3]);
  stat(0); stat(1); stat(2); stat(3);
  WAITV4(20, xr[4], xr[5], xr[6], xr[7]);
  stat(4); stat(5); stat(6); stat(7);
  WAITV4(16, xr[8], xr[9], xr[10], xr[11]);
  stat(8); stat(9); stat(10); stat(11);
  WAITV4(12, xr[12], xr[13], xr[14], xr[15]);
  STAMP(2);
  stat(12); stat(13); stat(14); stat(15);
  float rs[2], nb[2];
#pragma unroll
  for (int g2 = 0; g2 < 2; ++g2) {
    float a = sum[g2], c2 = sq[g2];
    a = dpp_add(a, 0); c2 = dpp_add(c2, 0);
    a = dpp_add(a, 1); c2 = dpp_add(c2, 1);
    a = dpp_add(a, 2); c2 = dpp_add(c2, 2);
    const float m = a * (1.f / 512.f);
    const float var = fmaxf(c2 * (1.f / 512.f) - m * m, 0.f);
    rs[g2] = __builtin_amdgcn_rsqf(var + 1e-5f);
    nb[g2] = -m * rs[g2];
  }
  const u32 stw = (16 * w + lr) * 256 + ((lc ^ lr) << 4);
  auto stage = [&](int rd) {
    const u32 base = L0 + (3 + (rd & 1)) * SLOT;
#pragma unroll
    for (int ii = 0; ii < 2; ++ii)
#pragma unroll
      for (int g2 = 0; g2 < 2; ++g2) {
        const u32x4 v = xr[2 * (2 * rd + ii) + g2];
        u32x4 o;
        o.x = pk2(fmaf(lo16(v.x), rs[g2], nb[g2]), fmaf(lo16(v.y), rs[g2], nb[g2]));
        o.y = pk2(fmaf(lo16(v.z), rs[g2], nb[g2]), fmaf(lo16(v.w), rs[g2], nb[g2]));
        o.z = pk2(fmaf(hi16(v.x), rs[g2], nb[g2]), fmaf(hi16(v.y), rs[g2], nb[g2]));
        o.w = pk2(fmaf(hi16(v.z), rs[g2], nb[g2]), fmaf(hi16(v.w), rs[g2], nb[g2]));
        lds_wr16(base + stw + g2 * 2048 + ((ii ^ g2) << 7), o);
      }
  };
  u32x4 xf[32];
  const u32 str = (32 * s + (lane & 31)) * 256 + ((h ^ (lane & 15)) << 4);
  auto unstage = [&](int rd) {
    const u32 base = L0 + (3 + (rd & 1)) * SLOT;
#pragma unroll
    for (int f = 0; f < 8; ++f) xf[8 * rd + f] = lds_rd16(base + (str ^ (32 * f)));
  };

  const u32 T = 4 * p + s;
  const u32 vlin = lane * 16;
  const u32 a2w = WS_A2 + T * A2_TILE;
  const u32x4 sw2 = mk_srd(pw2, 1376 * 1024), swo = mk_srd(pwo, 512 * 1024);
  const u32 vb2 = lr * 1024 + ((lc ^ (((lane >> 4) & 1) << 2)) << 4);
  const u32 rot2 = (ct * 11 + (r >> 3) * 22) % 88, rot3 = (ct * 4 + (r >> 3) * 8) & 31;
  auto kap_h = [&](u32 pos) { const u32 k = pos + rot2; return k >= 88 ? k - 88 : k; };
  auto b2block = [&](const int blk) {
#pragma unroll
    for (int t = 0; t < 4; ++t) {
      const u32 n = w + 8 * t;
      const int pos0 = 16 * blk + 4 * t;
      if (pos0 >= 120) continue;
      const u32 pos = pos0 + (w >> 1);
      const u32 dst = L0 + ((blk + 2) % 5) * SLOT + n * 1024;
      if (pos0 < 88) dma16(dst, vb2, sw2, kap_h(pos) * 16384 + (w & 1) * 8192 + ct * 128);
      else dma16(dst, vb2, swo, ((pos - 88 + rot3) & 31) * 16384 + (w & 1) * 8192 + ct * 128);
    }
  };

  auto step1 = [&](f32x16& acc, const bool xfirst) {
    stage(0);
    BARV(9);
    STAMP(3);
    if (w == 0 && lane == 0) { st4_sc1((u32)stamp[0], 0, sws, 2048 + b * 4); WAITV(0); }
    if (w == 0 && lane == 0) st8_sc1(u32x2{tlo, thi}, 0, sws, WS_ARR + b * 8);
    unstage(0);
    stage(1);
    kloop<true>(acc, xf, aE[0], aO[0], 0, 8, xfirst);
    BARV(6);
    unstage(1);
    stage(2);
    kloop<true>(acc, xf, aE[0], aO[0], 8, 16, xfirst);
    BARV(3);
    unstage(2);
    stage(3);
    kloop<true>(acc, xf, aE[0], aO[0], 16, 24, xfirst);
    BARV(0);
#pragma unroll
    for (int t = 0; t < 4; ++t) wblock(bc3, bp3, 3, t);
    unstage(3);
    BAR();
#pragma unroll
    for (int t = 0; t < 4; ++t) wblock(bc4, bp4, 4, t);
    kloop<true>(acc, xf, aE[0], aO[0], 24, 32, xfirst);
  };

  if (typeA) {
    f32x16 acc = zero16();
    step1(acc, true);
    STAMP(4);
    BARV(4);
#pragma unroll
    for (int t = 0; t < 4; ++t) wblock(bc5, bp5, 0, t);
    u32x4 pk[2];
#pragma unroll
    for (int i = 0; i < 8; ++i) pk[i >> 2][i & 3] = pk2(phi(acc[2 * i]), phi(acc[2 * i + 1]));
    acc = zero16();
    kloop<true>(acc, xf, aE[1], aO[1], 0, 32, true);
    STAMP(5);
    BARV(0);
    b2block(0);
    b2block(1);
    {
      u32x4 vv[2];
#pragma unroll
      for (int i = 0; i < 8; ++i) vv[i >> 2][i & 3] = pk2(acc[2 * i], acc[2 * i + 1]);
      f32x16 kv = mfma(pk[0], vv[0], zero16());
      kv = mfma(pk[1], vv[1], kv);
      const u32x4 ones = {0x3f803f80u, 0x3f803f80u, 0x3f803f80u, 0x3f803f80u};
      f32x16 zz = mfma(ones, pk[0], zero16());
      zz = mfma(ones, pk[1], zz);
      const u32 hm = 0u - ((lane >> 4) & 1);
      u32x4 u0, u1;
#pragma unroll
      for (int j = 0; j < 4; ++j) {
        u0[j] = (__float_as_uint(kv[8 + j]) & hm) | (__float_as_uint(kv[j]) & ~hm);
        u1[j] = (__float_as_uint(kv[12 + j]) & hm) | (__float_as_uint(kv[4 + j]) & ~hm);
      }
      lds_wr16(L0 + SLOT + w * 2048 + vlin, u0);
      lds_wr16(L0 + SLOT + w * 2048 + 1024 + vlin, u1);
      *(LAS float*)(L0 + SLOT + 16384 + w * 256 + lane * 4) = zz[0];
    }
    BAR();
    if (s == 0) {
      f32x4 a0 = {0, 0, 0, 0}, a1 = {0, 0, 0, 0};
      float z = 0.f;
#pragma unroll
      for (int j = 0; j < 4; ++j) {
        a0 += __builtin_bit_cast(f32x4, lds_rd16(L0 + SLOT + (w + j) * 2048 + vlin));
        a1 += __builtin_bit_cast(f32x4, lds_rd16(L0 + SLOT + (w + j) * 2048 + 1024 + vlin));
        z += *(LAS float*)(L0 + SLOT + 16384 + (w + j) * 256 + lane * 4);
      }
      const u32 u = (cl * 2 + g) * 16 + p;
      st16_sc1(__builtin_bit_cast(u32x4, a0), vlin, sws, WS_KV + u * 2048);
      st16_sc1(__builtin_bit_cast(u32x4, a1), vlin, sws, WS_KV + u * 2048 + 1024);
      if (lane < 32) st4_sc1(__float_as_uint(z), lane * 4, sws, WS_Z + u * 128);
    }
    acc = zero16();
    kloop<true>(acc, xf, aE[2], aO[2], 0, 16, false);
    if (s == 0) {
      WAITV(0);
      if (lane == 0) st8_sc1(u32x2{tlo, thi}, 0, sws, WS_KVF + ((cl * 2 + g) * 16 + p) * 8);
    }
    kloop<true>(acc, xf, aE[2], aO[2], 16, 32, false);
    STAMP(6);
    const u32 ug = (cl * 2 + g) * 16 + 4 * s;
    u32x2 fl = ld8_sc1((lane & 3) * 8, sws, WS_KVF + ug * 8);
    u32x4 pq[2];
#pragma unroll
    for (int i = 0; i < 8; ++i) pq[i >> 2][i & 3] = pk2(phi(acc[2 * i]), phi(acc[2 * i + 1]));
    BAR();
    WAITV1(0, fl);
    poll(fl, 0xfull, tlo, thi, (lane & 3) * 8, sws, WS_KVF + ug * 8);
    STAMP(7);
    {
      u32x4 t[8];
      u32 zt[2];
#pragma unroll
      for (int j = 0; j < 8; ++j) t[j] = ld16_sc1(vlin, sws, WS_KV + ug * 2048 + j * 1024);
      zt[0] = ld4_sc1(lane * 4, sws, WS_Z + ug * 128);
      zt[1] = ld4_sc1(lane * 4, sws, WS_Z + ug * 128 + 256);
      b2block(2);
      b2block(3);
      WAITV4(8, t[0], t[1], t[2], t[3]);
      WAITV4(8, t[4], t[5], t[6], t[7]);
      asm volatile("" : "+v"(zt[0]), "+v"(zt[1]));
      f32x4 a0 = (__builtin_bit_cast(f32x4, t[0]) + __builtin_bit_cast(f32x4, t[2])) + (__builtin_bit_cast(f32x4, t[4]) + __builtin_bit_cast(f32x4, t[6]));
      f32x4 a1 = (__builtin_bit_cast(f32x4, t[1]) + __builtin_bit_cast(f32x4, t[3])) + (__builtin_bit_cast(f32x4, t[5]) + __builtin_bit_cast(f32x4, t[7]));
      lds_wr16(L0 + SLOT + w * 2048 + vlin, __builtin_bit_cast(u32x4, a0));
      lds_wr16(L0 + SLOT + w * 2048 + 1024 + vlin, __builtin_bit_cast(u32x4, a1));
      *(LAS float*)(L0 + SLOT + 16384 + w * 256 + lane * 4) = __uint_as_float(zt[0]) + __uint_as_float(zt[1]);
    }
    BAR();
    {
      f32x4 a0 = {0, 0, 0, 0}, a1 = {0, 0, 0, 0}, z0 = {0, 0, 0, 0}, z1 = {0, 0, 0, 0};
      const u32 hd = (lane >> 4) & 1;
      const u32 za = L0 + SLOT + 16384 + g * 1024 + hd * 64 + h * 16;
#pragma unroll
      for (int j = 0; j < 4; ++j) {
        a0 += __builtin_bit_cast(f32x4, lds_rd16(L0 + SLOT + (4 * g + j) * 2048 + vlin));
        a1 += __builtin_bit_cast(f32x4, lds_rd16(L0 + SLOT + (4 * g + j) * 2048 + 1024 + vlin));
      }
#pragma unroll
      for (int j = 0; j < 8; ++j) {
        z0 += __builtin_bit_cast(f32x4, lds_rd16(za + j * 128));
        z1 += __builtin_bit_cast(f32x4, lds_rd16(za + j * 128 + 32));
      }
      const u32x4 kf = {pk2(a0[0], a0[1]), pk2(a0[2], a0[3]), pk2(a1[0], a1[1]), pk2(a1[2], a1[3])};
      const u32x4 zf = {pk2(z0[0], z0[1]), pk2(z0[2], z0[3]), pk2(z1[0], z1[1]), pk2(z1[2], z1[3])};
      const u32x4 zr = {0, 0, 0, 0};
      f32x16 num = mfma(hd ? zr : kf, pq[0], zero16());
      num = mfma(hd ? kf : zr, pq[1], num);
      f32x16 den = mfma(hd ? zr : zf, pq[0], zero16());
      den = mfma(hd ? zf : zr, pq[1], den);
      u32x4 o[2];
#pragma unroll
      for (int i = 0; i < 8; ++i)
        o[i >> 2][i & 3] = pk2(num[2 * i] * __builtin_amdgcn_rcpf(den[2 * i] + 1e-6f), num[2 * i + 1] * __builtin_amdgcn_rcpf(den[2 * i + 1] + 1e-6f));
      st16_sc1(o[0], vlin, sws, a2w + (86 + 4 * cl + 2 * g) * 1024);
      st16_sc1(o[1], vlin, sws, a2w + (87 + 4 * cl + 2 * g) * 1024);
    }
    BAR();
    b2block(4);
    WAITV(4);
    if (lane == 0) st8_sc1(u32x2{tlo, thi}, 0, sws, WS_F + T * 256 + 128 + (cl * 2 + g) * 8);
  } else {
    auto hstore = [&](const f32x16& acc, u32 u) {
      u32x4 o[2];
#pragma unroll
      for (int i = 0; i < 8; ++i) o[i >> 2][i & 3] = pk2(fmaxf(acc[2 * i], 0.f), fmaxf(acc[2 * i + 1], 0.f));
      st16_sc1(o[0], vlin, sws, a2w + u * 2048);
      st16_sc1(o[1], vlin, sws, a2w + u * 2048 + 1024);
    };
    f32x16 acc = zero16();
    step1(acc, false);
    STAMP(4);
    BARV(0);
#pragma unroll
    for (int t = 0; t < 4; ++t) wblock(bc5, bp5, 0, t);
    if (nhu == 6) {
#pragma unroll
      for (int t = 0; t < 4; ++t) wblock(bc6, bp6, 1, t);
    }
    hstore(acc, s0 + g);
    acc = zero16();
    kloop<false>(acc, xf, aE[1], aO[1], 0, 32, false);
    STAMP(5);
    BARV(0);
    b2block(0);
    b2block(1);
    hstore(acc, s0 + 2 + g);
    if (4 + g < nhu) {
      acc = zero16();
      kloop<false>(acc, xf, aE[2], aO[2], 0, 32, false);
      hstore(acc, s0 + 4 + g);
    }
    STAMP(6);
    BAR();
    b2block(2);
    b2block(3);
    b2block(4);
    WAITV(12);
    STAMP(7);
    if (lane == 0) st8_sc1(u32x2{tlo, thi}, 0, sws, WS_F + T * 256 + (cl * 2 + g) * 8);
  }

  const u32 kq = s, mh = g;
  const u32 T2 = 2 * rt + mh;
  const u32 fo = WS_F + T2 * 256, vf = (lane & 31) * 8;
  STAMP(8);
  {
    u32x2 fl = ld8_sc1(vf, sws, fo);
    WAITV1(0, fl);
    poll(fl, 0xffffull, tlo, thi, vf, sws, fo);
  }
  STAMP(9);
  u32x4 a2[30];
  const u32 a2r = WS_A2 + T2 * A2_TILE;
#pragma unroll
  for (int m = 0; m < 22; ++m) {
    const u32 k = kap_h(4 * m + kq);
    a2[m] = ld16_sc1(vlin, sws, k < 86 ? a2r + k * 1024 : WS_END);
  }
  u32x2 flo = ld8_sc1(vf, sws, fo);
  u32x4 xres = ld16((64 * rt + 8 * w + lr) * 1024 + lc * 16, sx, ct * 128);
  BAR();
  f32x16 acc2[2] = {zero16(), zero16()};
  const u32 tb0 = L0 + kq * 2048 + trb, tb1 = L0 + kq * 2048 + (trb ^ 64);
  auto pstep = [&](const int m) {
    const u32 o = ((m / 4 + 2) % 5) * SLOT + (m & 3) * 8192;
    acc2[0] = mfma(trfrag(tb0 + o, tb0 + o + 1024), a2[m], acc2[0]);
    acc2[1] = mfma(trfrag(tb1 + o, tb1 + o + 1024), a2[m], acc2[1]);
    asm volatile("" : "+v"(acc2[0]), "+v"(acc2[1]));
  };
  WAITV4(20, a2[0], a2[1], a2[2], a2[3]);
  STAMP(10);
  pstep(0); pstep(1); pstep(2); pstep(3);
  BAR();
  b2block(5);
  WAITV4(20, a2[4], a2[5], a2[6], a2[7]);
  pstep(4); pstep(5); pstep(6); pstep(7);
  BAR();
  b2block(6);
  WAITV4(20, a2[8], a2[9], a2[10], a2[11]);
  pstep(8); pstep(9); pstep(10); pstep(11);
  BAR();
  b2block(7);
  WAITV4(18, a2[12], a2[13], a2[14], a2[15]);
  pstep(12); pstep(13); pstep(14); pstep(15);
  WAITV4(14, a2[16], a2[17], a2[18], a2[19]);
  pstep(16); pstep(17); pstep(18); pstep(19);
  WAITV4(10, a2[20], a2[21], flo, xres);
  STAMP(11);
  poll(flo, 0xffff0000ull, tlo, thi, vf, sws, fo);
  STAMP(12);
#pragma unroll
  for (int m = 22; m < 30; ++m) a2[m] = ld16_sc1(vlin, sws, a2r + (86 + ((4 * (m - 22) + kq + rot3) & 31)) * 1024);
  BARV(8);
  pstep(20); pstep(21);
  WAITV4(4, a2[22], a2[23], a2[24], a2[25]);
  pstep(22); pstep(23); pstep(24); pstep(25);
  WAITV4(0, a2[26], a2[27], a2[28], a2[29]);
  STAMP(13);
  pstep(26); pstep(27); pstep(28); pstep(29);
  {
    const u32 wb = L0 + (kq * 2 + mh) * 8192 + (lane & 31) * 256;
#pragma unroll
    for (int nt = 0; nt < 2; ++nt)
#pragma unroll
      for (int rq = 0; rq < 4; ++rq) {
        const u32x4 v = {__float_as_uint(acc2[nt][4 * rq]), __float_as_uint(acc2[nt][4 * rq + 1]), __float_as_uint(acc2[nt][4 * rq + 2]), __float_as_uint(acc2[nt][4 * rq + 3])};
        lds_wr16(wb + (((8 * nt + 2 * rq + h) ^ (lane & 7)) << 4), v);
      }
  }
  BAR();
  {
    f32x4 r0 = {lo16(xres.x), hi16(xres.x), lo16(xres.y), hi16(xres.y)}, r1 = {lo16(xres.z), hi16(xres.z), lo16(xres.w), hi16(xres.w)};
    const u32 rb = L0 + (8 * w + lr) * 256;
#pragma unroll
    for (int j = 0; j < 4; ++j) {
      r0 += __builtin_bit_cast(f32x4, lds_rd16(rb + j * 16384 + (((2 * lc) ^ lr) << 4)));
      r1 += __builtin_bit_cast(f32x4, lds_rd16(rb + j * 16384 + (((2 * lc + 1) ^ lr) << 4)));
    }
    const u32x4 o = {pk2(r0[0], r0[1]), pk2(r0[2], r0[3]), pk2(r1[0], r1[1]), pk2(r1[2], r1[3])};
    const u32x4 so = mk_srd(pout, 2048 * 1024);
    st16_sc1(o, (64 * rt + 8 * w + lr) * 1024 + lc * 16, so, ct * 128);
  }
  STAMP(14);
  if (b == 0 && w == 0) {
#pragma unroll 1
    for (u32 j = 0; j < 4; ++j) {
      u32x2 fl = ld8_sc1(lane * 8, sws, WS_ARR + j * 512);
      WAITV1(0, fl);
      poll(fl, ~0ull, tlo, thi, lane * 8, sws, WS_ARR + j * 512);
    }
    if (lane == 0) st8_sc1(u32x2{tlo, thi}, 0, sws, WS_E);
    u64 cy1 = __builtin_amdgcn_s_memtime();
    STAMP(15);
    u32 X = 0;
    if (DIAG == 1) X = (u32)(stamp[15] - stamp[0]);
    if (DIAG == 2) X = (u32)(cy1 - cy0) / 24;
    if (DIAG == 3) X = (u32)(stamp[2] - stamp[0]);
    if (DIAG == 4) {
      u32x4 v = ld16_sc1(vlin, sws, 2048);
      WAITV1(0, v);
      int mn = 1 << 30, mx = -(1 << 30);
      for (int j = 0; j < 4; ++j) { int d = (int)(v[j] - (u32)stamp[0]); mn = min(mn, d); mx = max(mx, d); }
      for (int o = 32; o; o >>= 1) { mn = min(mn, __shfl_xor(mn, o)); mx = max(mx, __shfl_xor(mx, o)); }
      X = __builtin_amdgcn_readfirstlane(mx - mn);
    }
    if (DIAG == 5) X = (u32)(stamp[6] - stamp[0]);
    if (DIAG == 9) X = (u32)(stamp[13] - stamp[0]);
    if (DIAG == 10) X = (u32)(stamp[14] - stamp[0]);
    if (X > 10000) X = 10000;
    u64 until = stamp[0] + 6000 + 4 * X;
    while (__builtin_amdgcn_s_memrealtime() < until) __builtin_amdgcn_s_sleep(1);
  }
}

extern "C" void kernel_launch(void* const* d_in, const int* in_sizes, int n_in, void* d_out, int out_size, void* d_ws,
                              size_t ws_size, hipStream_t stream) {
  fused_block<<<256, 512, 0, stream>>>(d_in[0], d_in[1], d_in[2], d_in[4], d_in[5], d_out, d_ws);
}
```

```cpp
#define DIAG 23
#include <hip/hip_runtime.h>

typedef unsigned u32;
typedef unsigned long long u64;
typedef u32 u32x4 __attribute__((ext_vector_type(4)));
typedef u32 u32x2 __attribute__((ext_vector_type(2)));
typedef short s16x4 __attribute__((ext_vector_type(4)));
typedef short bf16x8 __attribute__((ext_vector_type(8)));
typedef float f32x16 __attribute__((ext_vector_type(16)));
typedef float f32x4 __attribute__((ext_vector_type(4)));
typedef float f32x2 __attribute__((ext_vector_type(2)));
typedef __bf16 bf2 __attribute__((ext_vector_type(2)));
#define LAS __attribute__((address_space(3)))
#define DI __device__ __forceinline__

namespace {
constexpr int SLOT = 32768;
constexpr u32 WS_E = 0, WS_ARR = 4096, WS_F = 8192, WS_KVF = 24576, WS_Z = 32768, WS_KV = 65536,
              WS_A2 = 1u << 20, A2_TILE = 118 * 1024, WS_END = WS_A2 + 64 * A2_TILE;
constexpr u32 SPIN_CAP = 1u << 17;

DI u32x4 mk_srd(const void* p, u32 bytes) {
  const u64 a = (u64)p;
  return u32x4{(u32)a, (u32)(a >> 32) & 0xffffu, bytes, 0x00020000u};
}
DI void dma16(u32 lds, u32 voff, u32x4 srd, u32 soff) {
  asm volatile("s_mov_b32 m0, %0\n\ts_nop 0\n\tbuffer_load_dwordx4 %1, %2, %3 offen lds" ::"s"(lds), "v"(voff), "s"(srd), "s"(soff) : "memory");
}
DI u32x4 ld16(u32 voff, u32x4 srd, u32 soff) {
  u32x4 r;
  asm volatile("buffer_load_dwordx4 %0, %1, %2, %3 offen" : "=v"(r) : "v"(voff), "s"(srd), "s"(soff) : "memory");
  return r;
}
DI u32x4 ld16_sc1(u32 voff, u32x4 srd, u32 soff) {
  u32x4 r;
  asm volatile("buffer_load_dwordx4 %0, %1, %2, %3 offen sc1" : "=v"(r) : "v"(voff), "s"(srd), "s"(soff) : "memory");
  return r;
}
DI u32x2 ld8_sc1(u32 voff, u32x4 srd, u32 soff) {
  u32x2 r;
  asm volatile("buffer_load_dwordx2 %0, %1, %2, %3 offen sc1" : "=v"(r) : "v"(voff), "s"(srd), "s"(soff) : "memory");
  return r;
}
DI u32 ld4_sc1(u32 voff, u32x4 srd, u32 soff) {
  u32 r;
  asm volatile("buffer_load_dword %0, %1, %2, %3 offen sc1" : "=v"(r) : "v"(voff), "s"(srd), "s"(soff) : "memory");
  return r;
}
DI void st16_sc1(u32x4 v, u32 voff, u32x4 srd, u32 soff) {
  asm volatile("buffer_store_dwordx4 %0, %1, %2, %3 offen sc1\n\ts_nop 1" ::"v"(v), "v"(voff), "s"(srd), "s"(soff) : "memory");
}
DI void st16(u32x4 v, u32 voff, u32x4 srd, u32 soff) {
  asm volatile("buffer_store_dwordx4 %0, %1, %2, %3 offen\n\ts_nop 1" ::"v"(v), "v"(voff), "s"(srd), "s"(soff) : "memory");
}
DI void st8_sc1(u32x2 v, u32 voff, u32x4 srd, u32 soff) {
  asm volatile("buffer_store_dwordx2 %0, %1, %2, %3 offen sc1" ::"v"(v), "v"(voff), "s"(srd), "s"(soff) : "memory");
}
DI void st4_sc1(u32 v, u32 voff, u32x4 srd, u32 soff) {
  asm volatile("buffer_store_dword %0, %1, %2, %3 offen sc1" ::"v"(v), "v"(voff), "s"(srd), "s"(soff) : "memory");
}
#define WAITV(n) asm volatile("s_waitcnt vmcnt(" #n ")" ::: "memory")
#define WAITV1(n, a) asm volatile("s_waitcnt vmcnt(" #n ")" : "+v"(a)::"memory")
#define WAITV4(n, a, b, c, d) asm volatile("s_waitcnt vmcnt(" #n ")" : "+v"(a), "+v"(b), "+v"(c), "+v"(d)::"memory")
#define SB() __builtin_amdgcn_sched_barrier(0)
#define BAR() do { SB(); asm volatile("s_waitcnt lgkmcnt(0)\n\ts_barrier" ::: "memory"); SB(); } while (0)
#define BARV(n) do { SB(); asm volatile("s_waitcnt vmcnt(" #n ") lgkmcnt(0)\n\ts_barrier" ::: "memory"); SB(); } while (0)

DI u32 pk2(float a, float b) { return __builtin_bit_cast(u32, __builtin_convertvector((f32x2{a, b}), bf2)); }
DI float lo16(u32 x) { return __uint_as_float(x << 16); }
DI float hi16(u32 x) { return __uint_as_float(x & 0xffff0000u); }
DI f32x16 mfma(u32x4 a, u32x4 b, f32x16 c) {
  return __builtin_amdgcn_mfma_f32_32x32x16_bf16(__builtin_bit_cast(bf16x8, a), __builtin_bit_cast(bf16x8, b), c, 0, 0, 0);
}
DI u32x4 trfrag(u32 a0, u32 a1) {
  const s16x4 v = __builtin_amdgcn_ds_read_tr16_b64_v4i16((LAS s16x4*)a0);
  const s16x4 w = __builtin_amdgcn_ds_read_tr16_b64_v4i16((LAS s16x4*)a1);
  return __builtin_bit_cast(u32x4, __builtin_shufflevector(v, w, 0, 1, 2, 3, 4, 5, 6, 7));
}
DI u32x4 lds_rd16(u32 a) { return *(LAS u32x4*)a; }
DI void lds_wr16(u32 a, u32x4 v) { *(LAS u32x4*)a = v; }
DI float phi(float t) { return (t > 0.f ? t + 1.f : __expf(t)) + 1e-6f; }
DI float dpp_add(float v, const int ctrl_sel) {
  int t;
  if (ctrl_sel == 0) t = __builtin_amdgcn_update_dpp(0, __float_as_int(v), 0xB1, 0xf, 0xf, true);
  else if (ctrl_sel == 1) t = __builtin_amdgcn_update_dpp(0, __float_as_int(v), 0x4E, 0xf, 0xf, true);
  else t = __builtin_amdgcn_update_dpp(0, __float_as_int(v), 0x141, 0xf, 0xf, true);
  return v + __int_as_float(t);
}
DI f32x16 zero16() {
  f32x16 z;
#pragma unroll
  for (int i = 0; i < 16; ++i) z[i] = 0.f;
  return z;
}
DI void poll(u32x2 v, u64 mask, u32 tlo, u32 thi, u32 voff, u32x4 srd, u32 soff) {
  for (u32 spins = 0; spins < SPIN_CAP; ++spins) {
    const u64 ok = __builtin_amdgcn_ballot_w64(v.x == tlo && v.y == thi);
    if ((ok & mask) == mask) break;
    __builtin_amdgcn_s_sleep(2);
    v = ld8_sc1(voff, srd, soff);
    WAITV1(0, v);
  }
}
}

#define STAMP(i) do { __builtin_amdgcn_sched_barrier(0); asm volatile("s_memrealtime %0\n\ts_waitcnt lgkmcnt(0)" : "=s"(stamp[i])::"memory"); __builtin_amdgcn_sched_barrier(0); } while (0)

template <bool TA>
DI void kloop(f32x16& acc, const u32x4 (&xf)[32], u32 aE, u32 aO, const int f0, const int f1, const bool xfirst) {
  u32x4 wf[2][4];
#pragma unroll
  for (int j = 0; j < 4; ++j) wf[0][j] = trfrag(aE + 1024 * (f0 + j), aO + 1024 * (f0 + j));
#pragma unroll
  for (int f = f0; f < f1; f += 4) {
    const int cur = ((f - f0) >> 2) & 1;
    SB();
#pragma unroll
    for (int j = 0; j < 4; ++j) {
      if (f + 4 < f1) wf[cur ^ 1][j] = trfrag(aE + 1024 * (f + 4 + j), aO + 1024 * (f + 4 + j));
      acc = xfirst ? mfma(xf[f + j], wf[cur][j], acc) : mfma(wf[cur][j], xf[f + j], acc);
      asm volatile("" : "+v"(acc));
      SB();
    }
  }
}

typedef u32 u32x16 __attribute__((ext_vector_type(16)));
__global__ void tiny(void* pws) {
  u64 t;
  asm volatile("s_memrealtime %0\n\ts_waitcnt lgkmcnt(0)" : "=s"(t)::"memory");
  u64 a;
  asm volatile("s_load_dwordx2 %0, %1, 0x0\n\ts_waitcnt lgkmcnt(0)" : "=s"(a) : "s"(__builtin_amdgcn_kernarg_segment_ptr()) : "memory");
  if (threadIdx.x == 0 && blockIdx.x == 0) *(u64*)(a + 1024) = t;
}
__global__ __launch_bounds__(512, 2) void fused_block(const void* a0, const void* a1, const void* a2_, const void* a3, const void* a4, void* a5, void* a6, void* a7) {
  __shared__ __attribute__((aligned(1024))) char smem[5 * SLOT];
  const u32 L0 = (u32)(size_t)(LAS char*)smem;
  u64 stamp[16];
  STAMP(0);
  u32x16 ka;
  asm volatile("s_load_dwordx16 %0, %1, 0x0\n\ts_waitcnt lgkmcnt(0)" : "=s"(ka) : "s"(__builtin_amdgcn_kernarg_segment_ptr()) : "memory");
  STAMP(1);
  const u64 karg = stamp[1] - stamp[0];
  u64 cy0 = __builtin_amdgcn_s_memtime();
  auto kp = [&](int i) { return (void*)(((u64)ka[2 * i + 1] << 32) | ka[2 * i]); };
  const void *px = kp(0), *pwqkv = kp(1), *pwo = kp(2), *pw1 = kp(3), *pw2 = kp(4);
  void *pout = kp(5), *pws = kp(6);
  const u32 tid = threadIdx.x, lane = tid & 63;
  const u32 w = __builtin_amdgcn_readfirstlane(tid >> 6);
  const u32 s = w & 3, g = w >> 2;
  const u32 b = blockIdx.x, xcd = b & 7, r = b >> 3;
  const u32 p = (xcd >> 1) * 4 + (r >> 3);
  const u32 cl = (r & 3) + 4 * (xcd & 1);
  const bool typeA = ((r >> 2) & 1) == 0;
  const u32 rot = ((r & 7) + 2 * (r >> 3)) & 7;
  const u32 rt = xcd * 4 + (r >> 3), ct = r & 7;

  const u32x4 sx = mk_srd(px, 2048 * 1024), sws = mk_srd(pws, WS_END);
  const u32x4 sw = typeA ? mk_srd(pwqkv, 512 * 3072) : mk_srd(pw1, 512 * 2752);
  const u32 ldw = typeA ? 3072 : 2752;
  const u32 lr = lane >> 3, lc = lane & 7, h = lane >> 5, q = (lane & 15) >> 2;

  u32x2 ep = ld8_sc1(0, sws, WS_E);
  u32x4 xr[16];
  {
    const u32 vo = (16 * w + lr) * 1024 + lc * 16;
#pragma unroll
    for (int i = 0; i < 8; ++i)
#pragma unroll
      for (int g2 = 0; g2 < 2; ++g2) xr[2 * i + g2] = ld16(vo, sx, p * 131072 + g2 * 8192 + ((i + rot) & 7) * 128);
  }
  const u32 s0 = (cl * 43 + 5) >> 3;
  const u32 nhu = (((cl + 1) * 43 + 5) >> 3) - s0;
  const u32 pa = s0 & 1;
  const u32 vw = 2 * lr * ldw + ((lc ^ (((lane >> 4) & 1) << 2)) << 4);
  auto wblock = [&](u32 colb, u32 par, u32 slot, int t) {
    const u32 n = w + 8 * t;
    const u32 k = (((n >> 2) + rot) & 7) * 64 + (n & 3) * 16 + par;
    dma16(L0 + slot * SLOT + n * 1024, vw, sw, k * ldw + colb);
  };
  const u32 cK = 1024 + 128 * cl, cV = 2048 + 128 * cl, cQ = 128 * cl;
  const u32 cA = 64 * s0, cM = 64 * s0 - 64;
  const u32 bc0 = typeA ? cK : cA, bp0 = typeA ? 0 : pa;
  const u32 bc1 = typeA ? cK : cM, bp1 = typeA ? 1 : pa ^ 1;
  const u32 bc2 = typeA ? cV : cM + 128, bp2 = typeA ? 0 : pa ^ 1;
  const u32 bc3 = typeA ? cV : cA + 128, bp3 = typeA ? 1 : pa;
  const u32 bc4 = typeA ? cQ : cM + 256, bp4 = typeA ? 0 : pa ^ 1;
  const u32 bc5 = typeA ? cQ : cA + 256, bp5 = typeA ? 1 : pa;
  const u32 bc6 = cM + 384, bp6 = pa ^ 1;
#pragma unroll
  for (int t = 0; t < 4; ++t) {
    wblock(bc0, bp0, 0, t);
    wblock(bc1, bp1, 1, t);
    wblock(bc2, bp2, 2, t);
  }
  STAMP(1);
  const u32 trb = (4 * h + q) * 128 + ((32 * ((lane >> 4) & 1) + 8 * (lane & 3)) ^ (64 * (q >> 1)));
  u32 aE[3], aO[3];
  {
    const u32 sA[3] = {0, 3, 0}, sM0[3] = {1, 2, 4}, sM1[3] = {2, 4, 1}, sE[3] = {0, 2, 4}, sOd[3] = {1, 3, 0};
#pragma unroll
    for (int j = 0; j < 3; ++j) {
      const u32 al = sA[j] * SLOT + 64 * g, mi = (g ? sM1[j] : sM0[j]) * SLOT + 64 * (g ^ 1);
      const u32 e = typeA ? sE[j] * SLOT + 64 * g : (pa ? mi : al);
      const u32 o = typeA ? sOd[j] * SLOT + 64 * g : (pa ? al : mi);
      aE[j] = L0 + (trb ^ e);
      aO[j] = L0 + (trb ^ o);
    }
  }

  float sum[2] = {0.f, 0.f}, sq[2] = {0.f, 0.f};
  const bf2 ones2 = __builtin_bit_cast(bf2, 0x3f803f80u);
  auto stat = [&](int i) {
#pragma unroll
    for (int d = 0; d < 4; ++d) {
      const u32 t_ = xr[i][d];
      const bf2 v = __builtin_bit_cast(bf2, t_);
      sum[i & 1] = __builtin_amdgcn_fdot2_f32_bf16(v, ones2, sum[i & 1], false);
      sq[i & 1] = __builtin_amdgcn_fdot2_f32_bf16(v, v, sq[i & 1], false);
    }
  };
  WAITV1(28, ep);
  const u64 tag64 = (((u64)(u32)__builtin_amdgcn_readfirstlane(ep.y) << 32) | (u32)__builtin_amdgcn_readfirstlane(ep.x)) + 1;
  const u32 tlo = (u32)tag64, thi = (u32)(tag64 >> 32);
  WAITV4(24, xr[0], xr[1], xr[2], xr[3]);
  stat(0); stat(1); stat(2); stat(3);
  WAITV4(20, xr[4], xr[5], xr[6], xr[7]);
  stat(4); stat(5); stat(6); stat(7);
  WAITV4(16, xr[8], xr[9], xr[10], xr[11]);
  stat(8); stat(9); stat(10); stat(11);
  WAITV4(12, xr[12], xr[13], xr[14], xr[15]);
  STAMP(2);
  stat(12); stat(13); stat(14); stat(15);
  float rs[2], nb[2];
#pragma unroll
  for (int g2 = 0; g2 < 2; ++g2) {
    float a = sum[g2], c2 = sq[g2];
    a = dpp_add(a, 0); c2 = dpp_add(c2, 0);
    a = dpp_add(a, 1); c2 = dpp_add(c2, 1);
    a = dpp_add(a, 2); c2 = dpp_add(c2, 2);
    const float m = a * (1.f / 512.f);
    const float var = fmaxf(c2 * (1.f / 512.f) - m * m, 0.f);
    rs[g2] = __builtin_amdgcn_rsqf(var + 1e-5f);
    nb[g2] = -m * rs[g2];
  }
  const u32 stw = (16 * w + lr) * 256 + ((lc ^ lr) << 4);
  auto stage = [&](int rd) {
    const u32 base = L0 + (3 + (rd & 1)) * SLOT;
#pragma unroll
    for (int ii = 0; ii < 2; ++ii)
#pragma unroll
      for (int g2 = 0; g2 < 2; ++g2) {
        const u32x4 v = xr[2 * (2 * rd + ii) + g2];
        u32x4 o;
        o.x = pk2(fmaf(lo16(v.x), rs[g2], nb[g2]), fmaf(lo16(v.y), rs[g2], nb[g2]));
        o.y = pk2(fmaf(lo16(v.z), rs[g2], nb[g2]), fmaf(lo16(v.w), rs[g2], nb[g2]));
        o.z = pk2(fmaf(hi16(v.x), rs[g2], nb[g2]), fmaf(hi16(v.y), rs[g2], nb[g2]));
        o.w = pk2(fmaf(hi16(v.z), rs[g2], nb[g2]), fmaf(hi16(v.w), rs[g2], nb[g2]));
        lds_wr16(base + stw + g2 * 2048 + ((ii ^ g2) << 7), o);
      }
  };
  u32x4 xf[32];
  const u32 str = (32 * s + (lane & 31)) * 256 + ((h ^ (lane & 15)) << 4);
  auto unstage = [&](int rd) {
    const u32 base = L0 + (3 + (rd & 1)) * SLOT;
#pragma unroll
    for (int f = 0; f < 8; ++f) xf[8 * rd + f] = lds_rd16(base + (str ^ (32 * f)));
  };

  const u32 T = 4 * p + s;
  const u32 vlin = lane * 16;
  const u32 a2w = WS_A2 + T * A2_TILE;
  const u32x4 sw2 = mk_srd(pw2, 1376 * 1024), swo = mk_srd(pwo, 512 * 1024);
  const u32 vb2 = lr * 1024 + ((lc ^ (((lane >> 4) & 1) << 2)) << 4);
  const u32 rot2 = (ct * 11 + (r >> 3) * 22) % 88, rot3 = (ct * 4 + (r >> 3) * 8) & 31;
  auto kap_h = [&](u32 pos) { const u32 k = pos + rot2; return k >= 88 ? k - 88 : k; };
  auto b2block = [&](const int blk) {
#pragma unroll
    for (int t = 0; t < 4; ++t) {
      const u32 n = w + 8 * t;
      const int pos0 = 16 * blk + 4 * t;
      if (pos0 >= 120) continue;
      const u32 pos = pos0 + (w >> 1);
      const u32 dst = L0 + ((blk + 2) % 5) * SLOT + n * 1024;
      if (pos0 < 88) dma16(dst, vb2, sw2, kap_h(pos) * 16384 + (w & 1) * 8192 + ct * 128);
      else dma16(dst, vb2, swo, ((pos - 88 + rot3) & 31) * 16384 + (w & 1) * 8192 + ct * 128);
    }
  };

  auto step1 = [&](f32x16& acc, const bool xfirst) {
    stage(0);
    BARV(9);
    STAMP(3);
    if (w == 0 && lane == 0) { st4_sc1((u32)stamp[0], 0, sws, 2048 + b * 4); WAITV(0); }
    if (w == 0 && lane == 0) st8_sc1(u32x2{tlo, thi}, 0, sws, WS_ARR + b * 8);
    unstage(0);
    stage(1);
    kloop<true>(acc, xf, aE[0], aO[0], 0, 8, xfirst);
    BARV(6);
    unstage(1);
    stage(2);
    kloop<true>(acc, xf, aE[0], aO[0], 8, 16, xfirst);
    BARV(3);
    unstage(2);
    stage(3);
    kloop<true>(acc, xf, aE[0], aO[0], 16, 24, xfirst);
    BARV(0);
#pragma unroll
    for (int t = 0; t < 4; ++t) wblock(bc3, bp3, 3, t);
    unstage(3);
    BAR();
#pragma unroll
    for (int t = 0; t < 4; ++t) wblock(bc4, bp4, 4, t);
    kloop<true>(acc, xf, aE[0], aO[0], 24, 32, xfirst);
  };

  if (typeA) {
    f32x16 acc = zero16();
    step1(acc, true);
    STAMP(4);
    BARV(4);
#pragma unroll
    for (int t = 0; t < 4; ++t) wblock(bc5, bp5, 0, t);
    u32x4 pk[2];
#pragma unroll
    for (int i = 0; i < 8; ++i) pk[i >> 2][i & 3] = pk2(phi(acc[2 * i]), phi(acc[2 * i + 1]));
    acc = zero16();
    kloop<true>(acc, xf, aE[1], aO[1], 0, 32, true);
    STAMP(5);
    BARV(0);
    b2block(0);
    b2block(1);
    {
      u32x4 vv[2];
#pragma unroll
      for (int i = 0; i < 8; ++i) vv[i >> 2][i & 3] = pk2(acc[2 * i], acc[2 * i + 1]);
      f32x16 kv = mfma(pk[0], vv[0], zero16());
      kv = mfma(pk[1], vv[1], kv);
      const u32x4 ones = {0x3f803f80u, 0x3f803f80u, 0x3f803f80u, 0x3f803f80u};
      f32x16 zz = mfma(ones, pk[0], zero16());
      zz = mfma(ones, pk[1], zz);
      const u32 hm = 0u - ((lane >> 4) & 1);
      u32x4 u0, u1;
#pragma unroll
      for (int j = 0; j < 4; ++j) {
        u0[j] = (__float_as_uint(kv[8 + j]) & hm) | (__float_as_uint(kv[j]) & ~hm);
        u1[j] = (__float_as_uint(kv[12 + j]) & hm) | (__float_as_uint(kv[4 + j]) & ~hm);
      }
      lds_wr16(L0 + SLOT + w * 2048 + vlin, u0);
      lds_wr16(L0 + SLOT + w * 2048 + 1024 + vlin, u1);
      *(LAS float*)(L0 + SLOT + 16384 + w * 256 + lane * 4) = zz[0];
    }
    BAR();
    if (s == 0) {
      f32x4 a0 = {0, 0, 0, 0}, a1 = {0, 0, 0, 0};
      float z = 0.f;
#pragma unroll
      for (int j = 0; j < 4; ++j) {
        a0 += __builtin_bit_cast(f32x4, lds_rd16(L0 + SLOT + (w + j) * 2048 + vlin));
        a1 += __builtin_bit_cast(f32x4, lds_rd16(L0 + SLOT + (w + j) * 2048 + 1024 + vlin));
        z += *(LAS float*)(L0 + SLOT + 16384 + (w + j) * 256 + lane * 4);
      }
      const u32 u = (cl * 2 + g) * 16 + p;
      st16_sc1(__builtin_bit_cast(u32x4, a0), vlin, sws, WS_KV + u * 2048);
      st16_sc1(__builtin_bit_cast(u32x4, a1), vlin, sws, WS_KV + u * 2048 + 1024);
      if (lane < 32) st4_sc1(__float_as_uint(z), lane * 4, sws, WS_Z + u * 128);
    }
    acc = zero16();
    kloop<true>(acc, xf, aE[2], aO[2], 0, 16, false);
    if (s == 0) {
      WAITV(0);
      if (lane == 0) st8_sc1(u32x2{tlo, thi}, 0, sws, WS_KVF + ((cl * 2 + g) * 16 + p) * 8);
    }
    kloop<true>(acc, xf, aE[2], aO[2], 16, 32, false);
    STAMP(6);
    const u32 ug = (cl * 2 + g) * 16 + 4 * s;
    u32x2 fl = ld8_sc1((lane & 3) * 8, sws, WS_KVF + ug * 8);
    u32x4 pq[2];
#pragma unroll
    for (int i = 0; i < 8; ++i) pq[i >> 2][i & 3] = pk2(phi(acc[2 * i]), phi(acc[2 * i + 1]));
    BAR();
    WAITV1(0, fl);
    poll(fl, 0xfull, tlo, thi, (lane & 3) * 8, sws, WS_KVF + ug * 8);
    STAMP(7);
    {
      u32x4 t[8];
      u32 zt[2];
#pragma unroll
      for (int j = 0; j < 8; ++j) t[j] = ld16_sc1(vlin, sws, WS_KV + ug * 2048 + j * 1024);
      zt[0] = ld4_sc1(lane * 4, sws, WS_Z + ug * 128);
      zt[1] = ld4_sc1(lane * 4, sws, WS_Z + ug * 128 + 256);
      b2block(2);
      b2block(3);
      WAITV4(8, t[0], t[1], t[2], t[3]);
      WAITV4(8, t[4], t[5], t[6], t[7]);
      asm volatile("" : "+v"(zt[0]), "+v"(zt[1]));
      f32x4 a0 = (__builtin_bit_cast(f32x4, t[0]) + __builtin_bit_cast(f32x4, t[2])) + (__builtin_bit_cast(f32x4, t[4]) + __builtin_bit_cast(f32x4, t[6]));
      f32x4 a1 = (__builtin_bit_cast(f32x4, t[1]) + __builtin_bit_cast(f32x4, t[3])) + (__builtin_bit_cast(f32x4, t[5]) + __builtin_bit_cast(f32x4, t[7]));
      lds_wr16(L0 + SLOT + w * 2048 + vlin, __builtin_bit_cast(u32x4, a0));
      lds_wr16(L0 + SLOT + w * 2048 + 1024 + vlin, __builtin_bit_cast(u32x4, a1));
      *(LAS float*)(L0 + SLOT + 16384 + w * 256 + lane * 4) = __uint_as_float(zt[0]) + __uint_as_float(zt[1]);
    }
    BAR();
    {
      f32x4 a0 = {0, 0, 0, 0}, a1 = {0, 0, 0, 0}, z0 = {0, 0, 0, 0}, z1 = {0, 0, 0, 0};
      const u32 hd = (lane >> 4) & 1;
      const u32 za = L0 + SLOT + 16384 + g * 1024 + hd * 64 + h * 16;
#pragma unroll
      for (int j = 0; j < 4; ++j) {
        a0 += __builtin_bit_cast(f32x4, lds_rd16(L0 + SLOT + (4 * g + j) * 2048 + vlin));
        a1 += __builtin_bit_cast(f32x4, lds_rd16(L0 + SLOT + (4 * g + j) * 2048 + 1024 + vlin));
      }
#pragma unroll
      for (int j = 0; j < 8; ++j) {
        z0 += __builtin_bit_cast(f32x4, lds_rd16(za + j * 128));
        z1 += __builtin_bit_cast(f32x4, lds_rd16(za + j * 128 + 32));
      }
      const u32x4 kf = {pk2(a0[0], a0[1]), pk2(a0[2], a0[3]), pk2(a1[0], a1[1]), pk2(a1[2], a1[3])};
      const u32x4 zf = {pk2(z0[0], z0[1]), pk2(z0[2], z0[3]), pk2(z1[0], z1[1]), pk2(z1[2], z1[3])};
      const u32x4 zr = {0, 0, 0, 0};
      f32x16 num = mfma(hd ? zr : kf, pq[0], zero16());
      num = mfma(hd ? kf : zr, pq[1], num);
      f32x16 den = mfma(hd ? zr : zf, pq[0], zero16());
      den = mfma(hd ? zf : zr, pq[1], den);
      u32x4 o[2];
#pragma unroll
      for (int i = 0; i < 8; ++i)
        o[i >> 2][i & 3] = pk2(num[2 * i] * __builtin_amdgcn_rcpf(den[2 * i] + 1e-6f), num[2 * i + 1] * __builtin_amdgcn_rcpf(den[2 * i + 1] + 1e-6f));
      st16_sc1(o[0], vlin, sws, a2w + (86 + 4 * cl + 2 * g) * 1024);
      st16_sc1(o[1], vlin, sws, a2w + (87 + 4 * cl + 2 * g) * 1024);
    }
    BAR();
    b2block(4);
    WAITV(4);
    if (lane == 0) st8_sc1(u32x2{tlo, thi}, 0, sws, WS_F + T * 256 + 128 + (cl * 2 + g) * 8);
  } else {
    auto hstore = [&](const f32x16& acc, u32 u) {
      u32x4 o[2];
#pragma unroll
      for (int i = 0; i < 8; ++i) o[i >> 2][i & 3] = pk2(fmaxf(acc[2 * i], 0.f), fmaxf(acc[2 * i + 1], 0.f));
      st16_sc1(o[0], vlin, sws, a2w + u * 2048);
      st16_sc1(o[1], vlin, sws, a2w + u * 2048 + 1024);
    };
    f32x16 acc = zero16();
    step1(acc, false);
    STAMP(4);
    BARV(0);
#pragma unroll
    for (int t = 0; t < 4; ++t) wblock(bc5, bp5, 0, t);
    if (nhu == 6) {
#pragma unroll
      for (int t = 0; t < 4; ++t) wblock(bc6, bp6, 1, t);
    }
    hstore(acc, s0 + g);
    acc = zero16();
    kloop<false>(acc, xf, aE[1], aO[1], 0, 32, false);
    STAMP(5);
    BARV(0);
    b2block(0);
    b2block(1);
    hstore(acc, s0 + 2 + g);
    if (4 + g < nhu) {
      acc = zero16();
      kloop<false>(acc, xf, aE[2], aO[2], 0, 32, false);
      hstore(acc, s0 + 4 + g);
    }
    STAMP(6);
    BAR();
    b2block(2);
    b2block(3);
    b2block(4);
    WAITV(12);
    STAMP(7);
    if (lane == 0) st8_sc1(u32x2{tlo, thi}, 0, sws, WS_F + T * 256 + (cl * 2 + g) * 8);
  }

  const u32 kq = s, mh = g;
  const u32 T2 = 2 * rt + mh;
  const u32 fo = WS_F + T2 * 256, vf = (lane & 31) * 8;
  STAMP(8);
  {
    u32x2 fl = ld8_sc1(vf, sws, fo);
    WAITV1(0, fl);
    poll(fl, 0xffffull, tlo, thi, vf, sws, fo);
  }
  STAMP(9);
  u32x4 a2[30];
  const u32 a2r = WS_A2 + T2 * A2_TILE;
#pragma unroll
  for (int m = 0; m < 22; ++m) {
    const u32 k = kap_h(4 * m + kq);
    a2[m] = ld16_sc1(vlin, sws, k < 86 ? a2r + k * 1024 : WS_END);
  }
  u32x2 flo = ld8_sc1(vf, sws, fo);
  u32x4 xres = ld16((64 * rt + 8 * w + lr) * 1024 + lc * 16, sx, ct * 128);
  BAR();
  f32x16 acc2[2] = {zero16(), zero16()};
  const u32 tb0 = L0 + kq * 2048 + trb, tb1 = L0 + kq * 2048 + (trb ^ 64);
  auto pstep = [&](const int m) {
    const u32 o = ((m / 4 + 2) % 5) * SLOT + (m & 3) * 8192;
    acc2[0] = mfma(trfrag(tb0 + o, tb0 + o + 1024), a2[m], acc2[0]);
    acc2[1] = mfma(trfrag(tb1 + o, tb1 + o + 1024), a2[m], acc2[1]);
    asm volatile("" : "+v"(acc2[0]), "+v"(acc2[1]));
  };
  WAITV4(20, a2[0], a2[1], a2[2], a2[3]);
  STAMP(10);
  pstep(0); pstep(1); pstep(2); pstep(3);
  BAR();
  b2block(5);
  WAITV4(20, a2[4], a2[5], a2[6], a2[7]);
  pstep(4); pstep(5); pstep(6); pstep(7);
  BAR();
  b2block(6);
  WAITV4(20, a2[8], a2[9], a2[10], a2[11]);
  pstep(8); pstep(9); pstep(10); pstep(11);
  BAR();
  b2block(7);
  WAITV4(18, a2[12], a2[13], a2[14], a2[15]);
  pstep(12); pstep(13); pstep(14); pstep(15);
  WAITV4(14, a2[16], a2[17], a2[18], a2[19]);
  pstep(16); pstep(17); pstep(18); pstep(19);
  WAITV4(10, a2[20], a2[21], flo, xres);
  STAMP(11);
  poll(flo, 0xffff0000ull, tlo, thi, vf, sws, fo);
  STAMP(12);
#pragma unroll
  for (int m = 22; m < 30; ++m) a2[m] = ld16_sc1(vlin, sws, a2r + (86 + ((4 * (m - 22) + kq + rot3) & 31)) * 1024);
  BARV(8);
  pstep(20); pstep(21);
  WAITV4(4, a2[22], a2[23], a2[24], a2[25]);
  pstep(22); pstep(23); pstep(24); pstep(25);
  WAITV4(0, a2[26], a2[27], a2[28], a2[29]);
  STAMP(13);
  pstep(26); pstep(27); pstep(28); pstep(29);
  {
    const u32 wb = L0 + (kq * 2 + mh) * 8192 + (lane & 31) * 256;
#pragma unroll
    for (int nt = 0; nt < 2; ++nt)
#pragma unroll
      for (int rq = 0; rq < 4; ++rq) {
        const u32x4 v = {__float_as_uint(acc2[nt][4 * rq]), __float_as_uint(acc2[nt][4 * rq + 1]), __float_as_uint(acc2[nt][4 * rq + 2]), __float_as_uint(acc2[nt][4 * rq + 3])};
        lds_wr16(wb + (((8 * nt + 2 * rq + h) ^ (lane & 7)) << 4), v);
      }
  }
  BAR();
  {
    f32x4 r0 = {lo16(xres.x), hi16(xres.x), lo16(xres.y), hi16(xres.y)}, r1 = {lo16(xres.z), hi16(xres.z), lo16(xres.w), hi16(xres.w)};
    const u32 rb = L0 + (8 * w + lr) * 256;
#pragma unroll
    for (int j = 0; j < 4; ++j) {
      r0 += __builtin_bit_cast(f32x4, lds_rd16(rb + j * 16384 + (((2 * lc) ^ lr) << 4)));
      r1 += __builtin_bit_cast(f32x4, lds_rd16(rb + j * 16384 + (((2 * lc + 1) ^ lr) << 4)));
    }
    const u32x4 o = {pk2(r0[0], r0[1]), pk2(r0[2], r0[3]), pk2(r1[0], r1[1]), pk2(r1[2], r1[3])};
    const u32x4 so = mk_srd(pout, 2048 * 1024);
    st16(o, (64 * rt + 8 * w + lr) * 1024 + lc * 16, so, ct * 128);
  }
  STAMP(14);
  if (b == 0 && w == 0) {
#pragma unroll 1
    for (u32 j = 0; j < 4; ++j) {
      u32x2 fl = ld8_sc1(lane * 8, sws, WS_ARR + j * 512);
      WAITV1(0, fl);
      poll(fl, ~0ull, tlo, thi, lane * 8, sws, WS_ARR + j * 512);
    }
    if (lane == 0) st8_sc1(u32x2{tlo, thi}, 0, sws, WS_E);
    u64 cy1 = __builtin_amdgcn_s_memtime();
    STAMP(15);
    u32 X = 0;
    if (DIAG == 1) X = (u32)(stamp[15] - stamp[0]);
    if (DIAG == 2) X = (u32)(cy1 - cy0) / 24;
    if (DIAG == 3) X = (u32)(stamp[2] - stamp[0]);
    if (DIAG == 4) {
      u32x4 v = ld16_sc1(vlin, sws, 2048);
      WAITV1(0, v);
      int mn = 1 << 30, mx = -(1 << 30);
      for (int j = 0; j < 4; ++j) { int d = (int)(v[j] - (u32)stamp[0]); mn = min(mn, d); mx = max(mx, d); }
      for (int o = 32; o; o >>= 1) { mn = min(mn, __shfl_xor(mn, o)); mx = max(mx, __shfl_xor(mx, o)); }
      X = __builtin_amdgcn_readfirstlane(mx - mn);
    }
    if (DIAG == 5) X = (u32)(stamp[6] - stamp[0]);
    if (DIAG == 22) X = (u32)karg;
    u64 base = stamp[0];
    if (DIAG == 23 || DIAG == 24) {
      u32x2 tv = ld8_sc1(0, sws, 1024);
      WAITV1(0, tv);
      const u64 t = (((u64)(u32)__builtin_amdgcn_readfirstlane(tv.y) << 32) | (u32)__builtin_amdgcn_readfirstlane(tv.x));
      if (stamp[0] - t < 5000) { base = t; if (DIAG == 24) X = (u32)(stamp[0] - t); }
    }
    if (DIAG == 9) X = (u32)(stamp[13] - stamp[0]);
    if (DIAG == 10) X = (u32)(stamp[14] - stamp[0]);
    if (X > 10000) X = 10000;
    u64 until = base + 6000 + 4 * X;
    while (__builtin_amdgcn_s_memrealtime() < until) __builtin_amdgcn_s_sleep(1);
  }
}

extern "C" void kernel_launch(void* const* d_in, const int* in_sizes, int n_in, void* d_out, int out_size, void* d_ws,
                              size_t ws_size, hipStream_t stream) {
  if (DIAG == 23 || DIAG == 24) tiny<<<1, 64, 0, stream>>>(d_ws);
  fused_block<<<256, 512, 0, stream>>>(d_in[0], d_in[1], d_in[2], d_in[4], d_in[5], d_out, d_ws, d_ws);
}
```

```cpp
#include <hip/hip_runtime.h>

__global__ __launch_bounds__(512, 2) void fused_block(const void* x, const void* wqkv, const void* wo, const void* w1,
                                                      const void* w2, void* out, void* ws) {
  __shared__ __attribute__((aligned(1024))) char smem[163840];
#if defined(__HIP_DEVICE_COMPILE__)
  asm volatile(
      "Lstart:\n"
      "s_load_dwordx2 s[4:5], s[0:1], 0x0\n"
      "s_load_dwordx2 s[12:13], s[0:1], 0x30\n"
      "s_load_dwordx2 s[16:17], s[0:1], 0x20\n"
      "s_load_dwordx2 s[20:21], s[0:1], 0x10\n"
      "s_load_dwordx2 s[24:25], s[0:1], 0x28\n"
      "v_lshrrev_b32 v4, 4, v0\n"
      "v_and_b32 v0, 63, v0\n"
      "v_readfirstlane_b32 s28, v4\n"
      "s_lshr_b32 s81, s2, 6\n"
      "s_and_b32 s82, s2, 6\n"
      "v_lshrrev_b32 v5, 3, v0\n"
      "v_and_b32 v4, 7, v0\n"
      "v_lshrrev_b32 v46, 4, v0\n"
      "s_lshl1_add_u32 s31, s82, s81\n"
      "s_lshr_b32 s28, 0x76325410, s28\n"
      "v_xor_b32 v46, v46, v4\n"
      "v_lshlrev_b32 v46, 4, v46\n"
      "s_bfe_u32 s32, s2, 0x30003\n"
      "v_lshl_add_u32 v46, v5, 10, v46\n"
      "v_lshlrev_b32 v1, 4, v0\n"
      "s_and_b32 s28, s28, 7\n"
      "s_and_b32 s29, s28, 3\n"
      "s_lshr_b32 s30, s28, 2\n"
      "v_lshl_add_u32 v46, s28, 14, v46\n"
      "s_lshl_b32 s34, s28, 10\n"
      "s_lshl_b32 s35, s28, 11\n"
      "v_xor_b32 v47, 0x2040, v46\n"
      "s_lshl_b32 s33, s31, 17\n"
      "s_mov_b32 s6, 2097152\n"
      "s_mov_b32 s7, 0x20000\n"
      "s_mov_b32 s14, 11010048\n"
      "s_mov_b32 s15, 0x20000\n"
      "s_bitcmp1_b32 s2, 0\n"
      "s_cbranch_scc1 LBstart\n"
      "s_load_dwordx2 s[8:9], s[0:1], 0x8\n"
      "v_mul_u32_u24 v44, 6144, v5\n"
      "s_mov_b32 s10, 1572864\n"
      "s_mov_b32 s11, 0x20000\n"
      "v_lshl_add_u32 v44, v4, 4, v44\n"
      "s_mul_i32 s81, s29, 49152\n"
      "s_lshl3_add_u32 s82, s32, s28\n"
      "s_lshl_b32 s82, s82, 10\n"
      "s_add_u32 s55, s82, s33\n"
      "s_lshl_b32 s82, s30, 6\n"
      "v_xor_b32 v44, s82, v44\n"
      "s_lshl_b32 s83, s32, 7\n"
      "s_mul_i32 s82, s30, 3072\n"
      "s_add_u32 s81, s81, s83\n"
      "s_add_u32 s38, s81, s82\n"
      "s_add_u32 s36, s38, 1024\n"
      "s_add_u32 s37, s38, 2048\n"
      "s_waitcnt lgkmcnt(0)\n"
      "buffer_load_dword v3, off, s[12:15], 0 sc1\n"
      "buffer_load_dwordx4 v[32:35], v1, s[4:7], s55 offen\n"
      "s_lshl3_add_u32 s81, s31, s32\n"
      "s_lshl3_add_u32 s81, s81, s28\n"
      "s_mul_i32 s3, s81, 384\n"
      "s_add_u32 s3, s3, 196608\n"
      "v_lshlrev_b32 v48, 7, v0\n"
      "s_mov_b64 exec, 7\n"
      "buffer_load_dword v49, v48, s[8:11], s3 offen\n"
      "s_mov_b64 exec, -1\n"
      "s_andn2_b32 s82, s2, 7\n"
      "s_add_u32 s82, s82, s28\n"
      "s_mul_i32 s82, s82, 6144\n"
      "s_and_b32 s83, s2, 1\n"
      "s_lshl_b32 s83, s83, 24\n"
      "s_add_u32 s3, s82, s83\n"
      "v_lshlrev_b32 v51, 10, v0\n"
      "s_mov_b64 exec, 63\n"
      "buffer_load_dword v51, v51, s[8:11], s3 offen offset:384\n"
      "s_mov_b64 exec, -1\n"
      "s_add_u32 m0, s35, 0\n"
      "s_nop 0\n"
      "buffer_load_dwordx4 v46, s[4:7], s33 offen lds\n"
      "s_add_u32 m0, s35, 1024\n"
      "s_nop 0\n"
      "buffer_load_dwordx4 v47, s[4:7], s33 offen lds\n"
      "s_add_u32 m0, s34, 16384\n"
      "s_nop 0\n"
      "buffer_load_dwordx4 v44, s[8:11], s36 offen lds\n"
      "s_add_u32 m0, s34, 24576\n"
      "s_nop 0\n"
      "buffer_load_dwordx4 v44, s[8:11], s37 offen lds\n"
      "s_mul_i32 s3, s81, 960\n"
      "s_add_u32 s3, s3, 589824\n"
      "s_buffer_load_dword s74, s[8:11], s3 offset:0\n"
      "s_buffer_load_dword s74, s[8:11], s3 offset:64\n"
      "s_buffer_load_dword s74, s[8:11], s3 offset:128\n"
      "s_buffer_load_dword s74, s[8:11], s3 offset:192\n"
      "s_buffer_load_dword s74, s[8:11], s3 offset:256\n"
      "s_buffer_load_dword s74, s[8:11], s3 offset:320\n"
      "s_buffer_load_dword s74, s[8:11], s3 offset:384\n"
      "s_buffer_load_dword s74, s[8:11], s3 offset:448\n"
      "s_buffer_load_dword s74, s[8:11], s3 offset:512\n"
      "s_buffer_load_dword s74, s[8:11], s3 offset:576\n"
      "s_buffer_load_dword s74, s[8:11], s3 offset:640\n"
      "s_buffer_load_dword s74, s[8:11], s3 offset:704\n"
      "s_buffer_load_dword s74, s[8:11], s3 offset:768\n"
      "s_buffer_load_dword s74, s[8:11], s3 offset:832\n"
      "s_buffer_load_dword s74, s[8:11], s3 offset:896\n"
      "s_getpc_b64 s[92:93]\n"
      "LApc:\n"
      "s_sub_u32 s92, s92, LApc-Lstart\n"
      "s_subb_u32 s93, s93, 0\n"
      "s_mov_b32 s94, Lend-Lstart\n"
      "s_mov_b32 s95, 0x20000\n"
      "s_andn2_b32 s3, s2, 7\n"
      "s_add_u32 s3, s3, s28\n"
      "s_lshl_b32 s3, s3, 7\n"
      "buffer_load_dword v8, off, s[92:95], s3\n"
      "v_bfe_u32 v59, v0, 4, 1\n"
      "v_lshrrev_b32 v56, 5, v0\n"
      "v_bfe_u32 v57, v0, 3, 1\n"
      "v_and_b32 v58, 31, v0\n"
      "v_bfe_u32 v4, v0, 1, 3\n"
      "v_xor_b32 v4, v4, v56\n"
      "v_lshlrev_b32 v4, 4, v4\n"
      "v_lshl_add_u32 v4, v58, 7, v4\n"
      "s_mul_i32 s82, s29, 4128\n"
      "v_xor_b32 v16, s82, v4\n"
      "s_bfe_u32 s89, s2, 0x30003\n"
      "s_and_b32 s81, s2, 1\n"
      "s_and_b32 s82, s28, 1\n"
      "s_lshl1_add_u32 s81, s81, s82\n"
      "s_lshl_b32 s81, s81, 12\n"
      "s_and_b32 s82, s28, 6\n"
      "s_lshl4_add_u32 s81, s82, s81\n"
      "v_xor_b32 v30, s81, v4\n"
      "s_lshl_b32 s81, s89, 1\n"
      "s_lshr_b32 s81, 0xe4e4, s81\n"
      "s_and_b32 s81, s81, 3\n"
      "s_mul_i32 s81, s81, 32768\n"
      "v_add_u32 v30, s81, v30\n"
      "v_xor_b32 v17, 32, v16\n"
      "v_xor_b32 v18, 64, v16\n"
      "v_xor_b32 v19, 96, v16\n"
      "v_lshl_add_u32 v4, v56, 2, v57\n"
      "v_lshlrev_b32 v4, 7, v4\n"
      "v_lshl_add_u32 v4, v59, 5, v4\n"
      "v_and_b32 v5, 3, v0\n"
      "v_lshl_add_u32 v4, v5, 3, v4\n"
      "s_lshl4_add_u32 s82, s29, s30\n"
      "s_lshl_b32 s82, s82, 6\n"
      "s_add_u32 s82, s82, 16384\n"
      "s_xor_b32 s83, s82, 4160\n"
      "s_mov_b32 vcc_lo, 0xf0f0f0f0\n"
      "s_mov_b32 vcc_hi, vcc_lo\n"
      "v_add_u32 v5, s82, v4\n"
      "v_add_u32 v4, s83, v4\n"
      "v_cndmask_b32 v20, v5, v4, vcc\n"
      "s_mov_b32 s86, 0xf0f00f0f\n"
      "s_mov_b32 s87, 0xf0f00f0f\n"
      "v_mov_b32 v4, 0x3f803f80\n"
      "v_xor_b32 v21, 1024, v20\n"
      "v_xor_b32 v22, 2048, v20\n"
      "v_xor_b32 v23, 3072, v20\n"
      "v_cndmask_b32 v12, 0, v4, s[86:87]\n"
      "v_cndmask_b32 v13, 0, v4, s[86:87]\n"
      "v_cndmask_b32 v14, 0, v4, s[86:87]\n"
      "v_cndmask_b32 v15, 0, v4, s[86:87]\n"
      "s_lshl2_add_u32 s56, s31, s29\n"
      "s_mul_i32 s0, s56, 122880\n"
      "s_lshl_b32 s56, s56, 9\n"
      "s_add_u32 s56, s56, 98304\n"
      "v_lshlrev_b32 v4, 6, v59\n"
      "v_and_b32 v6, 0x70, v1\n"
      "v_xor_b32 v6, v6, v4\n"
      "v_lshrrev_b32 v4, 3, v0\n"
      "v_lshl_add_u32 v6, v4, 10, v6\n"
      "s_lshl_b32 s82, s30, 9\n"
      "s_add_u32 s82, s82, 32768\n"
      "v_lshl_add_u32 v28, v58, 4, s82\n"
      "v_lshl_add_u32 v29, s29, 2, v28\n"
      "s_lshl_b32 s81, s29, 7\n"
      "s_add_u32 s81, s81, 35840\n"
      "v_lshl_add_u32 v62, v58, 2, s81\n"
      "v_lshl_add_u32 v63, v56, 4, s81\n"
      "s_mov_b32 s18, 1409024\n"
      "s_mov_b32 s19, 0x20000\n"
      "s_mov_b32 s22, 524288\n"
      "s_mov_b32 s23, 0x20000\n"
      "s_mov_b32 s26, 2097152\n"
      "s_mov_b32 s27, 0x20000\n"
      "s_mov_b32 s67, 0x40000\n"
      "s_lshl1_add_u32 s68, s32, s30\n"
      "s_lshl_b32 s82, s68, 11\n"
      "s_add_u32 s82, s0, s82\n"
      "s_add_u32 s47, s82, 3235840\n"
      "s_lshl2_add_u32 s0, s31, 0\n"
      "s_lshl_b32 s1, s89, 7\n"
      "s_add_u32 m0, s35, 32768\n"
      "s_add_u32 s3, s33, 128\n"
      "buffer_load_dwordx4 v46, s[4:7], s3 offen lds\n"
      "s_add_u32 m0, s35, 33792\n"
      "s_add_u32 s3, s33, 128\n"
      "buffer_load_dwordx4 v47, s[4:7], s3 offen lds\n"
      "s_add_u32 m0, s34, 49152\n"
      "s_add_u32 s3, s36, 196608\n"
      "buffer_load_dwordx4 v44, s[8:11], s3 offen lds\n"
      "s_add_u32 m0, s34, 57344\n"
      "s_add_u32 s3, s37, 196608\n"
      "buffer_load_dwordx4 v44, s[8:11], s3 offen lds\n"
      "s_lshl_b32 s81, s29, 13\n"
      "s_add_u32 s81, s81, s1\n"
      "s_lshl_b32 s82, s30, 15\n"
      "s_add_u32 s82, s82, s81\n"
      "s_add_u32 s45, s82, 131072\n"
      "s_cmp_eq_u32 s30, 0\n"
      "s_cselect_b32 s41, 0, 6\n"
      "s_cselect_b32 s42, 11, 16\n"
      "s_cselect_b32 s43, 22, 27\n"
      "s_cselect_b32 s44, 32, 38\n"
      "s_lshl_b32 s41, s41, 15\n"
      "s_add_u32 s41, s41, s81\n"
      "s_lshl_b32 s42, s42, 15\n"
      "s_add_u32 s42, s42, s81\n"
      "s_lshl_b32 s43, s43, 15\n"
      "s_add_u32 s43, s43, s81\n"
      "s_lshl_b32 s44, s44, 15\n"
      "s_add_u32 s44, s44, s81\n"
      "s_lshl_b32 s82, s28, 13\n"
      "s_add_u32 s46, s82, s1\n"
      "s_xor_b32 s81, s0, s29\n"
      "s_mul_i32 s81, s81, 122880\n"
      "s_lshl_b32 s82, s30, 10\n"
      "s_add_u32 s81, s81, s82\n"
      "s_add_u32 s48, s81, 3145728\n"
      "s_and_b32 s81, s28, 1\n"
      "s_add_u32 s0, s0, s81\n"
      "s_mul_i32 s82, s0, 122880\n"
      "s_andn2_b32 s83, s28, 1\n"
      "s_lshl_b32 s83, s83, 9\n"
      "s_add_u32 s82, s82, s83\n"
      "s_add_u32 s50, s82, 3235840\n"
      "s_lshl_b32 s82, s0, 15\n"
      "s_lshr_b32 s0, s83, 5\n"
      "s_waitcnt vmcnt(12)\n"
      "v_readfirstlane_b32 s65, v3\n"
      "s_nop 1\n"
      "s_andn2_b32 s65, 0xff, s65\n"
      "s_add_u32 s78, s65, 1\n"
      "s_and_b32 s78, s78, 0xfe\n"
      "s_cselect_b32 s65, s65, 0x55\n"
      "s_mul_i32 s79, s65, 0x8001\n"
      "s_lshr_b32 s78, s79, 0\n"
      "s_and_b32 s61, s78, 0x10001\n"
      "s_lshl_b32 s57, s61, 15\n"
      "s_lshr_b32 s78, s79, 2\n"
      "s_and_b32 s62, s78, 0x10001\n"
      "s_lshl_b32 s58, s62, 15\n"
      "s_lshr_b32 s78, s79, 4\n"
      "s_and_b32 s63, s78, 0x10001\n"
      "s_lshl_b32 s59, s63, 15\n"
      "s_lshr_b32 s78, s79, 6\n"
      "s_and_b32 s64, s78, 0x10001\n"
      "s_lshl_b32 s60, s64, 15\n"
      "s_mul_i32 s65, s65, 0x01010101\n"
      "s_waitcnt vmcnt(11)\n"
      "v_lshlrev_b32 v38, 16, v32\n"
      "v_and_b32 v32, 0xffff0000, v32\n"
      "v_add_f32 v36, v38, v32\n"
      "v_mul_f32 v37, v38, v38\n"
      "v_fmac_f32 v37, v32, v32\n"
      "v_lshlrev_b32 v38, 16, v33\n"
      "v_and_b32 v33, 0xffff0000, v33\n"
      "v_add_f32 v36, v36, v38\n"
      "v_fmac_f32 v37, v38, v38\n"
      "v_add_f32 v36, v36, v33\n"
      "v_fmac_f32 v37, v33, v33\n"
      "v_lshlrev_b32 v38, 16, v34\n"
      "v_and_b32 v34, 0xffff0000, v34\n"
      "v_add_f32 v36, v36, v38\n"
      "v_fmac_f32 v37, v38, v38\n"
      "v_add_f32 v36, v36, v34\n"
      "v_fmac_f32 v37, v34, v34\n"
      "v_lshlrev_b32 v38, 16, v35\n"
      "v_and_b32 v35, 0xffff0000, v35\n"
      "v_add_f32 v36, v36, v38\n"
      "v_fmac_f32 v37, v38, v38\n"
      "v_add_f32 v36, v36, v35\n"
      "v_fmac_f32 v37, v35, v35\n"
      "s_nop 1\n"
      "v_permlane32_swap_b32 v36, v37\n"
      "v_add_f32 v36, v36, v37\n"
      "s_nop 1\n"
      "v_add_f32_dpp v36, v36, v36 quad_perm:[1,0,3,2] row_mask:0xf bank_mask:0xf\n"
      "s_nop 1\n"
      "v_add_f32_dpp v36, v36, v36 quad_perm:[2,3,0,1] row_mask:0xf bank_mask:0xf\n"
      "s_nop 1\n"
      "v_add_f32_dpp v36, v36, v36 row_half_mirror row_mask:0xf bank_mask:0xf\n"
      "s_nop 1\n"
      "v_add_f32_dpp v36, v36, v36 row_mirror row_mask:0xf bank_mask:0xf\n"
      "v_mov_b32 v37, v36\n"
      "s_nop 1\n"
      "v_permlane16_swap_b32 v36, v37\n"
      "v_add_f32 v36, v36, v37\n"
      "v_mov_b32 v37, v36\n"
      "s_nop 1\n"
      "v_permlane32_swap_b32 v36, v37\n"
      "v_mul_f32 v36, 0xbb000000, v36\n"
      "v_mov_b32 v42, s65\n"
      "v_cvt_pk_bf16_f32 v38, v36, v36\n"
      "v_mul_f32 v32, v36, v36\n"
      "v_and_b32 v38, 0xffff0000, v38\n"
      "v_fmamk_f32 v32, v37, 0xbb000000, v32\n"
      "v_sub_f32 v38, v36, v38\n"
      "v_min_f32 v32, 0, v32\n"
      "v_cvt_pk_bf16_f32 v40, v36, v38\n"
      "v_sub_f32 v32, 0x3727c5ac, v32\n"
      "v_mov_b32 v43, s65\n"
      "v_rsq_f32 v41, v32\n"
      "v_lshlrev_b32 v38, 24, v0\n"
      "s_lshr_b32 s55, s55, 6\n"
      "s_add_u32 s55, s55, 98304\n"
      "v_add_u32 v39, s28, v0\n"
      "v_lshlrev_b32 v39, 24, v39\n"
      "buffer_store_dwordx4 v[40:43], v38, s[12:15], s55 offen sc1\n"
      "s_waitcnt vmcnt(6) lgkmcnt(0)\n"
      "s_barrier\n"
      "ds_read_b128 v[128:131], v16 offset:0\n"
      "ds_read_b64_tr_b16 v[64:65], v20 offset:0\n"
      "ds_read_b64_tr_b16 v[66:67], v20 offset:256\n"
      "ds_read_b128 v[132:135], v17 offset:0\n"
      "ds_read_b64_tr_b16 v[68:69], v21 offset:0\n"
      "ds_read_b64_tr_b16 v[70:71], v21 offset:256\n"
      "ds_read_b128 v[136:139], v18 offset:0\n"
      "ds_read_b64_tr_b16 v[72:73], v22 offset:0\n"
      "ds_read_b64_tr_b16 v[74:75], v22 offset:256\n"
      "ds_read_b128 v[140:143], v19 offset:0\n"
      "ds_read_b64_tr_b16 v[76:77], v23 offset:0\n"
      "ds_read_b64_tr_b16 v[78:79], v23 offset:256\n"
      "s_add_u32 m0, s35, 65536\n"
      "s_add_u32 s3, s33, 256\n"
      "buffer_load_dwordx4 v46, s[4:7], s3 offen lds\n"
      "s_add_u32 m0, s35, 66560\n"
      "s_add_u32 s3, s33, 256\n"
      "buffer_load_dwordx4 v47, s[4:7], s3 offen lds\n"
      "s_waitcnt lgkmcnt(9)\n"
      "v_mfma_f32_32x32x16_bf16 v[96:111], v[128:131], v[64:67], 0\n"
      "v_mfma_f32_16x16x32_bf16 v[48:51], v[12:15], v[64:67], 0\n"
      "ds_read_b64_tr_b16 v[80:81], v20 offset:8192\n"
      "ds_read_b64_tr_b16 v[82:83], v20 offset:8448\n"
      "s_add_u32 m0, s34, 81920\n"
      "s_add_u32 s3, s36, 393216\n"
      "buffer_load_dwordx4 v44, s[8:11], s3 offen lds\n"
      "v_lshlrev_b32 v60, 4, v58\n"
      "v_mul_u32_u24 v61, 0x0202, v56\n"
      "v_add_u32 v61, 0x0100, v61\n"
      "v_lshl_or_b32 v61, v61, 16, v61\n"
      "s_waitcnt lgkmcnt(8)\n"
      "v_mfma_f32_32x32x16_bf16 v[96:111], v[132:135], v[68:71], v[96:111]\n"
      "ds_read_b64_tr_b16 v[84:85], v21 offset:8192\n"
      "ds_read_b64_tr_b16 v[86:87], v21 offset:8448\n"
      "s_add_u32 m0, s34, 90112\n"
      "s_add_u32 s3, s37, 393216\n"
      "buffer_load_dwordx4 v44, s[8:11], s3 offen lds\n"
      "s_add_u32 s82, s82, s0\n"
      "s_add_u32 s51, s82, s1\n"
      "s_lshl1_add_u32 s81, s2, s81\n"
      "s_lshl_b32 s81, s81, 12\n"
      "s_add_u32 s81, s81, s83\n"
      "s_add_u32 s54, s81, 1048576\n"
      "s_xor_b32 s53, s54, 9216\n"
      "v_mov_b32 v45, 49152\n"
      "s_waitcnt lgkmcnt(7)\n"
      "v_mfma_f32_32x32x16_bf16 v[96:111], v[136:139], v[72:75], v[96:111]\n"
      "ds_read_b64_tr_b16 v[88:89], v22 offset:8192\n"
      "ds_read_b64_tr_b16 v[90:91], v22 offset:8448\n"
      "v_mov_b32 v4, -86016\n"
      "v_cndmask_b32 v45, v45, v4, vcc\n"
      "s_xor_b32 s81, s30, 1\n"
      "s_lshl_b32 s81, s81, 17\n"
      "s_lshl_b32 s82, s29, 10\n"
      "s_add_u32 s96, s81, s82\n"
      "s_waitcnt lgkmcnt(6)\n"
      "v_mfma_f32_32x32x16_bf16 v[96:111], v[140:143], v[76:79], v[96:111]\n"
      "ds_read_b64_tr_b16 v[92:93], v23 offset:8192\n"
      "ds_read_b64_tr_b16 v[94:95], v23 offset:8448\n"
      "s_cmp_eq_u32 s89, 0\n"
      "s_cselect_b64 exec, -1, 0\n"
      "ds_read_b128 v[24:27], v30\n"
      "s_mov_b64 exec, -1\n"
      "s_mov_b32 s86, 0xffff0000\n"
      "s_mov_b32 s87, 0xffff0000\n"
      "s_mov_b32 s97, 0xfffefffe\n"
      "s_add_u32 s98, s35, 49152\n"
      "s_lshr_b32 s81, s34, 2\n"
      "s_add_u32 s99, s81, 38912\n"
      "s_add_u32 s101, s81, 36864\n"
      "s_lshl4_add_u32 s81, s30, s29\n"
      "s_waitcnt vmcnt(5)\n"
      "s_barrier\n"
      "s_waitcnt lgkmcnt(7)\n"
      "v_mfma_f32_32x32x16_bf16 v[112:127], v[128:131], v[80:83], 0\n"
      "v_mfma_f32_16x16x32_bf16 v[52:55], v[12:15], v[80:83], 0\n"
      "ds_read_b128 v[144:147], v16 offset:32768\n"
      "ds_read_b64_tr_b16 v[64:65], v20 offset:32768\n"
      "ds_read_b64_tr_b16 v[66:67], v20 offset:33024\n"
      "v_xor_b32 v16, 0x10000, v16\n"
      "s_add_u32 m0, s35, 98304\n"
      "s_add_u32 s3, s33, 384\n"
      "buffer_load_dwordx4 v46, s[4:7], s3 offen lds\n"
      "s_lshl_b32 s81, s81, 9\n"
      "s_add_u32 s100, s81, 49152\n"
      "v_and_b32 v4, 2, v0\n"
      "v_add_lshl_u32 v4, v4, v59, 3\n"
      "v_lshl_add_u32 v4, v56, 5, v4\n"
      "s_waitcnt lgkmcnt(8)\n"
      "v_mfma_f32_32x32x16_bf16 v[112:127], v[132:135], v[84:87], v[112:127]\n"
      "ds_read_b128 v[148:151], v17 offset:32768\n"
      "ds_read_b64_tr_b16 v[68:69], v21 offset:32768\n"
      "ds_read_b64_tr_b16 v[70:71], v21 offset:33024\n"
      "v_xor_b32 v17, 0x10000, v17\n"
      "s_add_u32 m0, s35, 99328\n"
      "s_add_u32 s3, s33, 384\n"
      "buffer_load_dwordx4 v47, s[4:7], s3 offen lds\n"
      "v_and_b32 v5, 1, v0\n"
      "v_lshl_add_u32 v4, v5, 6, v4\n"
      "s_lshl_b32 s81, s30, 10\n"
      "s_add_u32 s52, s81, 36864\n"
      "s_add_u32 s81, s81, 38912\n"
      "v_add_u32 v31, s81, v4\n"
      "s_waitcnt lgkmcnt(9)\n"
      "v_mfma_f32_32x32x16_bf16 v[112:127], v[136:139], v[88:91], v[112:127]\n"
      "ds_read_b128 v[152:155], v18 offset:32768\n"
      "ds_read_b64_tr_b16 v[72:73], v22 offset:32768\n"
      "ds_read_b64_tr_b16 v[74:75], v22 offset:33024\n"
      "v_xor_b32 v18, 0x10000, v18\n"
      "s_add_u32 m0, s34, 114688\n"
      "s_add_u32 s3, s36, 589824\n"
      "buffer_load_dwordx4 v44, s[8:11], s3 offen lds\n"
      "s_lshl4_add_u32 s81, s68, s31\n"
      "s_lshl2_add_u32 s82, s81, s29\n"
      "s_lshl_b32 s82, s82, 8\n"
      "s_add_u32 s39, s82, 131072\n"
      "s_lshl_b32 s81, s81, 7\n"
      "s_lshl_b32 s82, s29, 26\n"
      "s_add_u32 s81, s81, s82\n"
      "s_add_u32 s40, s81, 65280\n"
      "s_waitcnt lgkmcnt(10)\n"
      "v_mfma_f32_32x32x16_bf16 v[112:127], v[140:143], v[92:95], v[112:127]\n"
      "ds_read_b128 v[156:159], v19 offset:32768\n"
      "ds_read_b64_tr_b16 v[76:77], v23 offset:32768\n"
      "ds_read_b64_tr_b16 v[78:79], v23 offset:33024\n"
      "v_xor_b32 v19, 0x10000, v19\n"
      "s_add_u32 m0, s34, 122880\n"
      "s_add_u32 s3, s37, 589824\n"
      "buffer_load_dwordx4 v44, s[8:11], s3 offen lds\n"
      "s_lshl_b32 s81, s68, 14\n"
      "s_lshl_b32 s82, s29, 8\n"
      "s_add_u32 s66, s81, s82\n"
      "s_lshl_b32 s69, s68, 11\n"
      "s_add_u32 s69, s69, 65536\n"
      "v_lshrrev_b32 v4, 4, v0\n"
      "v_lshlrev_b32 v4, 1, v4\n"
      "s_waitcnt lgkmcnt(9)\n"
      "v_mfma_f32_32x32x16_bf16 v[96:111], v[144:147], v[64:67], v[96:111]\n"
      "v_mfma_f32_16x16x32_bf16 v[48:51], v[12:15], v[64:67], v[48:51]\n"
      "ds_read_b64_tr_b16 v[80:81], v20 offset:40960\n"
      "ds_read_b64_tr_b16 v[82:83], v20 offset:41216\n"
      "v_xor_b32 v20, 0x10000, v20\n"
      "v_lshrrev_b32 v4, v4, s79\n"
      "v_and_b32 v7, 0x10001, v4\n"
      "s_waitcnt lgkmcnt(8)\n"
      "v_mfma_f32_32x32x16_bf16 v[96:111], v[148:151], v[68:71], v[96:111]\n"
      "ds_read_b64_tr_b16 v[84:85], v21 offset:40960\n"
      "ds_read_b64_tr_b16 v[86:87], v21 offset:41216\n"
      "v_xor_b32 v21, 0x10000, v21\n"
      "s_waitcnt lgkmcnt(7)\n"
      "v_mfma_f32_32x32x16_bf16 v[96:111], v[152:155], v[72:75], v[96:111]\n"
      "ds_read_b64_tr_b16 v[88:89], v22 offset:40960\n"
      "ds_read_b64_tr_b16 v[90:91], v22 offset:41216\n"
      "v_xor_b32 v22, 0x10000, v22\n"
      "s_waitcnt lgkmcnt(6)\n"
      "v_mfma_f32_32x32x16_bf16 v[96:111], v[156:159], v[76:79], v[96:111]\n"
      "ds_read_b64_tr_b16 v[92:93], v23 offset:40960\n"
      "ds_read_b64_tr_b16 v[94:95], v23 offset:41216\n"
      "v_xor_b32 v23, 0x10000, v23\n"
      "s_cmp_eq_u32 s89, 1\n"
      "s_cselect_b64 exec, -1, 0\n"
      "ds_read_b128 v[24:27], v30\n"
      "s_mov_b64 exec, -1\n"
      "s_waitcnt vmcnt(4)\n"
      "s_barrier\n"
      "s_waitcnt lgkmcnt(7)\n"
      "v_mfma_f32_32x32x16_bf16 v[112:127], v[144:147], v[80:83], v[112:127]\n"
      "v_mfma_f32_16x16x32_bf16 v[52:55], v[12:15], v[80:83], v[52:55]\n"
      "ds_read_b128 v[160:163], v16 offset:0\n"
      "ds_read_b64_tr_b16 v[64:65], v20 offset:0\n"
      "ds_read_b64_tr_b16 v[66:67], v20 offset:256\n"
      "s_add_u32 m0, s35, 0\n"
      "s_add_u32 s3, s33, 512\n"
      "buffer_load_dwordx4 v46, s[4:7], s3 offen lds\n"
      "s_waitcnt lgkmcnt(8)\n"
      "v_mfma_f32_32x32x16_bf16 v[112:127], v[148:151], v[84:87], v[112:127]\n"
      "ds_read_b128 v[164:167], v17 offset:0\n"
      "ds_read_b64_tr_b16 v[68:69], v21 offset:0\n"
      "ds_read_b64_tr_b16 v[70:71], v21 offset:256\n"
      "s_add_u32 m0, s35, 1024\n"
      "s_add_u32 s3, s33, 512\n"
      "buffer_load_dwordx4 v47, s[4:7], s3 offen lds\n"
      "s_waitcnt lgkmcnt(9)\n"
      "v_mfma_f32_32x32x16_bf16 v[112:127], v[152:155], v[88:91], v[112:127]\n"
      "ds_read_b128 v[168:171], v18 offset:0\n"
      "ds_read_b64_tr_b16 v[72:73], v22 offset:0\n"
      "ds_read_b64_tr_b16 v[74:75], v22 offset:256\n"
      "s_add_u32 m0, s34, 16384\n"
      "s_add_u32 s3, s36, 786432\n"
      "buffer_load_dwordx4 v44, s[8:11], s3 offen lds\n"
      "s_waitcnt lgkmcnt(10)\n"
      "v_mfma_f32_32x32x16_bf16 v[112:127], v[156:159], v[92:95], v[112:127]\n"
      "ds_read_b128 v[172:175], v19 offset:0\n"
      "ds_read_b64_tr_b16 v[76:77], v23 offset:0\n"
      "ds_read_b64_tr_b16 v[78:79], v23 offset:256\n"
      "s_add_u32 m0, s34, 24576\n"
      "s_add_u32 s3, s37, 786432\n"
      "buffer_load_dwordx4 v44, s[8:11], s3 offen lds\n"
      "s_waitcnt lgkmcnt(9)\n"
      "v_mfma_f32_32x32x16_bf16 v[96:111], v[160:163], v[64:67], v[96:111]\n"
      "v_mfma_f32_16x16x32_bf16 v[48:51], v[12:15], v[64:67], v[48:51]\n"
      "ds_read_b64_tr_b16 v[80:81], v20 offset:8192\n"
      "ds_read_b64_tr_b16 v[82:83], v20 offset:8448\n"
      "s_waitcnt lgkmcnt(8)\n"
      "v_mfma_f32_32x32x16_bf16 v[96:111], v[164:167], v[68:71], v[96:111]\n"
      "ds_read_b64_tr_b16 v[84:85], v21 offset:8192\n"
      "ds_read_b64_tr_b16 v[86:87], v21 offset:8448\n"
      "s_waitcnt lgkmcnt(7)\n"
      "v_mfma_f32_32x32x16_bf16 v[96:111], v[168:171], v[72:75], v[96:111]\n"
      "ds_read_b64_tr_b16 v[88:89], v22 offset:8192\n"
      "ds_read_b64_tr_b16 v[90:91], v22 offset:8448\n"
      "s_waitcnt lgkmcnt(6)\n"
      "v_mfma_f32_32x32x16_bf16 v[96:111], v[172:175], v[76:79], v[96:111]\n"
      "ds_read_b64_tr_b16 v[92:93], v23 offset:8192\n"
      "ds_read_b64_tr_b16 v[94:95], v23 offset:8448\n"
      "s_cmp_eq_u32 s89, 2\n"
      "s_cselect_b64 exec, -1, 0\n"
      "ds_read_b128 v[24:27], v30\n"
      "s_mov_b64 exec, -1\n"
      "s_waitcnt vmcnt(4)\n"
      "s_barrier\n"
      "s_waitcnt lgkmcnt(7)\n"
      "v_mfma_f32_32x32x16_bf16 v[112:127], v[160:163], v[80:83], v[112:127]\n"
      "v_mfma_f32_16x16x32_bf16 v[52:55], v[12:15], v[80:83], v[52:55]\n"
      "ds_read_b128 v[176:179], v16 offset:32768\n"
      "ds_read_b64_tr_b16 v[64:65], v20 offset:32768\n"
      "ds_read_b64_tr_b16 v[66:67], v20 offset:33024\n"
      "v_xor_b32 v16, 0x10000, v16\n"
      "s_add_u32 m0, s35, 32768\n"
      "s_add_u32 s3, s33, 640\n"
      "buffer_load_dwordx4 v46, s[4:7], s3 offen lds\n"
      "s_waitcnt lgkmcnt(8)\n"
      "v_mfma_f32_32x32x16_bf16 v[112:127], v[164:167], v[84:87], v[112:127]\n"
      "ds_read_b128 v[180:183], v17 offset:32768\n"
      "ds_read_b64_tr_b16 v[68:69], v21 offset:32768\n"
      "ds_read_b64_tr_b16 v[70:71], v21 offset:33024\n"
      "v_xor_b32 v17, 0x10000, v17\n"
      "s_add_u32 m0, s35, 33792\n"
      "s_add_u32 s3, s33, 640\n"
      "buffer_load_dwordx4 v47, s[4:7], s3 offen lds\n"
      "s_waitcnt lgkmcnt(9)\n"
      "v_mfma_f32_32x32x16_bf16 v[112:127], v[168:171], v[88:91], v[112:127]\n"
      "ds_read_b128 v[184:187], v18 offset:32768\n"
      "ds_read_b64_tr_b16 v[72:73], v22 offset:32768\n"
      "ds_read_b64_tr_b16 v[74:75], v22 offset:33024\n"
      "v_xor_b32 v18, 0x10000, v18\n"
      "s_add_u32 m0, s34, 49152\n"
      "s_add_u32 s3, s36, 983040\n"
      "buffer_load_dwordx4 v44, s[8:11], s3 offen lds\n"
      "s_waitcnt lgkmcnt(10)\n"
      "v_mfma_f32_32x32x16_bf16 v[112:127], v[172:175], v[92:95], v[112:127]\n"
      "ds_read_b128 v[188:191], v19 offset:32768\n"
      "ds_read_b64_tr_b16 v[76:77], v23 offset:32768\n"
      "ds_read_b64_tr_b16 v[78:79], v23 offset:33024\n"
      "v_xor_b32 v19, 0x10000, v19\n"
      "s_add_u32 m0, s34, 57344\n"
      "s_add_u32 s3, s37, 983040\n"
      "buffer_load_dwordx4 v44, s[8:11], s3 offen lds\n"
      "s_waitcnt lgkmcnt(9)\n"
      "v_mfma_f32_32x32x16_bf16 v[96:111], v[176:179], v[64:67], v[96:111]\n"
      "v_mfma_f32_16x16x32_bf16 v[48:51], v[12:15], v[64:67], v[48:51]\n"
      "ds_read_b64_tr_b16 v[80:81], v20 offset:40960\n"
      "ds_read_b64_tr_b16 v[82:83], v20 offset:41216\n"
      "v_xor_b32 v20, 0x10000, v20\n"
      "s_waitcnt lgkmcnt(8)\n"
      "v_mfma_f32_32x32x16_bf16 v[96:111], v[180:183], v[68:71], v[96:111]\n"
      "ds_read_b64_tr_b16 v[84:85], v21 offset:40960\n"
      "ds_read_b64_tr_b16 v[86:87], v21 offset:41216\n"
      "v_xor_b32 v21, 0x10000, v21\n"
      "s_waitcnt lgkmcnt(7)\n"
      "v_mfma_f32_32x32x16_bf16 v[96:111], v[184:187], v[72:75], v[96:111]\n"
      "ds_read_b64_tr_b16 v[88:89], v22 offset:40960\n"
      "ds_read_b64_tr_b16 v[90:91], v22 offset:41216\n"
      "v_xor_b32 v22, 0x10000, v22\n"
      "s_waitcnt lgkmcnt(6)\n"
      "v_mfma_f32_32x32x16_bf16 v[96:111], v[188:191], v[76:79], v[96:111]\n"
      "ds_read_b64_tr_b16 v[92:93], v23 offset:40960\n"
      "ds_read_b64_tr_b16 v[94:95], v23 offset:41216\n"
      "v_xor_b32 v23, 0x10000, v23\n"
      "s_cmp_eq_u32 s89, 3\n"
      "s_cselect_b64 exec, -1, 0\n"
      "ds_read_b128 v[24:27], v30\n"
      "s_mov_b64 exec, -1\n"
      "s_waitcnt vmcnt(4)\n"
      "s_barrier\n"
      "s_waitcnt lgkmcnt(7)\n"
      "v_mfma_f32_32x32x16_bf16 v[112:127], v[176:179], v[80:83], v[112:127]\n"
      "v_mfma_f32_16x16x32_bf16 v[52:55], v[12:15], v[80:83], v[52:55]\n"
      "ds_read_b128 v[192:195], v16 offset:0\n"
      "ds_read_b64_tr_b16 v[64:65], v20 offset:0\n"
      "ds_read_b64_tr_b16 v[66:67], v20 offset:256\n"
      "s_add_u32 m0, s35, 65536\n"
      "s_add_u32 s3, s33, 768\n"
      "buffer_load_dwordx4 v46, s[4:7], s3 offen lds\n"
      "s_waitcnt lgkmcnt(8)\n"
      "v_mfma_f32_32x32x16_bf16 v[112:127], v[180:183], v[84:87], v[112:127]\n"
      "ds_read_b128 v[196:199], v17 offset:0\n"
      "ds_read_b64_tr_b16 v[68:69], v21 offset:0\n"
      "ds_read_b64_tr_b16 v[70:71], v21 offset:256\n"
      "s_add_u32 m0, s35, 66560\n"
      "s_add_u32 s3, s33, 768\n"
      "buffer_load_dwordx4 v47, s[4:7], s3 offen lds\n"
      "s_waitcnt lgkmcnt(9)\n"
      "v_mfma_f32_32x32x16_bf16 v[112:127], v[184:187], v[88:91], v[112:127]\n"
      "ds_read_b128 v[200:203], v18 offset:0\n"
      "ds_read_b64_tr_b16 v[72:73], v22 offset:0\n"
      "ds_read_b64_tr_b16 v[74:75], v22 offset:256\n"
      "s_add_u32 m0, s34, 81920\n"
      "s_add_u32 s3, s36, 1179648\n"
      "buffer_load_dwordx4 v44, s[8:11], s3 offen lds\n"
      "s_waitcnt lgkmcnt(10)\n"
      "v_mfma_f32_32x32x16_bf16 v[112:127], v[188:191], v[92:95], v[112:127]\n"
      "ds_read_b128 v[204:207], v19 offset:0\n"
      "ds_read_b64_tr_b16 v[76:77], v23 offset:0\n"
      "ds_read_b64_tr_b16 v[78:79], v23 offset:256\n"
      "s_add_u32 m0, s34, 90112\n"
      "s_add_u32 s3, s37, 1179648\n"
      "buffer_load_dwordx4 v44, s[8:11], s3 offen lds\n"
      "s_waitcnt lgkmcnt(9)\n"
      "v_mfma_f32_32x32x16_bf16 v[96:111], v[192:195], v[64:67], v[96:111]\n"
      "v_mfma_f32_16x16x32_bf16 v[48:51], v[12:15], v[64:67], v[48:51]\n"
      "ds_read_b64_tr_b16 v[80:81], v20 offset:8192\n"
      "ds_read_b64_tr_b16 v[82:83], v20 offset:8448\n"
      "s_waitcnt lgkmcnt(8)\n"
      "v_mfma_f32_32x32x16_bf16 v[96:111], v[196:199], v[68:71], v[96:111]\n"
      "ds_read_b64_tr_b16 v[84:85], v21 offset:8192\n"
      "ds_read_b64_tr_b16 v[86:87], v21 offset:8448\n"
      "s_waitcnt lgkmcnt(7)\n"
      "v_mfma_f32_32x32x16_bf16 v[96:111], v[200:203], v[72:75], v[96:111]\n"
      "ds_read_b64_tr_b16 v[88:89], v22 offset:8192\n"
      "ds_read_b64_tr_b16 v[90:91], v22 offset:8448\n"
      "s_waitcnt lgkmcnt(6)\n"
      "v_mfma_f32_32x32x16_bf16 v[96:111], v[204:207], v[76:79], v[96:111]\n"
      "ds_read_b64_tr_b16 v[92:93], v23 offset:8192\n"
      "ds_read_b64_tr_b16 v[94:95], v23 offset:8448\n"
      "s_cmp_eq_u32 s89, 4\n"
      "s_cselect_b64 exec, -1, 0\n"
      "ds_read_b128 v[24:27], v30\n"
      "s_mov_b64 exec, -1\n"
      "s_waitcnt vmcnt(4)\n"
      "s_barrier\n"
      "s_waitcnt lgkmcnt(7)\n"
      "v_mfma_f32_32x32x16_bf16 v[112:127], v[192:195], v[80:83], v[112:127]\n"
      "v_mfma_f32_16x16x32_bf16 v[52:55], v[12:15], v[80:83], v[52:55]\n"
      "ds_read_b128 v[208:211], v16 offset:32768\n"
      "ds_read_b64_tr_b16 v[64:65], v20 offset:32768\n"
      "ds_read_b64_tr_b16 v[66:67], v20 offset:33024\n"
      "v_xor_b32 v16, 0x10000, v16\n"
      "buffer_load_dwordx4 v[32:35], v60, s[12:15], s56 offen sc1\n"
      "s_waitcnt lgkmcnt(8)\n"
      "v_mfma_f32_32x32x16_bf16 v[112:127], v[196:199], v[84:87], v[112:127]\n"
      "ds_read_b128 v[212:215], v17 offset:32768\n"
      "ds_read_b64_tr_b16 v[68:69], v21 offset:32768\n"
      "ds_read_b64_tr_b16 v[70:71], v21 offset:33024\n"
      "v_xor_b32 v17, 0x10000, v17\n"
      "s_add_u32 m0, s35, 98304\n"
      "s_add_u32 s3, s33, 896\n"
      "buffer_load_dwordx4 v46, s[4:7], s3 offen lds\n"
      "s_waitcnt lgkmcnt(9)\n"
      "v_mfma_f32_32x32x16_bf16 v[112:127], v[200:203], v[88:91], v[112:127]\n"
      "ds_read_b128 v[216:219], v18 offset:32768\n"
      "ds_read_b64_tr_b16 v[72:73], v22 offset:32768\n"
      "ds_read_b64_tr_b16 v[74:75], v22 offset:33024\n"
      "v_xor_b32 v18, 0x10000, v18\n"
      "s_add_u32 m0, s35, 99328\n"
      "s_add_u32 s3, s33, 896\n"
      "buffer_load_dwordx4 v47, s[4:7], s3 offen lds\n"
      "s_waitcnt lgkmcnt(10)\n"
      "v_mfma_f32_32x32x16_bf16 v[112:127], v[204:207], v[92:95], v[112:127]\n"
      "ds_read_b128 v[220:223], v19 offset:32768\n"
      "ds_read_b64_tr_b16 v[76:77], v23 offset:32768\n"
      "ds_read_b64_tr_b16 v[78:79], v23 offset:33024\n"
      "v_xor_b32 v19, 0x10000, v19\n"
      "s_add_u32 m0, s34, 114688\n"
      "s_add_u32 s3, s36, 1376256\n"
      "buffer_load_dwordx4 v44, s[8:11], s3 offen lds\n"
      "s_waitcnt lgkmcnt(9)\n"
      "v_mfma_f32_32x32x16_bf16 v[96:111], v[208:211], v[64:67], v[96:111]\n"
      "v_mfma_f32_16x16x32_bf16 v[48:51], v[12:15], v[64:67], v[48:51]\n"
      "ds_read_b64_tr_b16 v[80:81], v20 offset:40960\n"
      "ds_read_b64_tr_b16 v[82:83], v20 offset:41216\n"
      "v_xor_b32 v20, 0x10000, v20\n"
      "s_add_u32 m0, s34, 122880\n"
      "s_add_u32 s3, s37, 1376256\n"
      "buffer_load_dwordx4 v44, s[8:11], s3 offen lds\n"
      "s_waitcnt lgkmcnt(8)\n"
      "v_mfma_f32_32x32x16_bf16 v[96:111], v[212:215], v[68:71], v[96:111]\n"
      "ds_read_b64_tr_b16 v[84:85], v21 offset:40960\n"
      "ds_read_b64_tr_b16 v[86:87], v21 offset:41216\n"
      "v_xor_b32 v21, 0x10000, v21\n"
      "s_waitcnt lgkmcnt(7)\n"
      "v_mfma_f32_32x32x16_bf16 v[96:111], v[216:219], v[72:75], v[96:111]\n"
      "ds_read_b64_tr_b16 v[88:89], v22 offset:40960\n"
      "ds_read_b64_tr_b16 v[90:91], v22 offset:41216\n"
      "v_xor_b32 v22, 0x10000, v22\n"
      "s_waitcnt lgkmcnt(6)\n"
      "v_mfma_f32_32x32x16_bf16 v[96:111], v[220:223], v[76:79], v[96:111]\n"
      "ds_read_b64_tr_b16 v[92:93], v23 offset:40960\n"
      "ds_read_b64_tr_b16 v[94:95], v23 offset:41216\n"
      "v_xor_b32 v23, 0x10000, v23\n"
      "s_cmp_eq_u32 s89, 5\n"
      "s_cselect_b64 exec, -1, 0\n"
      "ds_read_b128 v[24:27], v30\n"
      "s_mov_b64 exec, -1\n"
      "s_waitcnt vmcnt(5)\n"
      "s_barrier\n"
      "s_memrealtime s[90:91]\n"
      "s_waitcnt lgkmcnt(0)\n"
      "v_mfma_f32_32x32x16_bf16 v[112:127], v[208:211], v[80:83], v[112:127]\n"
      "v_mfma_f32_16x16x32_bf16 v[52:55], v[12:15], v[80:83], v[52:55]\n"
      "ds_read_b128 v[224:227], v16 offset:0\n"
      "ds_read_b64_tr_b16 v[64:65], v20 offset:0\n"
      "ds_read_b64_tr_b16 v[66:67], v20 offset:256\n"
      "v_mov_b32 v42, s90\n"
      "s_lshl3_add_u32 s3, s2, 32768\n"
      "buffer_store_dwordx2 v[42:43], v39, s[12:15], s3 offen sc1\n"
      "v_mfma_f32_32x32x16_bf16 v[112:127], v[212:215], v[84:87], v[112:127]\n"
      "ds_read_b128 v[228:231], v17 offset:0\n"
      "ds_read_b64_tr_b16 v[68:69], v21 offset:0\n"
      "ds_read_b64_tr_b16 v[70:71], v21 offset:256\n"
      "s_add_u32 m0, s96, 0\n"
      "s_nop 0\n"
      "buffer_load_dwordx4 v44, s[8:11], s38 offen lds\n"
      "v_mfma_f32_32x32x16_bf16 v[112:127], v[216:219], v[88:91], v[112:127]\n"
      "ds_read_b128 v[232:235], v18 offset:0\n"
      "ds_read_b64_tr_b16 v[72:73], v22 offset:0\n"
      "ds_read_b64_tr_b16 v[74:75], v22 offset:256\n"
      "s_add_u32 m0, s96, 4096\n"
      "s_add_u32 s3, s38, 196608\n"
      "buffer_load_dwordx4 v44, s[8:11], s3 offen lds\n"
      "v_mfma_f32_32x32x16_bf16 v[112:127], v[220:223], v[92:95], v[112:127]\n"
      "ds_read_b128 v[236:239], v19 offset:0\n"
      "ds_read_b64_tr_b16 v[76:77], v23 offset:0\n"
      "ds_read_b64_tr_b16 v[78:79], v23 offset:256\n"
      "s_add_u32 m0, s96, 8192\n"
      "s_add_u32 s3, s38, 393216\n"
      "buffer_load_dwordx4 v44, s[8:11], s3 offen lds\n"
      "s_waitcnt vmcnt(8)\n"
      "v_xor_b32 v4, s65, v34\n"
      "v_bitop3_b32 v4, v35, s65, v4 bitop3:0xbe\n"
      "v_cmp_ne_u32 vcc, 0, v4\n"
      "s_nop 1\n"
      "s_cbranch_vccnz LAr1\n"
      "LAo1:\n"
      "s_waitcnt lgkmcnt(9)\n"
      "v_mfma_f32_32x32x16_bf16 v[96:111], v[224:227], v[64:67], v[96:111]\n"
      "v_mfma_f32_16x16x32_bf16 v[48:51], v[12:15], v[64:67], v[48:51]\n"
      "ds_read_b64_tr_b16 v[80:81], v20 offset:8192\n"
      "ds_read_b64_tr_b16 v[82:83], v20 offset:8448\n"
      "s_add_u32 m0, s96, 12288\n"
      "s_add_u32 s3, s38, 589824\n"
      "buffer_load_dwordx4 v44, s[8:11], s3 offen lds\n"
      "v_perm_b32 v8, v32, v32, v61\n"
      "v_mul_f32 v3, 0x3fb8aa3b, v33\n"
      "v_mov_b32 v9, v8\n"
      "v_mov_b32 v10, v8\n"
      "s_waitcnt lgkmcnt(8)\n"
      "v_mfma_f32_32x32x16_bf16 v[96:111], v[228:231], v[68:71], v[96:111]\n"
      "ds_read_b64_tr_b16 v[84:85], v21 offset:8192\n"
      "ds_read_b64_tr_b16 v[86:87], v21 offset:8448\n"
      "s_add_u32 m0, s96, 16384\n"
      "s_add_u32 s3, s38, 786432\n"
      "buffer_load_dwordx4 v44, s[8:11], s3 offen lds\n"
      "v_mov_b32 v11, v8\n"
      "s_waitcnt lgkmcnt(7)\n"
      "v_mfma_f32_32x32x16_bf16 v[96:111], v[232:235], v[72:75], v[96:111]\n"
      "ds_read_b64_tr_b16 v[88:89], v22 offset:8192\n"
      "ds_read_b64_tr_b16 v[90:91], v22 offset:8448\n"
      "s_add_u32 m0, s96, 20480\n"
      "s_add_u32 s3, s38, 983040\n"
      "buffer_load_dwordx4 v44, s[8:11], s3 offen lds\n"
      "s_waitcnt lgkmcnt(6)\n"
      "v_mfma_f32_32x32x16_bf16 v[96:111], v[236:239], v[76:79], v[96:111]\n"
      "ds_read_b64_tr_b16 v[92:93], v23 offset:8192\n"
      "ds_read_b64_tr_b16 v[94:95], v23 offset:8448\n"
      "s_cmp_eq_u32 s89, 6\n"
      "s_cselect_b64 exec, -1, 0\n"
      "ds_read_b128 v[24:27], v30\n"
      "s_mov_b64 exec, -1\n"
      "s_waitcnt vmcnt(7)\n"
      "s_barrier\n"
      "s_waitcnt lgkmcnt(7)\n"
      "v_mfma_f32_32x32x16_bf16 v[112:127], v[224:227], v[80:83], v[112:127]\n"
      "v_mfma_f32_16x16x32_bf16 v[52:55], v[12:15], v[80:83], v[52:55]\n"
      "ds_read_b128 v[240:243], v16 offset:32768\n"
      "ds_read_b64_tr_b16 v[64:65], v20 offset:32768\n"
      "ds_read_b64_tr_b16 v[66:67], v20 offset:33024\n"
      "ds_read_b64_tr_b16 v[80:81], v20 offset:40960\n"
      "ds_read_b64_tr_b16 v[82:83], v20 offset:41216\n"
      "s_lshl3_add_u32 s3, s31, s32\n"
      "s_lshl3_add_u32 s3, s3, s28\n"
      "s_mul_i32 s3, s3, 512\n"
      "v_lshlrev_b32 v57, 7, v0\n"
      "s_mov_b64 exec, 15\n"
      "buffer_load_dword v57, v57, s[20:23], s3 offen\n"
      "s_mov_b64 exec, -1\n"
      "s_waitcnt lgkmcnt(10)\n"
      "v_mfma_f32_32x32x16_bf16 v[112:127], v[228:231], v[84:87], v[112:127]\n"
      "ds_read_b128 v[244:247], v17 offset:32768\n"
      "ds_read_b64_tr_b16 v[68:69], v21 offset:32768\n"
      "ds_read_b64_tr_b16 v[70:71], v21 offset:33024\n"
      "ds_read_b64_tr_b16 v[84:85], v21 offset:40960\n"
      "ds_read_b64_tr_b16 v[86:87], v21 offset:41216\n"
      "s_andn2_b32 s78, s2, 7\n"
      "s_add_u32 s78, s78, s28\n"
      "s_mul_i32 s3, s78, 6144\n"
      "v_lshlrev_b32 v56, 10, v0\n"
      "s_mov_b64 exec, 63\n"
      "buffer_load_dword v58, v56, s[16:19], s3 offen offset:384\n"
      "s_lshl_b32 s3, s78, 11\n"
      "s_mov_b64 exec, 3\n"
      "buffer_load_dword v59, v56, s[20:23], s3 offen offset:384\n"
      "s_mov_b64 exec, -1\n"
      "s_waitcnt lgkmcnt(13)\n"
      "v_mfma_f32_32x32x16_bf16 v[112:127], v[232:235], v[88:91], v[112:127]\n"
      "ds_read_b128 v[248:251], v18 offset:32768\n"
      "ds_read_b64_tr_b16 v[72:73], v22 offset:32768\n"
      "ds_read_b64_tr_b16 v[74:75], v22 offset:33024\n"
      "ds_read_b64_tr_b16 v[88:89], v22 offset:40960\n"
      "ds_read_b64_tr_b16 v[90:91], v22 offset:41216\n"
      "s_waitcnt lgkmcnt(15)\n"
      "v_mfma_f32_32x32x16_bf16 v[112:127], v[236:239], v[92:95], v[112:127]\n"
      "ds_read_b128 v[252:255], v19 offset:32768\n"
      "ds_read_b64_tr_b16 v[76:77], v23 offset:32768\n"
      "ds_read_b64_tr_b16 v[78:79], v23 offset:33024\n"
      "ds_read_b64_tr_b16 v[92:93], v23 offset:40960\n"
      "ds_read_b64_tr_b16 v[94:95], v23 offset:41216\n"
      "s_cmp_eq_u32 s89, 7\n"
      "s_cselect_b64 exec, -1, 0\n"
      "ds_read_b128 v[24:27], v30\n"
      "s_mov_b64 exec, -1\n"
      "ds_write_b32 v62, v3\n"
      "s_waitcnt lgkmcnt(15)\n"
      "v_mfma_f32_16x16x32_bf16 v[48:51], v[12:15], v[64:67], v[48:51]\n"
      "v_mfma_f32_16x16x32_bf16 v[52:55], v[12:15], v[80:83], v[52:55]\n"
      "v_mfma_f32_32x32x16_bf16 v[96:111], v[240:243], v[64:67], v[96:111]\n"
      "v_add_u32 v20, v20, v45\n"
      "v_add_u32 v21, v21, v45\n"
      "v_add_u32 v22, v22, v45\n"
      "v_add_u32 v23, v23, v45\n"
      "v_mov_b32 v30, v63\n"
      "s_nop 0\n"
      "v_cvt_pk_bf16_f32 v49, v48, v48\n"
      "v_cvt_pk_bf16_f32 v53, v52, v52\n"
      "v_lshlrev_b32 v49, 16, v49\n"
      "v_lshlrev_b32 v53, 16, v53\n"
      "v_sub_f32 v49, v48, v49\n"
      "v_sub_f32 v53, v52, v53\n"
      "v_cvt_pk_bf16_f32 v49, v48, v49\n"
      "v_cvt_pk_bf16_f32 v53, v52, v53\n"
      "ds_write_b32 v29, v49 offset:0\n"
      "ds_write_b32 v29, v53 offset:1024\n"
      "s_waitcnt lgkmcnt(0)\n"
      "v_mfma_f32_32x32x16_bf16 v[96:111], v[244:247], v[68:71], v[96:111]\n"
      "s_waitcnt vmcnt(5) lgkmcnt(0)\n"
      "s_barrier\n"
      "v_mfma_f32_32x32x16_bf16 v[96:111], v[248:251], v[72:75], v[96:111]\n"
      "ds_read_b128 v[48:51], v28 offset:0\n"
      "ds_read_b128 v[52:55], v28 offset:1024\n"
      "ds_read_b128 v[16:19], v30 offset:0\n"
      "ds_read_b128 v[60:63], v30 offset:32\n"
      "ds_read_b64_tr_b16 v[64:65], v20 offset:0\n"
      "ds_read_b64_tr_b16 v[66:67], v20 offset:256\n"
      "s_add_u32 m0, s96, 24576\n"
      "s_add_u32 s3, s38, 1179648\n"
      "buffer_load_dwordx4 v44, s[8:11], s3 offen lds\n"
      "v_mfma_f32_32x32x16_bf16 v[96:111], v[252:255], v[76:79], v[96:111]\n"
      "ds_read_b64_tr_b16 v[68:69], v21 offset:0\n"
      "ds_read_b64_tr_b16 v[70:71], v21 offset:256\n"
      "ds_read_b64_tr_b16 v[72:73], v22 offset:0\n"
      "ds_read_b64_tr_b16 v[74:75], v22 offset:256\n"
      "ds_read_b64_tr_b16 v[76:77], v23 offset:0\n"
      "ds_read_b64_tr_b16 v[78:79], v23 offset:256\n"
      "s_add_u32 m0, s96, 28672\n"
      "s_add_u32 s3, s38, 1376256\n"
      "buffer_load_dwordx4 v44, s[8:11], s3 offen lds\n"
      "v_mfma_f32_32x32x16_bf16 v[112:127], v[240:243], v[80:83], v[112:127]\n"
      "ds_read_b64_tr_b16 v[80:81], v20 offset:4096\n"
      "ds_read_b64_tr_b16 v[82:83], v20 offset:4352\n"
      "s_add_u32 m0, s34, 98304\n"
      "s_nop 0\n"
      "buffer_load_dwordx4 v6, s[16:19], s41 offen lds\n"
      "s_waitcnt lgkmcnt(13)\n"
      "v_mfma_f32_32x32x16_bf16 v[96:111], v[8:11], v[48:51], v[96:111]\n"
      "ds_read_b128 v[48:51], v30 offset:64\n"
      "v_mfma_f32_32x32x16_bf16 v[112:127], v[244:247], v[84:87], v[112:127]\n"
      "ds_read_b64_tr_b16 v[84:85], v21 offset:4096\n"
      "ds_read_b64_tr_b16 v[86:87], v21 offset:4352\n"
      "s_add_u32 m0, s34, 106496\n"
      "s_nop 0\n"
      "buffer_load_dwordx4 v6, s[16:19], s42 offen lds\n"
      "s_waitcnt lgkmcnt(14)\n"
      "s_nop 3\n"
      "v_mul_f32 v96, v96, v16\n"
      "v_mul_f32 v97, v97, v17\n"
      "v_exp_f32 v4, v96 clamp\n"
      "v_exp_f32 v5, v97 clamp\n"
      "v_max_f32 v96, 0x35c1a2a0, v96\n"
      "v_mfma_f32_32x32x16_bf16 v[112:127], v[248:251], v[88:91], v[112:127]\n"
      "ds_read_b64_tr_b16 v[88:89], v22 offset:4096\n"
      "ds_read_b64_tr_b16 v[90:91], v22 offset:4352\n"
      "v_max_f32 v97, 0x35c1a2a0, v97\n"
      "v_fmamk_f32 v96, v96, 0x3f317218, v4\n"
      "v_fmamk_f32 v97, v97, 0x3f317218, v5\n"
      "v_cvt_pk_bf16_f32 v96, v96, v97\n"
      "v_mul_f32 v98, v98, v18\n"
      "v_mul_f32 v99, v99, v19\n"
      "v_exp_f32 v4, v98 clamp\n"
      "v_mfma_f32_32x32x16_bf16 v[112:127], v[252:255], v[92:95], v[112:127]\n"
      "ds_read_b64_tr_b16 v[92:93], v23 offset:4096\n"
      "ds_read_b64_tr_b16 v[94:95], v23 offset:4352\n"
      "s_add_u32 m0, s34, 114688\n"
      "s_nop 0\n"
      "buffer_load_dwordx4 v6, s[16:19], s43 offen lds\n"
      "v_mfma_f32_32x32x16_bf16 v[112:127], v[8:11], v[52:55], v[112:127]\n"
      "ds_read_b128 v[52:55], v30 offset:96\n"
      "v_exp_f32 v5, v99 clamp\n"
      "v_max_f32 v98, 0x35c1a2a0, v98\n"
      "v_max_f32 v99, 0x35c1a2a0, v99\n"
      "v_fmamk_f32 v98, v98, 0x3f317218, v4\n"
      "v_fmamk_f32 v99, v99, 0x3f317218, v5\n"
      "v_cvt_pk_bf16_f32 v97, v98, v99\n"
      "s_waitcnt vmcnt(5) lgkmcnt(15)\n"
      "v_mfma_f32_16x16x32_bf16 v[56:59], v[12:15], v[64:67], 0\n"
      "v_mfma_f32_32x32x16_bf16 v[32:47], v[64:67], v[128:131], 0\n"
      "ds_read_b64_tr_b16 v[64:65], v20 offset:8192\n"
      "ds_read_b64_tr_b16 v[66:67], v20 offset:8448\n"
      "s_add_u32 m0, s34, 122880\n"
      "s_nop 0\n"
      "buffer_load_dwordx4 v6, s[16:19], s44 offen lds\n"
      "v_mul_f32 v100, v100, v60\n"
      "v_mul_f32 v101, v101, v61\n"
      "v_exp_f32 v4, v100 clamp\n"
      "v_exp_f32 v5, v101 clamp\n"
      "v_max_f32 v100, 0x35c1a2a0, v100\n"
      "s_waitcnt lgkmcnt(15)\n"
      "v_mfma_f32_32x32x16_bf16 v[32:47], v[68:71], v[132:135], v[32:47]\n"
      "ds_read_b64_tr_b16 v[68:69], v21 offset:8192\n"
      "ds_read_b64_tr_b16 v[70:71], v21 offset:8448\n"
      "v_max_f32 v101, 0x35c1a2a0, v101\n"
      "v_fmamk_f32 v100, v100, 0x3f317218, v4\n"
      "v_fmamk_f32 v101, v101, 0x3f317218, v5\n"
      "v_cvt_pk_bf16_f32 v98, v100, v101\n"
      "v_mul_f32 v102, v102, v62\n"
      "v_mul_f32 v103, v103, v63\n"
      "v_exp_f32 v4, v102 clamp\n"
      "s_waitcnt lgkmcnt(15)\n"
      "v_mfma_f32_32x32x16_bf16 v[32:47], v[72:75], v[136:139], v[32:47]\n"
      "ds_read_b64_tr_b16 v[72:73], v22 offset:8192\n"
      "ds_read_b64_tr_b16 v[74:75], v22 offset:8448\n"
      "s_add_u32 m0, s34, 65536\n"
      "s_add_u32 s3, s41, 65536\n"
      "buffer_load_dwordx4 v6, s[16:19], s3 offen lds\n"
      "v_exp_f32 v5, v103 clamp\n"
      "v_max_f32 v102, 0x35c1a2a0, v102\n"
      "v_max_f32 v103, 0x35c1a2a0, v103\n"
      "v_fmamk_f32 v102, v102, 0x3f317218, v4\n"
      "v_fmamk_f32 v103, v103, 0x3f317218, v5\n"
      "v_cvt_pk_bf16_f32 v99, v102, v103\n"
      "s_waitcnt lgkmcnt(15)\n"
      "v_mfma_f32_32x32x16_bf16 v[32:47], v[76:79], v[140:143], v[32:47]\n"
      "ds_read_b64_tr_b16 v[76:77], v23 offset:8192\n"
      "ds_read_b64_tr_b16 v[78:79], v23 offset:8448\n"
      "s_add_u32 m0, s34, 73728\n"
      "s_add_u32 s3, s42, 65536\n"
      "buffer_load_dwordx4 v6, s[16:19], s3 offen lds\n"
      "s_waitcnt lgkmcnt(15)\n"
      "v_mul_f32 v104, v104, v48\n"
      "v_mul_f32 v105, v105, v49\n"
      "v_exp_f32 v4, v104 clamp\n"
      "v_exp_f32 v5, v105 clamp\n"
      "v_max_f32 v104, 0x35c1a2a0, v104\n"
      "v_mfma_f32_16x16x32_bf16 v[56:59], v[12:15], v[80:83], v[56:59]\n"
      "v_mfma_f32_32x32x16_bf16 v[32:47], v[80:83], v[144:147], v[32:47]\n"
      "ds_read_b64_tr_b16 v[80:81], v20 offset:12288\n"
      "ds_read_b64_tr_b16 v[82:83], v20 offset:12544\n"
      "v_max_f32 v105, 0x35c1a2a0, v105\n"
      "v_fmamk_f32 v104, v104, 0x3f317218, v4\n"
      "v_fmamk_f32 v105, v105, 0x3f317218, v5\n"
      "v_cvt_pk_bf16_f32 v100, v104, v105\n"
      "v_mul_f32 v106, v106, v50\n"
      "v_mul_f32 v107, v107, v51\n"
      "v_exp_f32 v4, v106 clamp\n"
      "s_waitcnt lgkmcnt(15)\n"
      "v_mfma_f32_32x32x16_bf16 v[32:47], v[84:87], v[148:151], v[32:47]\n"
      "ds_read_b64_tr_b16 v[84:85], v21 offset:12288\n"
      "ds_read_b64_tr_b16 v[86:87], v21 offset:12544\n"
      "s_add_u32 m0, s34, 81920\n"
      "s_add_u32 s3, s43, 65536\n"
      "buffer_load_dwordx4 v6, s[16:19], s3 offen lds\n"
      "v_exp_f32 v5, v107 clamp\n"
      "v_max_f32 v106, 0x35c1a2a0, v106\n"
      "v_max_f32 v107, 0x35c1a2a0, v107\n"
      "v_fmamk_f32 v106, v106, 0x3f317218, v4\n"
      "v_fmamk_f32 v107, v107, 0x3f317218, v5\n"
      "v_cvt_pk_bf16_f32 v101, v106, v107\n"
      "s_waitcnt lgkmcnt(15)\n"
      "v_mfma_f32_32x32x16_bf16 v[32:47], v[88:91], v[152:155], v[32:47]\n"
      "ds_read_b64_tr_b16 v[88:89], v22 offset:12288\n"
      "ds_read_b64_tr_b16 v[90:91], v22 offset:12544\n"
      "s_add_u32 m0, s34, 90112\n"
      "s_add_u32 s3, s44, 65536\n"
      "buffer_load_dwordx4 v6, s[16:19], s3 offen lds\n"
      "s_waitcnt lgkmcnt(14)\n"
      "v_mul_f32 v108, v108, v52\n"
      "v_mul_f32 v109, v109, v53\n"
      "v_exp_f32 v4, v108 clamp\n"
      "v_exp_f32 v5, v109 clamp\n"
      "v_max_f32 v108, 0x35c1a2a0, v108\n"
      "v_mfma_f32_32x32x16_bf16 v[32:47], v[92:95], v[156:159], v[32:47]\n"
      "ds_read_b64_tr_b16 v[92:93], v23 offset:12288\n"
      "ds_read_b64_tr_b16 v[94:95], v23 offset:12544\n"
      "v_max_f32 v109, 0x35c1a2a0, v109\n"
      "v_fmamk_f32 v108, v108, 0x3f317218, v4\n"
      "v_fmamk_f32 v109, v109, 0x3f317218, v5\n"
      "v_cvt_pk_bf16_f32 v102, v108, v109\n"
      "v_mul_f32 v110, v110, v54\n"
      "v_mul_f32 v111, v111, v55\n"
      "v_exp_f32 v4, v110 clamp\n"
      "s_waitcnt lgkmcnt(14)\n"
      "v_mfma_f32_16x16x32_bf16 v[56:59], v[12:15], v[64:67], v[56:59]\n"
      "v_mfma_f32_32x32x16_bf16 v[32:47], v[64:67], v[160:163], v[32:47]\n"
      "v_exp_f32 v5, v111 clamp\n"
      "v_max_f32 v110, 0x35c1a2a0, v110\n"
      "v_max_f32 v111, 0x35c1a2a0, v111\n"
      "v_fmamk_f32 v110, v110, 0x3f317218, v4\n"
      "v_fmamk_f32 v111, v111, 0x3f317218, v5\n"
      "v_cvt_pk_bf16_f32 v103, v110, v111\n"
      "s_waitcnt lgkmcnt(12)\n"
      "v_mfma_f32_32x32x16_bf16 v[32:47], v[68:71], v[164:167], v[32:47]\n"
      "s_waitcnt vmcnt(8) lgkmcnt(0)\n"
      "s_barrier\n"
      "ds_read_b64_tr_b16 v[64:65], v20 offset:16384\n"
      "ds_read_b64_tr_b16 v[66:67], v20 offset:16640\n"
      "ds_read_b64_tr_b16 v[68:69], v21 offset:16384\n"
      "ds_read_b64_tr_b16 v[70:71], v21 offset:16640\n"
      "v_mul_f32 v112, v112, v16\n"
      "v_mul_f32 v113, v113, v17\n"
      "v_cvt_pk_bf16_f32 v112, v112, v113\n"
      "v_mul_f32 v114, v114, v18\n"
      "v_mul_f32 v115, v115, v19\n"
      "v_cvt_pk_bf16_f32 v113, v114, v115\n"
      "v_mul_f32 v116, v116, v60\n"
      "v_mfma_f32_32x32x16_bf16 v[32:47], v[72:75], v[168:171], v[32:47]\n"
      "ds_read_b64_tr_b16 v[72:73], v22 offset:16384\n"
      "ds_read_b64_tr_b16 v[74:75], v22 offset:16640\n"
      "s_add_u32 m0, s34, 0\n"
      "s_nop 0\n"
      "buffer_load_dwordx4 v6, s[20:23], s46 offen lds\n"
      "v_mul_f32 v117, v117, v61\n"
      "v_cvt_pk_bf16_f32 v114, v116, v117\n"
      "v_mul_f32 v118, v118, v62\n"
      "v_mul_f32 v119, v119, v63\n"
      "v_cvt_pk_bf16_f32 v115, v118, v119\n"
      "v_mul_f32 v120, v120, v48\n"
      "v_mul_f32 v121, v121, v49\n"
      "v_mfma_f32_32x32x16_bf16 v[32:47], v[76:79], v[172:175], v[32:47]\n"
      "ds_read_b64_tr_b16 v[76:77], v23 offset:16384\n"
      "ds_read_b64_tr_b16 v[78:79], v23 offset:16640\n"
      "v_cvt_pk_bf16_f32 v116, v120, v121\n"
      "v_mul_f32 v122, v122, v50\n"
      "v_mul_f32 v123, v123, v51\n"
      "v_cvt_pk_bf16_f32 v117, v122, v123\n"
      "v_mul_f32 v124, v124, v52\n"
      "v_mul_f32 v125, v125, v53\n"
      "v_cvt_pk_bf16_f32 v118, v124, v125\n"
      "v_mfma_f32_16x16x32_bf16 v[56:59], v[12:15], v[80:83], v[56:59]\n"
      "v_mfma_f32_32x32x16_bf16 v[32:47], v[80:83], v[176:179], v[32:47]\n"
      "ds_read_b64_tr_b16 v[80:81], v20 offset:20480\n"
      "ds_read_b64_tr_b16 v[82:83], v20 offset:20736\n"
      "s_add_u32 m0, s34, 8192\n"
      "s_add_u32 s3, s46, 65536\n"
      "buffer_load_dwordx4 v6, s[20:23], s3 offen lds\n"
      "v_mul_f32 v126, v126, v54\n"
      "v_mul_f32 v127, v127, v55\n"
      "v_cvt_pk_bf16_f32 v119, v126, v127\n"
      "v_mfma_f32_32x32x16_bf16 v[32:47], v[84:87], v[180:183], v[32:47]\n"
      "ds_read_b64_tr_b16 v[84:85], v21 offset:20480\n"
      "ds_read_b64_tr_b16 v[86:87], v21 offset:20736\n"
      "v_mfma_f32_32x32x16_bf16 v[128:143], v[96:99], v[112:115], 0\n"
      "v_mfma_f32_32x32x16_bf16 v[128:143], v[100:103], v[116:119], v[128:143]\n"
      "v_mfma_f32_16x16x32_bf16 v[120:123], v[12:15], v[96:99], 0\n"
      "v_mfma_f32_16x16x32_bf16 v[120:123], v[12:15], v[100:103], v[120:123]\n"
      "v_lshl_add_u32 v16, v0, 5, s98\n"
      "v_lshl_add_u32 v17, v0, 2, s99\n"
      "v_and_b32 v4, 0xf0, v1\n"
      "v_lshrrev_b32 v5, 4, v0\n"
      "v_lshl_add_u32 v18, v4, 1, s100\n"
      "v_lshl_add_u32 v18, v5, 3, v18\n"
      "v_add_u32 v19, s39, v1\n"
      "v_add_u32 v4, s40, v1\n"
      "v_cndmask_b32 v19, v19, v4, s[86:87]\n"
      "v_mfma_f32_32x32x16_bf16 v[32:47], v[88:91], v[184:187], v[32:47]\n"
      "ds_read_b64_tr_b16 v[88:89], v22 offset:20480\n"
      "ds_read_b64_tr_b16 v[90:91], v22 offset:20736\n"
      "s_add_u32 m0, s34, 131072\n"
      "s_add_u32 s3, s46, 262144\n"
      "buffer_load_dwordx4 v6, s[20:23], s3 offen lds\n"
      "v_cndmask_b32 v128, v128, v136, s[86:87]\n"
      "v_cndmask_b32 v129, v129, v137, s[86:87]\n"
      "v_cndmask_b32 v130, v130, v138, s[86:87]\n"
      "v_cndmask_b32 v131, v131, v139, s[86:87]\n"
      "v_cndmask_b32 v132, v132, v140, s[86:87]\n"
      "v_cndmask_b32 v133, v133, v141, s[86:87]\n"
      "v_cndmask_b32 v134, v134, v142, s[86:87]\n"
      "v_mfma_f32_32x32x16_bf16 v[32:47], v[92:95], v[188:191], v[32:47]\n"
      "v_cndmask_b32 v135, v135, v143, s[86:87]\n"
      "ds_write_b128 v16, v[128:131]\n"
      "ds_write_b128 v16, v[132:135] offset:16\n"
      "ds_write_b32 v17, v120\n"
      "s_waitcnt lgkmcnt(0)\n"
      "s_barrier\n"
      "ds_read_b64 v[128:129], v18 offset:0\n"
      "ds_read_b64 v[130:131], v18 offset:2048\n"
      "ds_read_b64 v[132:133], v18 offset:4096\n"
      "ds_read_b64 v[134:135], v18 offset:6144\n"
      "ds_read_b64 v[136:137], v31 offset:0\n"
      "ds_read_b64 v[138:139], v31 offset:256\n"
      "ds_read_b64 v[140:141], v31 offset:512\n"
      "ds_read_b64 v[142:143], v31 offset:768\n"
      "v_mfma_f32_16x16x32_bf16 v[56:59], v[12:15], v[64:67], v[56:59]\n"
      "v_mfma_f32_32x32x16_bf16 v[32:47], v[64:67], v[192:195], v[32:47]\n"
      "ds_read_b64_tr_b16 v[92:93], v23 offset:20480\n"
      "ds_read_b64_tr_b16 v[94:95], v23 offset:20736\n"
      "ds_read_b64_tr_b16 v[64:65], v20 offset:24576\n"
      "ds_read_b64_tr_b16 v[66:67], v20 offset:24832\n"
      "s_waitcnt lgkmcnt(10)\n"
      "v_add_f32 v128, v128, v130\n"
      "v_add_f32 v129, v129, v131\n"
      "s_waitcnt lgkmcnt(8)\n"
      "v_add_f32 v132, v132, v134\n"
      "v_add_f32 v133, v133, v135\n"
      "v_add_f32 v128, v128, v132\n"
      "v_add_f32 v129, v129, v133\n"
      "v_mul_f32 v128, 0x3f317218, v128\n"
      "v_mul_f32 v129, 0x3f317218, v129\n"
      "v_cvt_pk_bf16_f32 v128, v128, v129\n"
      "v_and_or_b32 v128, v128, s97, v7\n"
      "s_waitcnt lgkmcnt(6)\n"
      "v_add_f32 v136, v136, v138\n"
      "v_add_f32 v137, v137, v139\n"
      "s_waitcnt lgkmcnt(4)\n"
      "v_add_f32 v140, v140, v142\n"
      "v_add_f32 v141, v141, v143\n"
      "v_add_f32 v136, v136, v140\n"
      "v_add_f32 v137, v137, v141\n"
      "v_cvt_pk_bf16_f32 v136, v136, v137\n"
      "v_and_or_b32 v129, v136, s97, v7\n"
      "s_nop 1\n"
      "v_permlane16_swap_b32 v128, v129\n"
      "v_mov_b32 v130, v128\n"
      "v_mov_b32 v131, v129\n"
      "s_nop 0\n"
      "v_permlane32_swap_b32 v128, v130\n"
      "v_permlane32_swap_b32 v129, v131\n"
      "s_mov_b64 exec, 0xfffff\n"
      "buffer_store_dwordx4 v[128:131], v19, s[12:15], 0 offen sc1\n"
      "s_mov_b64 exec, -1\n"
      "v_mov_b32 v7, 0xfffefffe\n"
      "v_lshrrev_b32 v5, 4, v0\n"
      "v_and_b32 v4, 0xf0, v1\n"
      "v_lshl_add_u32 v16, v5, 10, v4\n"
      "v_mfma_f32_32x32x16_bf16 v[32:47], v[68:71], v[196:199], v[32:47]\n"
      "ds_read_b64_tr_b16 v[68:69], v21 offset:24576\n"
      "ds_read_b64_tr_b16 v[70:71], v21 offset:24832\n"
      "s_add_u32 m0, s34, 139264\n"
      "s_add_u32 s3, s46, 327680\n"
      "buffer_load_dwordx4 v6, s[20:23], s3 offen lds\n"
      "v_lshlrev_b32 v17, 4, v5\n"
      "v_lshl_add_u32 v17, v4, 3, v17\n"
      "v_and_b32 v5, 1, v5\n"
      "v_lshl_add_u32 v4, v5, 3, v4\n"
      "v_mfma_f32_32x32x16_bf16 v[32:47], v[72:75], v[200:203], v[32:47]\n"
      "ds_read_b64_tr_b16 v[72:73], v22 offset:24576\n"
      "ds_read_b64_tr_b16 v[74:75], v22 offset:24832\n"
      "v_add_u32 v18, s101, v4\n"
      "v_add_u32 v19, s52, v1\n"
      "v_and_b32 v5, 16, v1\n"
      "v_mov_b32 v4, 0x3f80\n"
      "v_mfma_f32_32x32x16_bf16 v[32:47], v[76:79], v[204:207], v[32:47]\n"
      "ds_read_b64_tr_b16 v[76:77], v23 offset:24576\n"
      "ds_read_b64_tr_b16 v[78:79], v23 offset:24832\n"
      "v_lshlrev_b32 v4, v5, v4\n"
      "s_mov_b32 vcc_lo, 0x3030303\n"
      "s_mov_b32 vcc_hi, vcc_lo\n"
      "v_cndmask_b32 v60, 0, v4, vcc\n"
      "s_mov_b32 vcc_lo, 0xc0c0c0c\n"
      "s_mov_b32 vcc_hi, vcc_lo\n"
      "v_mfma_f32_16x16x32_bf16 v[56:59], v[12:15], v[80:83], v[56:59]\n"
      "v_mfma_f32_32x32x16_bf16 v[32:47], v[80:83], v[208:211], v[32:47]\n"
      "ds_read_b64_tr_b16 v[80:81], v20 offset:28672\n"
      "ds_read_b64_tr_b16 v[82:83], v20 offset:28928\n"
      "v_cndmask_b32 v61, 0, v4, vcc\n"
      "s_mov_b32 vcc_lo, 0x30303030\n"
      "s_mov_b32 vcc_hi, vcc_lo\n"
      "v_cndmask_b32 v62, 0, v4, vcc\n"
      "s_mov_b32 vcc_lo, 0xc0c0c0c0\n"
      "s_mov_b32 vcc_hi, vcc_lo\n"
      "v_mfma_f32_32x32x16_bf16 v[32:47], v[84:87], v[212:215], v[32:47]\n"
      "ds_read_b64_tr_b16 v[84:85], v21 offset:28672\n"
      "ds_read_b64_tr_b16 v[86:87], v21 offset:28928\n"
      "v_cndmask_b32 v63, 0, v4, vcc\n"
      "v_mfma_f32_32x32x16_bf16 v[32:47], v[88:91], v[216:219], v[32:47]\n"
      "ds_read_b64_tr_b16 v[88:89], v22 offset:28672\n"
      "ds_read_b64_tr_b16 v[90:91], v22 offset:28928\n"
      "s_waitcnt lgkmcnt(14)\n"
      "v_mfma_f32_32x32x16_bf16 v[32:47], v[92:95], v[220:223], v[32:47]\n"
      "ds_read_b64_tr_b16 v[92:93], v23 offset:28672\n"
      "ds_read_b64_tr_b16 v[94:95], v23 offset:28928\n"
      "s_waitcnt lgkmcnt(14)\n"
      "v_mfma_f32_16x16x32_bf16 v[56:59], v[12:15], v[64:67], v[56:59]\n"
      "s_waitcnt lgkmcnt(6)\n"
      "v_mfma_f32_16x16x32_bf16 v[56:59], v[12:15], v[80:83], v[56:59]\n"
      "v_mfma_f32_32x32x16_bf16 v[32:47], v[64:67], v[224:227], v[32:47]\n"
      "s_nop 6\n"
      "v_cvt_pk_bf16_f32 v57, v56, v56\n"
      "v_lshlrev_b32 v57, 16, v57\n"
      "v_sub_f32 v57, v56, v57\n"
      "v_cvt_pk_bf16_f32 v57, v56, v57\n"
      "ds_write_b32 v29, v57 offset:2048\n"
      "s_waitcnt lgkmcnt(0)\n"
      "v_mfma_f32_32x32x16_bf16 v[32:47], v[68:71], v[228:231], v[32:47]\n"
      "s_waitcnt vmcnt(9) lgkmcnt(0)\n"
      "s_barrier\n"
      "v_mfma_f32_32x32x16_bf16 v[32:47], v[72:75], v[232:235], v[32:47]\n"
      "ds_read_b128 v[56:59], v28 offset:2048\n"
      "s_add_u32 m0, s34, 49152\n"
      "s_nop 0\n"
      "buffer_load_dwordx4 v6, s[16:19], s45 offen lds\n"
      "v_lshrrev_b32 v220, 5, v0\n"
      "v_and_b32 v222, 31, v0\n"
      "v_bfe_u32 v223, v0, 4, 1\n"
      "v_bfe_u32 v4, v0, 2, 2\n"
      "v_mfma_f32_32x32x16_bf16 v[32:47], v[76:79], v[236:239], v[32:47]\n"
      "s_add_u32 m0, s34, 16384\n"
      "s_add_u32 s3, s46, 131072\n"
      "buffer_load_dwordx4 v6, s[20:23], s3 offen lds\n"
      "v_lshl_add_u32 v4, v220, 2, v4\n"
      "v_lshlrev_b32 v4, 7, v4\n"
      "v_lshl_add_u32 v4, v223, 5, v4\n"
      "v_and_b32 v5, 11, v0\n"
      "v_mfma_f32_32x32x16_bf16 v[32:47], v[80:83], v[240:243], v[32:47]\n"
      "s_add_u32 m0, s34, 24576\n"
      "s_add_u32 s3, s46, 196608\n"
      "buffer_load_dwordx4 v6, s[20:23], s3 offen lds\n"
      "v_lshl_add_u32 v4, v5, 3, v4\n"
      "s_mul_i32 s81, s30, 4160\n"
      "s_lshr_b32 s82, s29, 1\n"
      "s_mul_i32 s82, s82, 2080\n"
      "s_or_b32 s82, s82, s81\n"
      "s_or_b32 s82, s82, 0\n"
      "v_xor_b32 v20, s82, v4\n"
      "v_mfma_f32_32x32x16_bf16 v[32:47], v[84:87], v[244:247], v[32:47]\n"
      "s_add_u32 m0, s34, 147456\n"
      "s_add_u32 s3, s46, 393216\n"
      "buffer_load_dwordx4 v6, s[20:23], s3 offen lds\n"
      "v_xor_b32 v21, 64, v20\n"
      "s_mul_i32 s81, s30, 2112\n"
      "s_or_b32 s81, s81, 65536\n"
      "v_xor_b32 v22, s81, v4\n"
      "v_xor_b32 v23, 64, v22\n"
      "v_mfma_f32_32x32x16_bf16 v[32:47], v[88:91], v[248:251], v[32:47]\n"
      "s_add_u32 m0, s34, 155648\n"
      "s_add_u32 s3, s46, 458752\n"
      "buffer_load_dwordx4 v6, s[20:23], s3 offen lds\n"
      "v_add_u32 v28, 131072, v20\n"
      "v_add_u32 v29, 131072, v21\n"
      "v_mov_b32 v7, 0xfffefffe\n"
      "s_lshl_b32 s81, s28, 12\n"
      "v_mfma_f32_32x32x16_bf16 v[32:47], v[92:95], v[252:255], v[32:47]\n"
      "s_waitcnt lgkmcnt(0)\n"
      "v_mfma_f32_32x32x16_bf16 v[32:47], v[56:59], v[8:11], v[32:47]\n"
      "ds_read_b64_tr_b16 v[64:65], v22 offset:32768\n"
      "ds_read_b64_tr_b16 v[66:67], v22 offset:33792\n"
      "ds_read_b64_tr_b16 v[68:69], v23 offset:32768\n"
      "ds_read_b64_tr_b16 v[70:71], v23 offset:33792\n"
      "ds_read_b64_tr_b16 v[72:73], v22 offset:36864\n"
      "ds_read_b64_tr_b16 v[74:75], v22 offset:37888\n"
      "ds_read_b64_tr_b16 v[76:77], v23 offset:36864\n"
      "ds_read_b64_tr_b16 v[78:79], v23 offset:37888\n"
      "s_nop 3\n"
      "v_mul_f32 v32, v32, v3\n"
      "v_mul_f32 v33, v33, v3\n"
      "v_exp_f32 v4, v32 clamp\n"
      "v_exp_f32 v5, v33 clamp\n"
      "v_max_f32 v32, 0x35c1a2a0, v32\n"
      "v_max_f32 v33, 0x35c1a2a0, v33\n"
      "v_fmamk_f32 v32, v32, 0x3f317218, v4\n"
      "v_fmamk_f32 v33, v33, 0x3f317218, v5\n"
      "v_cvt_pk_bf16_f32 v32, v32, v33\n"
      "v_mul_f32 v34, v34, v3\n"
      "v_mul_f32 v35, v35, v3\n"
      "v_exp_f32 v4, v34 clamp\n"
      "v_exp_f32 v5, v35 clamp\n"
      "v_max_f32 v34, 0x35c1a2a0, v34\n"
      "v_max_f32 v35, 0x35c1a2a0, v35\n"
      "v_fmamk_f32 v34, v34, 0x3f317218, v4\n"
      "v_fmamk_f32 v35, v35, 0x3f317218, v5\n"
      "v_cvt_pk_bf16_f32 v33, v34, v35\n"
      "v_mul_f32 v36, v36, v3\n"
      "v_mul_f32 v37, v37, v3\n"
      "v_exp_f32 v4, v36 clamp\n"
      "v_exp_f32 v5, v37 clamp\n"
      "v_max_f32 v36, 0x35c1a2a0, v36\n"
      "v_max_f32 v37, 0x35c1a2a0, v37\n"
      "v_fmamk_f32 v36, v36, 0x3f317218, v4\n"
      "v_fmamk_f32 v37, v37, 0x3f317218, v5\n"
      "v_cvt_pk_bf16_f32 v34, v36, v37\n"
      "s_add_u32 s3, s66, 131072\n"
      "buffer_load_dwordx4 v[160:163], v16, s[12:15], s3 offen sc1\n"
      "v_mul_f32 v38, v38, v3\n"
      "v_mul_f32 v39, v39, v3\n"
      "v_exp_f32 v4, v38 clamp\n"
      "v_exp_f32 v5, v39 clamp\n"
      "v_max_f32 v38, 0x35c1a2a0, v38\n"
      "v_max_f32 v39, 0x35c1a2a0, v39\n"
      "v_fmamk_f32 v38, v38, 0x3f317218, v4\n"
      "v_fmamk_f32 v39, v39, 0x3f317218, v5\n"
      "v_cvt_pk_bf16_f32 v35, v38, v39\n"
      "s_add_u32 s3, s66, 135168\n"
      "buffer_load_dwordx4 v[164:167], v16, s[12:15], s3 offen sc1\n"
      "v_mul_f32 v40, v40, v3\n"
      "v_mul_f32 v41, v41, v3\n"
      "v_exp_f32 v4, v40 clamp\n"
      "v_exp_f32 v5, v41 clamp\n"
      "v_max_f32 v40, 0x35c1a2a0, v40\n"
      "v_max_f32 v41, 0x35c1a2a0, v41\n"
      "v_fmamk_f32 v40, v40, 0x3f317218, v4\n"
      "v_fmamk_f32 v41, v41, 0x3f317218, v5\n"
      "v_cvt_pk_bf16_f32 v36, v40, v41\n"
      "s_add_u32 s3, s66, 139264\n"
      "buffer_load_dwordx4 v[168:171], v16, s[12:15], s3 offen sc1\n"
      "v_mul_f32 v42, v42, v3\n"
      "v_mul_f32 v43, v43, v3\n"
      "v_exp_f32 v4, v42 clamp\n"
      "v_exp_f32 v5, v43 clamp\n"
      "v_max_f32 v42, 0x35c1a2a0, v42\n"
      "v_max_f32 v43, 0x35c1a2a0, v43\n"
      "v_fmamk_f32 v42, v42, 0x3f317218, v4\n"
      "v_fmamk_f32 v43, v43, 0x3f317218, v5\n"
      "v_cvt_pk_bf16_f32 v37, v42, v43\n"
      "s_add_u32 s3, s66, 143360\n"
      "buffer_load_dwordx4 v[172:175], v16, s[12:15], s3 offen sc1\n"
      "v_mul_f32 v44, v44, v3\n"
      "v_mul_f32 v45, v45, v3\n"
      "v_exp_f32 v4, v44 clamp\n"
      "v_exp_f32 v5, v45 clamp\n"
      "v_max_f32 v44, 0x35c1a2a0, v44\n"
      "v_max_f32 v45, 0x35c1a2a0, v45\n"
      "v_fmamk_f32 v44, v44, 0x3f317218, v4\n"
      "v_fmamk_f32 v45, v45, 0x3f317218, v5\n"
      "v_cvt_pk_bf16_f32 v38, v44, v45\n"
      "buffer_load_dwordx4 v[176:179], v17, s[12:15], s69 offen sc1\n"
      "v_mul_f32 v46, v46, v3\n"
      "v_mul_f32 v47, v47, v3\n"
      "v_exp_f32 v4, v46 clamp\n"
      "v_exp_f32 v5, v47 clamp\n"
      "v_max_f32 v46, 0x35c1a2a0, v46\n"
      "v_max_f32 v47, 0x35c1a2a0, v47\n"
      "v_fmamk_f32 v46, v46, 0x3f317218, v4\n"
      "v_fmamk_f32 v47, v47, 0x3f317218, v5\n"
      "v_cvt_pk_bf16_f32 v39, v46, v47\n"
      "buffer_load_dwordx4 v[128:131], v1, s[12:15], s48 offen sc1\n"
      "s_add_u32 s3, s48, 4096\n"
      "buffer_load_dwordx4 v[132:135], v1, s[12:15], s3 offen sc1\n"
      "s_waitcnt vmcnt(6)\n"
      "v_xor_b32 v160, s61, v160\n"
      "v_xor_b32 v161, s62, v161\n"
      "v_xor_b32 v162, s63, v162\n"
      "v_xor_b32 v163, s64, v163\n"
      "s_waitcnt vmcnt(5)\n"
      "v_xor_b32 v164, s61, v164\n"
      "v_xor_b32 v165, s62, v165\n"
      "v_xor_b32 v166, s63, v166\n"
      "v_xor_b32 v167, s64, v167\n"
      "s_waitcnt vmcnt(4)\n"
      "v_xor_b32 v168, s61, v168\n"
      "v_xor_b32 v169, s62, v169\n"
      "v_xor_b32 v170, s63, v170\n"
      "v_xor_b32 v171, s64, v171\n"
      "s_waitcnt vmcnt(3)\n"
      "v_xor_b32 v172, s61, v172\n"
      "v_xor_b32 v173, s62, v173\n"
      "v_xor_b32 v174, s63, v174\n"
      "v_xor_b32 v175, s64, v175\n"
      "s_waitcnt vmcnt(2)\n"
      "v_xor_b32 v176, s61, v176\n"
      "v_xor_b32 v177, s62, v177\n"
      "v_xor_b32 v178, s63, v178\n"
      "v_xor_b32 v179, s64, v179\n"
      "v_or3_b32 v4, v160, v161, v162\n"
      "v_or3_b32 v4, v4, v163, v164\n"
      "v_or3_b32 v4, v4, v165, v166\n"
      "v_or3_b32 v4, v4, v167, v168\n"
      "v_or3_b32 v4, v4, v169, v170\n"
      "v_or3_b32 v4, v4, v171, v172\n"
      "v_or3_b32 v4, v4, v173, v174\n"
      "v_or3_b32 v4, v4, v175, v176\n"
      "v_or3_b32 v4, v4, v177, v178\n"
      "v_or_b32 v4, v4, v179\n"
      "v_and_b32 v4, 0x10001, v4\n"
      "v_cmp_ne_u32 vcc, 0, v4\n"
      "s_nop 1\n"
      "s_cbranch_vccnz LAr2\n"
      "LAo2:\n"
      "v_mfma_f32_16x16x32_bf16 v[180:183], v[60:63], v[160:163], 0\n"
      "v_mfma_f32_16x16x32_bf16 v[180:183], v[60:63], v[164:167], v[180:183]\n"
      "v_mfma_f32_16x16x32_bf16 v[180:183], v[60:63], v[168:171], v[180:183]\n"
      "v_mfma_f32_16x16x32_bf16 v[180:183], v[60:63], v[172:175], v[180:183]\n"
      "v_mfma_f32_32x32x16_bf16 v[184:199], v[176:179], v[32:35], 0\n"
      "v_mfma_f32_32x32x16_bf16 v[200:215], v[176:179], v[36:39], 0\n"
      "s_nop 5\n"
      "v_cvt_pk_bf16_f32 v180, v180, v181\n"
      "v_cvt_pk_bf16_f32 v181, v182, v183\n"
      "ds_write_b64 v18, v[180:181]\n"
      "s_nop 1\n"
      "v_add_f32 v184, v184, v188\n"
      "v_add_f32 v185, v185, v189\n"
      "v_add_f32 v186, v186, v190\n"
      "v_add_f32 v187, v187, v191\n"
      "v_add_f32 v208, v208, v212\n"
      "v_add_f32 v209, v209, v213\n"
      "v_add_f32 v210, v210, v214\n"
      "v_add_f32 v211, v211, v215\n"
      "v_add_f32 v184, v184, v186\n"
      "v_add_f32 v185, v185, v187\n"
      "v_add_f32 v208, v208, v210\n"
      "v_add_f32 v209, v209, v211\n"
      "v_add_f32 v160, v184, v185\n"
      "v_add_f32 v162, v208, v209\n"
      "v_mov_b32 v161, v160\n"
      "v_mov_b32 v163, v162\n"
      "s_waitcnt lgkmcnt(0)\n"
      "s_barrier\n"
      "ds_read_b128 v[180:183], v19\n"
      "v_permlane32_swap_b32 v160, v161\n"
      "v_permlane32_swap_b32 v162, v163\n"
      "v_add_f32 v160, v160, v161\n"
      "v_add_f32 v162, v162, v163\n"
      "v_add_f32 v160, 0x358637bd, v160\n"
      "v_add_f32 v162, 0x358637bd, v162\n"
      "v_rcp_f32 v160, v160\n"
      "v_rcp_f32 v162, v162\n"
      "s_waitcnt lgkmcnt(0)\n"
      "v_mfma_f32_32x32x16_bf16 v[184:199], v[180:183], v[32:35], 0\n"
      "v_mfma_f32_32x32x16_bf16 v[200:215], v[180:183], v[36:39], 0\n"
      "s_add_u32 s3, s48, 8192\n"
      "buffer_load_dwordx4 v[136:139], v1, s[12:15], s3 offen sc1\n"
      "s_add_u32 s3, s48, 12288\n"
      "buffer_load_dwordx4 v[140:143], v1, s[12:15], s3 offen sc1\n"
      "s_add_u32 s3, s48, 16384\n"
      "buffer_load_dwordx4 v[144:147], v1, s[12:15], s3 offen sc1\n"
      "s_add_u32 s3, s48, 20480\n"
      "buffer_load_dwordx4 v[148:151], v1, s[12:15], s3 offen sc1\n"
      "s_add_u32 s3, s48, 24576\n"
      "buffer_load_dwordx4 v[152:155], v1, s[12:15], s3 offen sc1\n"
      "s_add_u32 s3, s48, 28672\n"
      "buffer_load_dwordx4 v[156:159], v1, s[12:15], s3 offen sc1\n"
      "v_mul_f32 v184, v184, v160\n"
      "v_mul_f32 v185, v185, v160\n"
      "v_cvt_pk_bf16_f32 v168, v184, v185\n"
      "v_and_or_b32 v168, v168, v7, s61\n"
      "v_mul_f32 v186, v186, v160\n"
      "v_mul_f32 v187, v187, v160\n"
      "v_cvt_pk_bf16_f32 v169, v186, v187\n"
      "v_and_or_b32 v169, v169, v7, s62\n"
      "v_mul_f32 v188, v188, v160\n"
      "v_mul_f32 v189, v189, v160\n"
      "v_cvt_pk_bf16_f32 v170, v188, v189\n"
      "v_and_or_b32 v170, v170, v7, s63\n"
      "v_mul_f32 v190, v190, v160\n"
      "v_mul_f32 v191, v191, v160\n"
      "v_cvt_pk_bf16_f32 v171, v190, v191\n"
      "v_and_or_b32 v171, v171, v7, s64\n"
      "buffer_store_dwordx4 v[168:171], v1, s[12:15], s47 offen offset:0 sc1\n"
      "v_mul_f32 v208, v208, v162\n"
      "v_mul_f32 v209, v209, v162\n"
      "v_cvt_pk_bf16_f32 v172, v208, v209\n"
      "v_and_or_b32 v172, v172, v7, s61\n"
      "v_mul_f32 v210, v210, v162\n"
      "v_mul_f32 v211, v211, v162\n"
      "v_cvt_pk_bf16_f32 v173, v210, v211\n"
      "v_and_or_b32 v173, v173, v7, s62\n"
      "v_mul_f32 v212, v212, v162\n"
      "v_mul_f32 v213, v213, v162\n"
      "v_cvt_pk_bf16_f32 v174, v212, v213\n"
      "v_and_or_b32 v174, v174, v7, s63\n"
      "v_mul_f32 v214, v214, v162\n"
      "v_mul_f32 v215, v215, v162\n"
      "v_cvt_pk_bf16_f32 v175, v214, v215\n"
      "v_and_or_b32 v175, v175, v7, s64\n"
      "buffer_store_dwordx4 v[172:175], v1, s[12:15], s47 offen offset:1024 sc1\n"
      "s_waitcnt vmcnt(9)\n"
      "v_xor_b32 v128, s57, v128\n"
      "v_xor_b32 v129, s58, v129\n"
      "v_xor_b32 v130, s59, v130\n"
      "v_xor_b32 v131, s60, v131\n"
      "s_waitcnt vmcnt(8)\n"
      "v_xor_b32 v132, s57, v132\n"
      "v_xor_b32 v133, s58, v133\n"
      "v_xor_b32 v134, s59, v134\n"
      "v_xor_b32 v135, s60, v135\n"
      "v_or3_b32 v4, v128, v129, v130\n"
      "v_or3_b32 v4, v4, v131, v132\n"
      "v_or3_b32 v4, v4, v133, v134\n"
      "v_or_b32 v4, v4, v135\n"
      "v_and_b32 v4, 0x80008000, v4\n"
      "v_cmp_ne_u32 vcc, 0, v4\n"
      "s_nop 1\n"
      "s_cbranch_vccnz LAr3\n"
      "LAo3:\n"
      "v_mfma_f32_32x32x16_bf16 v[112:127], v[64:67], v[128:131], 0\n"
      "ds_read_b64_tr_b16 v[80:81], v22 offset:40960\n"
      "ds_read_b64_tr_b16 v[82:83], v22 offset:41984\n"
      "s_add_u32 s3, s48, 32768\n"
      "buffer_load_dwordx4 v[160:163], v1, s[12:15], s3 offen sc1\n"
      "s_add_u32 s81, s81, 32768\n"
      "v_add_u32 v30, s81, v1\n"
      "v_xor_b32 v31, 16384, v30\n"
      "v_lshlrev_b32 v5, 4, v220\n"
      "v_lshl_add_u32 v13, v222, 10, v5\n"
      "s_add_u32 s81, s35, 65536\n"
      "s_waitcnt vmcnt(8)\n"
      "v_xor_b32 v136, s57, v136\n"
      "v_mfma_f32_32x32x16_bf16 v[96:111], v[68:71], v[128:131], 0\n"
      "ds_read_b64_tr_b16 v[84:85], v23 offset:40960\n"
      "ds_read_b64_tr_b16 v[86:87], v23 offset:41984\n"
      "s_add_u32 s3, s48, 36864\n"
      "buffer_load_dwordx4 v[164:167], v1, s[12:15], s3 offen sc1\n"
      "v_xor_b32 v137, s58, v137\n"
      "v_xor_b32 v138, s59, v138\n"
      "v_xor_b32 v139, s60, v139\n"
      "s_waitcnt vmcnt(8)\n"
      "v_xor_b32 v140, s57, v140\n"
      "v_xor_b32 v141, s58, v141\n"
      "v_xor_b32 v142, s59, v142\n"
      "v_mfma_f32_32x32x16_bf16 v[112:127], v[72:75], v[132:135], v[112:127]\n"
      "ds_read_b64_tr_b16 v[88:89], v22 offset:45056\n"
      "ds_read_b64_tr_b16 v[90:91], v22 offset:46080\n"
      "v_xor_b32 v143, s60, v143\n"
      "v_or3_b32 v4, v136, v137, v138\n"
      "v_or3_b32 v4, v4, v139, v140\n"
      "v_or3_b32 v4, v4, v141, v142\n"
      "v_or_b32 v4, v4, v143\n"
      "v_and_b32 v4, 0x80008000, v4\n"
      "v_mfma_f32_32x32x16_bf16 v[96:111], v[76:79], v[132:135], v[96:111]\n"
      "ds_read_b64_tr_b16 v[92:93], v23 offset:45056\n"
      "ds_read_b64_tr_b16 v[94:95], v23 offset:46080\n"
      "v_cmp_ne_u32 vcc, 0, v4\n"
      "s_nop 1\n"
      "s_cbranch_vccnz LAr4\n"
      "LAo4:\n"
      "s_waitcnt lgkmcnt(6)\n"
      "v_mfma_f32_32x32x16_bf16 v[112:127], v[80:83], v[136:139], v[112:127]\n"
      "ds_read_b64_tr_b16 v[64:65], v22 offset:49152\n"
      "ds_read_b64_tr_b16 v[66:67], v22 offset:50176\n"
      "s_add_u32 s3, s48, 40960\n"
      "buffer_load_dwordx4 v[168:171], v1, s[12:15], s3 offen sc1\n"
      "v_add_u32 v8, s81, v1\n"
      "v_xor_b32 v9, 4096, v8\n"
      "v_xor_b32 v10, 12288, v8\n"
      "s_lshl3_add_u32 s81, s30, s29\n"
      "s_lshl_b32 s81, s81, 11\n"
      "s_add_u32 s81, s81, 32768\n"
      "s_waitcnt vmcnt(8)\n"
      "v_xor_b32 v144, s57, v144\n"
      "v_xor_b32 v145, s58, v145\n"
      "s_waitcnt lgkmcnt(6)\n"
      "v_mfma_f32_32x32x16_bf16 v[96:111], v[84:87], v[136:139], v[96:111]\n"
      "ds_read_b64_tr_b16 v[68:69], v23 offset:49152\n"
      "ds_read_b64_tr_b16 v[70:71], v23 offset:50176\n"
      "s_add_u32 s3, s48, 45056\n"
      "buffer_load_dwordx4 v[172:175], v1, s[12:15], s3 offen sc1\n"
      "v_xor_b32 v146, s59, v146\n"
      "v_xor_b32 v147, s60, v147\n"
      "s_waitcnt vmcnt(8)\n"
      "v_xor_b32 v148, s57, v148\n"
      "v_xor_b32 v149, s58, v149\n"
      "v_xor_b32 v150, s59, v150\n"
      "v_xor_b32 v151, s60, v151\n"
      "s_waitcnt lgkmcnt(6)\n"
      "v_mfma_f32_32x32x16_bf16 v[112:127], v[88:91], v[140:143], v[112:127]\n"
      "ds_read_b64_tr_b16 v[72:73], v22 offset:53248\n"
      "ds_read_b64_tr_b16 v[74:75], v22 offset:54272\n"
      "v_or3_b32 v4, v144, v145, v146\n"
      "v_or3_b32 v4, v4, v147, v148\n"
      "v_or3_b32 v4, v4, v149, v150\n"
      "v_or_b32 v4, v4, v151\n"
      "v_and_b32 v4, 0x80008000, v4\n"
      "v_cmp_ne_u32 vcc, 0, v4\n"
      "s_waitcnt lgkmcnt(6)\n"
      "v_mfma_f32_32x32x16_bf16 v[96:111], v[92:95], v[140:143], v[96:111]\n"
      "ds_read_b64_tr_b16 v[76:77], v23 offset:53248\n"
      "ds_read_b64_tr_b16 v[78:79], v23 offset:54272\n"
      "s_cbranch_vccnz LAr5\n"
      "LAo5:\n"
      "s_waitcnt lgkmcnt(6)\n"
      "v_mfma_f32_32x32x16_bf16 v[112:127], v[64:67], v[144:147], v[112:127]\n"
      "ds_read_b64_tr_b16 v[80:81], v22 offset:57344\n"
      "ds_read_b64_tr_b16 v[82:83], v22 offset:58368\n"
      "s_add_u32 s3, s48, 49152\n"
      "buffer_load_dwordx4 v[176:179], v1, s[12:15], s3 offen sc1\n"
      "v_add_u32 v11, s81, v1\n"
      "v_xor_b32 v12, 16384, v11\n"
      "v_subrev_u32 v14, 16384, v22\n"
      "v_subrev_u32 v15, 16384, v23\n"
      "v_permlane32_swap_b32 v24, v26\n"
      "v_permlane32_swap_b32 v25, v27\n"
      "s_waitcnt lgkmcnt(6)\n"
      "v_mfma_f32_32x32x16_bf16 v[96:111], v[68:71], v[144:147], v[96:111]\n"
      "ds_read_b64_tr_b16 v[84:85], v23 offset:57344\n"
      "ds_read_b64_tr_b16 v[86:87], v23 offset:58368\n"
      "s_add_u32 s3, s48, 53248\n"
      "buffer_load_dwordx4 v[180:183], v1, s[12:15], s3 offen sc1\n"
      "s_waitcnt vmcnt(9)\n"
      "v_xor_b32 v152, s57, v152\n"
      "v_xor_b32 v153, s58, v153\n"
      "v_xor_b32 v154, s59, v154\n"
      "v_xor_b32 v155, s60, v155\n"
      "s_waitcnt vmcnt(8)\n"
      "v_xor_b32 v156, s57, v156\n"
      "v_xor_b32 v157, s58, v157\n"
      "s_waitcnt lgkmcnt(6)\n"
      "v_mfma_f32_32x32x16_bf16 v[112:127], v[72:75], v[148:151], v[112:127]\n"
      "ds_read_b64_tr_b16 v[88:89], v22 offset:61440\n"
      "ds_read_b64_tr_b16 v[90:91], v22 offset:62464\n"
      "v_xor_b32 v158, s59, v158\n"
      "v_xor_b32 v159, s60, v159\n"
      "v_or3_b32 v4, v152, v153, v154\n"
      "v_or3_b32 v4, v4, v155, v156\n"
      "v_or3_b32 v4, v4, v157, v158\n"
      "v_or_b32 v4, v4, v159\n"
      "s_waitcnt lgkmcnt(6)\n"
      "v_mfma_f32_32x32x16_bf16 v[96:111], v[76:79], v[148:151], v[96:111]\n"
      "ds_read_b64_tr_b16 v[92:93], v23 offset:61440\n"
      "ds_read_b64_tr_b16 v[94:95], v23 offset:62464\n"
      "v_and_b32 v4, 0x80008000, v4\n"
      "v_cmp_ne_u32 vcc, 0, v4\n"
      "s_nop 1\n"
      "s_cbranch_vccnz LAr6\n"
      "LAo6:\n"
      "s_waitcnt lgkmcnt(6)\n"
      "v_mfma_f32_32x32x16_bf16 v[112:127], v[80:83], v[152:155], v[112:127]\n"
      "ds_read_b64_tr_b16 v[64:65], v22 offset:0\n"
      "ds_read_b64_tr_b16 v[66:67], v22 offset:1024\n"
      "s_add_u32 s3, s48, 57344\n"
      "buffer_load_dwordx4 v[184:187], v1, s[12:15], s3 offen sc1\n"
      "v_lshlrev_b32 v48, 16, v24\n"
      "v_and_b32 v49, 0xffff0000, v24\n"
      "v_lshlrev_b32 v50, 16, v25\n"
      "v_and_b32 v51, 0xffff0000, v25\n"
      "v_lshlrev_b32 v52, 16, v26\n"
      "v_and_b32 v53, 0xffff0000, v26\n"
      "s_waitcnt lgkmcnt(6)\n"
      "v_mfma_f32_32x32x16_bf16 v[96:111], v[84:87], v[152:155], v[96:111]\n"
      "ds_read_b64_tr_b16 v[68:69], v23 offset:0\n"
      "ds_read_b64_tr_b16 v[70:71], v23 offset:1024\n"
      "s_add_u32 s3, s48, 61440\n"
      "buffer_load_dwordx4 v[188:191], v1, s[12:15], s3 offen sc1\n"
      "s_waitcnt vmcnt(7)\n"
      "v_xor_b32 v160, s57, v160\n"
      "v_xor_b32 v161, s58, v161\n"
      "v_xor_b32 v162, s59, v162\n"
      "v_xor_b32 v163, s60, v163\n"
      "s_waitcnt vmcnt(6)\n"
      "v_xor_b32 v164, s57, v164\n"
      "v_xor_b32 v165, s58, v165\n"
      "s_waitcnt lgkmcnt(6)\n"
      "v_mfma_f32_32x32x16_bf16 v[112:127], v[88:91], v[156:159], v[112:127]\n"
      "ds_read_b64_tr_b16 v[72:73], v22 offset:4096\n"
      "ds_read_b64_tr_b16 v[74:75], v22 offset:5120\n"
      "v_xor_b32 v166, s59, v166\n"
      "v_xor_b32 v167, s60, v167\n"
      "v_or3_b32 v4, v160, v161, v162\n"
      "v_or3_b32 v4, v4, v163, v164\n"
      "v_or3_b32 v4, v4, v165, v166\n"
      "v_or_b32 v4, v4, v167\n"
      "s_waitcnt lgkmcnt(6)\n"
      "v_mfma_f32_32x32x16_bf16 v[96:111], v[92:95], v[156:159], v[96:111]\n"
      "ds_read_b64_tr_b16 v[76:77], v23 offset:4096\n"
      "ds_read_b64_tr_b16 v[78:79], v23 offset:5120\n"
      "v_and_b32 v4, 0x80008000, v4\n"
      "v_cmp_ne_u32 vcc, 0, v4\n"
      "s_nop 1\n"
      "s_cbranch_vccnz LAr7\n"
      "LAo7:\n"
      "s_waitcnt lgkmcnt(6)\n"
      "v_mfma_f32_32x32x16_bf16 v[112:127], v[64:67], v[160:163], v[112:127]\n"
      "ds_read_b64_tr_b16 v[80:81], v22 offset:8192\n"
      "ds_read_b64_tr_b16 v[82:83], v22 offset:9216\n"
      "s_add_u32 s3, s48, 65536\n"
      "buffer_load_dwordx4 v[192:195], v1, s[12:15], s3 offen sc1\n"
      "v_lshlrev_b32 v54, 16, v27\n"
      "v_and_b32 v55, 0xffff0000, v27\n"
      "s_waitcnt vmcnt(6)\n"
      "v_xor_b32 v168, s57, v168\n"
      "v_xor_b32 v169, s58, v169\n"
      "v_xor_b32 v170, s59, v170\n"
      "v_xor_b32 v171, s60, v171\n"
      "s_waitcnt lgkmcnt(6)\n"
      "v_mfma_f32_32x32x16_bf16 v[96:111], v[68:71], v[160:163], v[96:111]\n"
      "ds_read_b64_tr_b16 v[84:85], v23 offset:8192\n"
      "ds_read_b64_tr_b16 v[86:87], v23 offset:9216\n"
      "s_add_u32 s3, s48, 67584\n"
      "buffer_load_dwordx4 v[196:199], v1, s[12:15], s3 offen sc1\n"
      "s_waitcnt vmcnt(6)\n"
      "v_xor_b32 v172, s57, v172\n"
      "v_xor_b32 v173, s58, v173\n"
      "v_xor_b32 v174, s59, v174\n"
      "v_xor_b32 v175, s60, v175\n"
      "v_or3_b32 v4, v168, v169, v170\n"
      "v_or3_b32 v4, v4, v171, v172\n"
      "s_waitcnt lgkmcnt(6)\n"
      "v_mfma_f32_32x32x16_bf16 v[112:127], v[72:75], v[164:167], v[112:127]\n"
      "ds_read_b64_tr_b16 v[88:89], v22 offset:12288\n"
      "ds_read_b64_tr_b16 v[90:91], v22 offset:13312\n"
      "v_or3_b32 v4, v4, v173, v174\n"
      "v_or_b32 v4, v4, v175\n"
      "v_and_b32 v4, 0x80008000, v4\n"
      "v_cmp_ne_u32 vcc, 0, v4\n"
      "s_waitcnt lgkmcnt(6)\n"
      "v_mfma_f32_32x32x16_bf16 v[96:111], v[76:79], v[164:167], v[96:111]\n"
      "ds_read_b64_tr_b16 v[92:93], v23 offset:12288\n"
      "ds_read_b64_tr_b16 v[94:95], v23 offset:13312\n"
      "s_cbranch_vccnz LAr8\n"
      "LAo8:\n"
      "s_waitcnt lgkmcnt(6)\n"
      "v_mfma_f32_32x32x16_bf16 v[112:127], v[80:83], v[168:171], v[112:127]\n"
      "ds_read_b64_tr_b16 v[64:65], v22 offset:16384\n"
      "ds_read_b64_tr_b16 v[66:67], v22 offset:17408\n"
      "buffer_load_dwordx4 v[200:203], v1, s[12:15], s50 offen sc1\n"
      "s_waitcnt lgkmcnt(6)\n"
      "v_mfma_f32_32x32x16_bf16 v[96:111], v[84:87], v[168:171], v[96:111]\n"
      "ds_read_b64_tr_b16 v[68:69], v23 offset:16384\n"
      "ds_read_b64_tr_b16 v[70:71], v23 offset:17408\n"
      "s_add_u32 s3, s50, 4096\n"
      "buffer_load_dwordx4 v[204:207], v1, s[12:15], s3 offen sc1\n"
      "s_waitcnt vmcnt(7)\n"
      "v_xor_b32 v176, s57, v176\n"
      "v_xor_b32 v177, s58, v177\n"
      "v_xor_b32 v178, s59, v178\n"
      "v_xor_b32 v179, s60, v179\n"
      "s_waitcnt vmcnt(6)\n"
      "v_xor_b32 v180, s57, v180\n"
      "v_xor_b32 v181, s58, v181\n"
      "s_waitcnt lgkmcnt(6)\n"
      "v_mfma_f32_32x32x16_bf16 v[112:127], v[88:91], v[172:175], v[112:127]\n"
      "ds_read_b64_tr_b16 v[72:73], v22 offset:20480\n"
      "ds_read_b64_tr_b16 v[74:75], v22 offset:21504\n"
      "v_xor_b32 v182, s59, v182\n"
      "v_xor_b32 v183, s60, v183\n"
      "v_or3_b32 v4, v176, v177, v178\n"
      "v_or3_b32 v4, v4, v179, v180\n"
      "v_or3_b32 v4, v4, v181, v182\n"
      "v_or_b32 v4, v4, v183\n"
      "s_waitcnt lgkmcnt(6)\n"
      "v_mfma_f32_32x32x16_bf16 v[96:111], v[92:95], v[172:175], v[96:111]\n"
      "ds_read_b64_tr_b16 v[76:77], v23 offset:20480\n"
      "ds_read_b64_tr_b16 v[78:79], v23 offset:21504\n"
      "v_and_b32 v4, 0x80008000, v4\n"
      "v_cmp_ne_u32 vcc, 0, v4\n"
      "s_nop 1\n"
      "s_cbranch_vccnz LAr9\n"
      "LAo9:\n"
      "s_waitcnt lgkmcnt(6)\n"
      "v_mfma_f32_32x32x16_bf16 v[112:127], v[64:67], v[176:179], v[112:127]\n"
      "ds_read_b64_tr_b16 v[80:81], v22 offset:24576\n"
      "ds_read_b64_tr_b16 v[82:83], v22 offset:25600\n"
      "s_add_u32 s3, s50, 8192\n"
      "buffer_load_dwordx4 v[208:211], v1, s[12:15], s3 offen sc1\n"
      "s_waitcnt lgkmcnt(6)\n"
      "v_mfma_f32_32x32x16_bf16 v[96:111], v[68:71], v[176:179], v[96:111]\n"
      "ds_read_b64_tr_b16 v[84:85], v23 offset:24576\n"
      "ds_read_b64_tr_b16 v[86:87], v23 offset:25600\n"
      "s_add_u32 s3, s50, 12288\n"
      "buffer_load_dwordx4 v[212:215], v1, s[12:15], s3 offen sc1\n"
      "s_waitcnt vmcnt(7)\n"
      "v_xor_b32 v184, s57, v184\n"
      "v_xor_b32 v185, s58, v185\n"
      "v_xor_b32 v186, s59, v186\n"
      "v_xor_b32 v187, s60, v187\n"
      "s_waitcnt vmcnt(6)\n"
      "v_xor_b32 v188, s57, v188\n"
      "v_xor_b32 v189, s58, v189\n"
      "s_waitcnt lgkmcnt(6)\n"
      "v_mfma_f32_32x32x16_bf16 v[112:127], v[72:75], v[180:183], v[112:127]\n"
      "ds_read_b64_tr_b16 v[88:89], v22 offset:28672\n"
      "ds_read_b64_tr_b16 v[90:91], v22 offset:29696\n"
      "v_xor_b32 v190, s59, v190\n"
      "v_xor_b32 v191, s60, v191\n"
      "v_or3_b32 v4, v184, v185, v186\n"
      "v_or3_b32 v4, v4, v187, v188\n"
      "v_or3_b32 v4, v4, v189, v190\n"
      "v_or_b32 v4, v4, v191\n"
      "s_waitcnt lgkmcnt(6)\n"
      "v_mfma_f32_32x32x16_bf16 v[96:111], v[76:79], v[180:183], v[96:111]\n"
      "ds_read_b64_tr_b16 v[92:93], v23 offset:28672\n"
      "ds_read_b64_tr_b16 v[94:95], v23 offset:29696\n"
      "v_and_b32 v4, 0x80008000, v4\n"
      "v_cmp_ne_u32 vcc, 0, v4\n"
      "s_nop 1\n"
      "s_cbranch_vccnz LAr10\n"
      "LAo10:\n"
      "s_waitcnt lgkmcnt(6)\n"
      "v_mfma_f32_32x32x16_bf16 v[112:127], v[80:83], v[184:187], v[112:127]\n"
      "ds_read_b64_tr_b16 v[64:65], v14 offset:0\n"
      "ds_read_b64_tr_b16 v[66:67], v14 offset:1024\n"
      "s_add_u32 s3, s50, 16384\n"
      "buffer_load_dwordx4 v[216:219], v1, s[12:15], s3 offen sc1\n"
      "s_waitcnt lgkmcnt(6)\n"
      "v_mfma_f32_32x32x16_bf16 v[96:111], v[84:87], v[184:187], v[96:111]\n"
      "ds_read_b64_tr_b16 v[68:69], v15 offset:0\n"
      "ds_read_b64_tr_b16 v[70:71], v15 offset:1024\n"
      "s_add_u32 s3, s50, 20480\n"
      "buffer_load_dwordx4 v[220:223], v1, s[12:15], s3 offen sc1\n"
      "s_waitcnt vmcnt(7)\n"
      "v_xor_b32 v192, s57, v192\n"
      "v_xor_b32 v193, s58, v193\n"
      "v_xor_b32 v194, s59, v194\n"
      "v_xor_b32 v195, s60, v195\n"
      "s_waitcnt vmcnt(6)\n"
      "v_xor_b32 v196, s57, v196\n"
      "v_xor_b32 v197, s58, v197\n"
      "s_waitcnt lgkmcnt(6)\n"
      "v_mfma_f32_32x32x16_bf16 v[112:127], v[88:91], v[188:191], v[112:127]\n"
      "ds_read_b64_tr_b16 v[72:73], v14 offset:4096\n"
      "ds_read_b64_tr_b16 v[74:75], v14 offset:5120\n"
      "v_xor_b32 v198, s59, v198\n"
      "v_xor_b32 v199, s60, v199\n"
      "v_or3_b32 v4, v192, v193, v194\n"
      "v_or3_b32 v4, v4, v195, v196\n"
      "v_or3_b32 v4, v4, v197, v198\n"
      "v_or_b32 v4, v4, v199\n"
      "s_waitcnt lgkmcnt(6)\n"
      "v_mfma_f32_32x32x16_bf16 v[96:111], v[92:95], v[188:191], v[96:111]\n"
      "ds_read_b64_tr_b16 v[76:77], v15 offset:4096\n"
      "ds_read_b64_tr_b16 v[78:79], v15 offset:5120\n"
      "v_and_b32 v4, 0x80008000, v4\n"
      "v_cmp_ne_u32 vcc, 0, v4\n"
      "s_nop 1\n"
      "s_cbranch_vccnz LAr11\n"
      "LAo11:\n"
      "s_waitcnt lgkmcnt(4)\n"
      "v_mfma_f32_32x32x16_bf16 v[96:111], v[68:71], v[192:195], v[96:111]\n"
      "s_waitcnt lgkmcnt(0)\n"
      "v_mfma_f32_32x32x16_bf16 v[96:111], v[76:79], v[196:199], v[96:111]\n"
      "s_add_u32 s3, s50, 24576\n"
      "buffer_load_dwordx4 v[224:227], v1, s[12:15], s3 offen sc1\n"
      "s_add_u32 s3, s50, 28672\n"
      "buffer_load_dwordx4 v[228:231], v1, s[12:15], s3 offen sc1\n"
      "v_mfma_f32_32x32x16_bf16 v[112:127], v[64:67], v[192:195], v[112:127]\n"
      "v_mfma_f32_32x32x16_bf16 v[112:127], v[72:75], v[196:199], v[112:127]\n"
      "s_cmp_lt_u32 s29, 2\n"
      "s_cbranch_scc1 LAf0\n"
      "s_nop 3\n"
      "ds_write_b128 v30, v[96:99] offset:0\n"
      "ds_write_b128 v30, v[100:103] offset:1024\n"
      "ds_write_b128 v30, v[104:107] offset:2048\n"
      "ds_write_b128 v30, v[108:111] offset:3072\n"
      "LAf0:\n"
      "s_cmp_lt_u32 s29, 2\n"
      "s_waitcnt lgkmcnt(0)\n"
      "s_barrier\n"
      "s_cbranch_scc1 LAf1\n"
      "ds_read_b128 v[160:163], v31 offset:0\n"
      "ds_read_b128 v[164:167], v31 offset:1024\n"
      "ds_read_b128 v[168:171], v31 offset:2048\n"
      "ds_read_b128 v[172:175], v31 offset:3072\n"
      "s_waitcnt lgkmcnt(3)\n"
      "s_nop 0\n"
      "v_add_f32 v112, v112, v160\n"
      "v_add_f32 v113, v113, v161\n"
      "v_cvt_pk_bf16_f32 v160, v112, v113\n"
      "v_and_or_b32 v160, v160, v7, s61\n"
      "v_add_f32 v114, v114, v162\n"
      "v_add_f32 v115, v115, v163\n"
      "v_cvt_pk_bf16_f32 v161, v114, v115\n"
      "v_and_or_b32 v161, v161, v7, s62\n"
      "s_waitcnt lgkmcnt(2)\n"
      "v_add_f32 v116, v116, v164\n"
      "v_add_f32 v117, v117, v165\n"
      "v_cvt_pk_bf16_f32 v162, v116, v117\n"
      "v_and_or_b32 v162, v162, v7, s63\n"
      "v_add_f32 v118, v118, v166\n"
      "v_add_f32 v119, v119, v167\n"
      "v_cvt_pk_bf16_f32 v163, v118, v119\n"
      "v_and_or_b32 v163, v163, v7, s64\n"
      "buffer_store_dwordx4 v[160:163], v1, s[12:15], s53 offen offset:0 sc1\n"
      "s_waitcnt lgkmcnt(1)\n"
      "v_add_f32 v120, v120, v168\n"
      "v_add_f32 v121, v121, v169\n"
      "v_cvt_pk_bf16_f32 v164, v120, v121\n"
      "v_and_or_b32 v164, v164, v7, s61\n"
      "v_add_f32 v122, v122, v170\n"
      "v_add_f32 v123, v123, v171\n"
      "v_cvt_pk_bf16_f32 v165, v122, v123\n"
      "v_and_or_b32 v165, v165, v7, s62\n"
      "s_waitcnt lgkmcnt(0)\n"
      "v_add_f32 v124, v124, v172\n"
      "v_add_f32 v125, v125, v173\n"
      "v_cvt_pk_bf16_f32 v166, v124, v125\n"
      "v_and_or_b32 v166, v166, v7, s63\n"
      "v_add_f32 v126, v126, v174\n"
      "v_add_f32 v127, v127, v175\n"
      "v_cvt_pk_bf16_f32 v167, v126, v127\n"
      "v_and_or_b32 v167, v167, v7, s64\n"
      "buffer_store_dwordx4 v[164:167], v1, s[12:15], s53 offen offset:1024 sc1\n"
      "LAf1:\n"
      "ds_read_b64_tr_b16 v[80:81], v20 offset:0\n"
      "ds_read_b64_tr_b16 v[82:83], v20 offset:1024\n"
      "ds_read_b64_tr_b16 v[84:85], v21 offset:0\n"
      "ds_read_b64_tr_b16 v[86:87], v21 offset:1024\n"
      "ds_read_b64_tr_b16 v[88:89], v20 offset:8192\n"
      "ds_read_b64_tr_b16 v[90:91], v20 offset:9216\n"
      "ds_read_b64_tr_b16 v[92:93], v21 offset:8192\n"
      "ds_read_b64_tr_b16 v[94:95], v21 offset:9216\n"
      "s_waitcnt vmcnt(7)\n"
      "v_xor_b32 v200, s61, v200\n"
      "v_xor_b32 v201, s62, v201\n"
      "v_xor_b32 v202, s63, v202\n"
      "v_xor_b32 v203, s64, v203\n"
      "s_waitcnt vmcnt(6)\n"
      "v_xor_b32 v204, s61, v204\n"
      "v_xor_b32 v205, s62, v205\n"
      "v_xor_b32 v206, s63, v206\n"
      "v_xor_b32 v207, s64, v207\n"
      "v_or3_b32 v4, v200, v201, v202\n"
      "v_or3_b32 v4, v4, v203, v204\n"
      "v_or3_b32 v4, v4, v205, v206\n"
      "v_or_b32 v4, v4, v207\n"
      "v_and_b32 v4, 0x10001, v4\n"
      "v_cmp_ne_u32 vcc, 0, v4\n"
      "s_nop 1\n"
      "s_cbranch_vccnz LAr12\n"
      "LAo12:\n"
      "s_cmp_lt_u32 s29, 2\n"
      "s_waitcnt lgkmcnt(0)\n"
      "s_cbranch_scc1 LAz0\n"
      "v_mfma_f32_32x32x16_bf16 v[112:127], v[80:83], v[200:203], 0\n"
      "v_mfma_f32_32x32x16_bf16 v[96:111], v[84:87], v[200:203], 0\n"
      "LAz0:\n"
      "s_cbranch_scc0 LAz1\n"
      "v_mfma_f32_32x32x16_bf16 v[112:127], v[80:83], v[200:203], v[112:127]\n"
      "v_mfma_f32_32x32x16_bf16 v[96:111], v[84:87], v[200:203], v[96:111]\n"
      "LAz1:\n"
      "v_mfma_f32_32x32x16_bf16 v[112:127], v[88:91], v[204:207], v[112:127]\n"
      "ds_read_b64_tr_b16 v[64:65], v20 offset:16384\n"
      "ds_read_b64_tr_b16 v[66:67], v20 offset:17408\n"
      "ds_read_b64_tr_b16 v[68:69], v21 offset:16384\n"
      "ds_read_b64_tr_b16 v[70:71], v21 offset:17408\n"
      "v_mfma_f32_32x32x16_bf16 v[96:111], v[92:95], v[204:207], v[96:111]\n"
      "ds_read_b64_tr_b16 v[72:73], v20 offset:24576\n"
      "ds_read_b64_tr_b16 v[74:75], v20 offset:25600\n"
      "ds_read_b64_tr_b16 v[76:77], v21 offset:24576\n"
      "ds_read_b64_tr_b16 v[78:79], v21 offset:25600\n"
      "s_waitcnt vmcnt(5)\n"
      "v_xor_b32 v208, s61, v208\n"
      "v_xor_b32 v209, s62, v209\n"
      "v_xor_b32 v210, s63, v210\n"
      "v_xor_b32 v211, s64, v211\n"
      "s_waitcnt vmcnt(4)\n"
      "v_xor_b32 v212, s61, v212\n"
      "v_xor_b32 v213, s62, v213\n"
      "v_xor_b32 v214, s63, v214\n"
      "v_xor_b32 v215, s64, v215\n"
      "v_or3_b32 v4, v208, v209, v210\n"
      "v_or3_b32 v4, v4, v211, v212\n"
      "v_or3_b32 v4, v4, v213, v214\n"
      "v_or_b32 v4, v4, v215\n"
      "v_and_b32 v4, 0x10001, v4\n"
      "v_cmp_ne_u32 vcc, 0, v4\n"
      "s_nop 1\n"
      "s_cbranch_vccnz LAr13\n"
      "LAo13:\n"
      "s_waitcnt lgkmcnt(6)\n"
      "v_mfma_f32_32x32x16_bf16 v[112:127], v[64:67], v[208:211], v[112:127]\n"
      "ds_read_b64_tr_b16 v[80:81], v28 offset:0\n"
      "ds_read_b64_tr_b16 v[82:83], v28 offset:1024\n"
      "s_waitcnt lgkmcnt(6)\n"
      "v_mfma_f32_32x32x16_bf16 v[96:111], v[68:71], v[208:211], v[96:111]\n"
      "ds_read_b64_tr_b16 v[84:85], v29 offset:0\n"
      "ds_read_b64_tr_b16 v[86:87], v29 offset:1024\n"
      "s_waitcnt vmcnt(3)\n"
      "v_xor_b32 v216, s61, v216\n"
      "v_xor_b32 v217, s62, v217\n"
      "v_xor_b32 v218, s63, v218\n"
      "v_xor_b32 v219, s64, v219\n"
      "s_waitcnt vmcnt(2)\n"
      "v_xor_b32 v220, s61, v220\n"
      "v_xor_b32 v221, s62, v221\n"
      "s_waitcnt lgkmcnt(6)\n"
      "v_mfma_f32_32x32x16_bf16 v[112:127], v[72:75], v[212:215], v[112:127]\n"
      "ds_read_b64_tr_b16 v[88:89], v28 offset:8192\n"
      "ds_read_b64_tr_b16 v[90:91], v28 offset:9216\n"
      "v_xor_b32 v222, s63, v222\n"
      "v_xor_b32 v223, s64, v223\n"
      "v_or3_b32 v4, v216, v217, v218\n"
      "v_or3_b32 v4, v4, v219, v220\n"
      "v_or3_b32 v4, v4, v221, v222\n"
      "v_or_b32 v4, v4, v223\n"
      "s_waitcnt lgkmcnt(6)\n"
      "v_mfma_f32_32x32x16_bf16 v[96:111], v[76:79], v[212:215], v[96:111]\n"
      "ds_read_b64_tr_b16 v[92:93], v29 offset:8192\n"
      "ds_read_b64_tr_b16 v[94:95], v29 offset:9216\n"
      "v_and_b32 v4, 0x10001, v4\n"
      "v_cmp_ne_u32 vcc, 0, v4\n"
      "s_nop 1\n"
      "s_cbranch_vccnz LAr14\n"
      "LAo14:\n"
      "s_waitcnt lgkmcnt(6)\n"
      "v_mfma_f32_32x32x16_bf16 v[112:127], v[80:83], v[216:219], v[112:127]\n"
      "ds_read_b64_tr_b16 v[64:65], v28 offset:16384\n"
      "ds_read_b64_tr_b16 v[66:67], v28 offset:17408\n"
      "s_waitcnt lgkmcnt(6)\n"
      "v_mfma_f32_32x32x16_bf16 v[96:111], v[84:87], v[216:219], v[96:111]\n"
      "ds_read_b64_tr_b16 v[68:69], v29 offset:16384\n"
      "ds_read_b64_tr_b16 v[70:71], v29 offset:17408\n"
      "s_waitcnt vmcnt(1)\n"
      "v_xor_b32 v224, s61, v224\n"
      "v_xor_b32 v225, s62, v225\n"
      "v_xor_b32 v226, s63, v226\n"
      "v_xor_b32 v227, s64, v227\n"
      "s_waitcnt vmcnt(0)\n"
      "v_xor_b32 v228, s61, v228\n"
      "v_xor_b32 v229, s62, v229\n"
      "s_waitcnt lgkmcnt(6)\n"
      "v_mfma_f32_32x32x16_bf16 v[112:127], v[88:91], v[220:223], v[112:127]\n"
      "ds_read_b64_tr_b16 v[72:73], v28 offset:24576\n"
      "ds_read_b64_tr_b16 v[74:75], v28 offset:25600\n"
      "v_xor_b32 v230, s63, v230\n"
      "v_xor_b32 v231, s64, v231\n"
      "v_or3_b32 v4, v224, v225, v226\n"
      "v_or3_b32 v4, v4, v227, v228\n"
      "v_or3_b32 v4, v4, v229, v230\n"
      "v_or_b32 v4, v4, v231\n"
      "s_waitcnt lgkmcnt(6)\n"
      "v_mfma_f32_32x32x16_bf16 v[96:111], v[92:95], v[220:223], v[96:111]\n"
      "ds_read_b64_tr_b16 v[76:77], v29 offset:24576\n"
      "ds_read_b64_tr_b16 v[78:79], v29 offset:25600\n"
      "v_and_b32 v4, 0x10001, v4\n"
      "v_cmp_ne_u32 vcc, 0, v4\n"
      "s_nop 1\n"
      "s_cbranch_vccnz LAr15\n"
      "LAo15:\n"
      "s_waitcnt lgkmcnt(4)\n"
      "v_mfma_f32_32x32x16_bf16 v[96:111], v[68:71], v[224:227], v[96:111]\n"
      "s_waitcnt lgkmcnt(0)\n"
      "v_mfma_f32_32x32x16_bf16 v[96:111], v[76:79], v[228:231], v[96:111]\n"
      "buffer_load_dwordx4 v[248:251], v1, s[12:15], s54 offen sc1\n"
      "v_mfma_f32_32x32x16_bf16 v[112:127], v[64:67], v[224:227], v[112:127]\n"
      "v_mfma_f32_32x32x16_bf16 v[112:127], v[72:75], v[228:231], v[112:127]\n"
      "s_nop 8\n"
      "ds_write_b128 v11, v[96:99] offset:0\n"
      "ds_write_b128 v11, v[100:103] offset:1024\n"
      "ds_write_b128 v8, v[104:107] offset:16384\n"
      "ds_write_b128 v8, v[108:111] offset:17408\n"
      "ds_write_b128 v8, v[120:123] offset:0\n"
      "ds_write_b128 v8, v[124:127] offset:1024\n"
      "v_add_f32 v48, v48, v112\n"
      "v_add_f32 v49, v49, v113\n"
      "v_add_f32 v50, v50, v114\n"
      "v_add_f32 v51, v51, v115\n"
      "v_add_f32 v52, v52, v116\n"
      "v_add_f32 v53, v53, v117\n"
      "v_add_f32 v54, v54, v118\n"
      "v_add_f32 v55, v55, v119\n"
      "s_waitcnt lgkmcnt(0)\n"
      "s_barrier\n"
      "ds_read_b128 v[64:67], v9 offset:0\n"
      "ds_read_b128 v[68:71], v9 offset:1024\n"
      "ds_read_b128 v[72:75], v12 offset:0\n"
      "ds_read_b128 v[76:79], v12 offset:1024\n"
      "ds_read_b128 v[80:83], v10 offset:16384\n"
      "ds_read_b128 v[84:87], v10 offset:17408\n"
      "s_waitcnt lgkmcnt(5)\n"
      "v_add_f32 v48, v48, v64\n"
      "v_add_f32 v49, v49, v65\n"
      "v_add_f32 v50, v50, v66\n"
      "v_add_f32 v51, v51, v67\n"
      "s_waitcnt lgkmcnt(4)\n"
      "v_add_f32 v52, v52, v68\n"
      "v_add_f32 v53, v53, v69\n"
      "v_add_f32 v54, v54, v70\n"
      "v_add_f32 v55, v55, v71\n"
      "s_waitcnt lgkmcnt(3)\n"
      "v_add_f32 v48, v48, v72\n"
      "v_add_f32 v49, v49, v73\n"
      "v_add_f32 v50, v50, v74\n"
      "v_add_f32 v51, v51, v75\n"
      "s_waitcnt lgkmcnt(2)\n"
      "v_add_f32 v52, v52, v76\n"
      "v_add_f32 v53, v53, v77\n"
      "v_add_f32 v54, v54, v78\n"
      "v_add_f32 v55, v55, v79\n"
      "s_waitcnt lgkmcnt(1)\n"
      "v_add_f32 v48, v48, v80\n"
      "v_add_f32 v49, v49, v81\n"
      "v_add_f32 v50, v50, v82\n"
      "v_add_f32 v51, v51, v83\n"
      "s_waitcnt lgkmcnt(0)\n"
      "v_add_f32 v52, v52, v84\n"
      "v_add_f32 v53, v53, v85\n"
      "v_add_f32 v54, v54, v86\n"
      "v_add_f32 v55, v55, v87\n"
      "s_waitcnt vmcnt(0)\n"
      "v_xor_b32 v248, s61, v248\n"
      "v_xor_b32 v249, s62, v249\n"
      "v_xor_b32 v250, s63, v250\n"
      "v_xor_b32 v251, s64, v251\n"
      "v_or3_b32 v4, v248, v249, v250\n"
      "v_or_b32 v4, v4, v251\n"
      "v_and_b32 v4, 0x10001, v4\n"
      "v_cmp_ne_u32 vcc, 0, v4\n"
      "s_nop 1\n"
      "s_cbranch_vccnz LAr16\n"
      "LAo16:\n"
      "v_lshlrev_b32 v4, 16, v248\n"
      "v_and_b32 v5, 0xffff0000, v248\n"
      "v_add_f32 v48, v48, v4\n"
      "v_add_f32 v49, v49, v5\n"
      "v_lshlrev_b32 v4, 16, v249\n"
      "v_and_b32 v5, 0xffff0000, v249\n"
      "v_add_f32 v50, v50, v4\n"
      "v_add_f32 v51, v51, v5\n"
      "v_lshlrev_b32 v4, 16, v250\n"
      "v_and_b32 v5, 0xffff0000, v250\n"
      "v_add_f32 v52, v52, v4\n"
      "v_add_f32 v53, v53, v5\n"
      "v_lshlrev_b32 v4, 16, v251\n"
      "v_and_b32 v5, 0xffff0000, v251\n"
      "v_add_f32 v54, v54, v4\n"
      "v_add_f32 v55, v55, v5\n"
      "v_cvt_pk_bf16_f32 v48, v48, v49\n"
      "v_cvt_pk_bf16_f32 v49, v50, v51\n"
      "v_cvt_pk_bf16_f32 v50, v52, v53\n"
      "v_cvt_pk_bf16_f32 v51, v54, v55\n"
      "s_nop 0\n"
      "v_permlane32_swap_b32 v48, v50\n"
      "v_permlane32_swap_b32 v49, v51\n"
      "buffer_store_dwordx4 v[48:51], v13, s[24:27], s51 offen\n"
      "s_endpgm\n"
      "LAr1:\n"
      "s_sub_u32 s67, s67, 1\n"
      "s_cbranch_scc1 LAr1_x\n"
      "s_sleep 16\n"
      "buffer_load_dwordx4 v[32:35], v60, s[12:15], s56 offen sc1\n"
      "s_waitcnt vmcnt(0)\n"
      "v_xor_b32 v4, s65, v34\n"
      "v_bitop3_b32 v4, v35, s65, v4 bitop3:0xbe\n"
      "v_cmp_ne_u32 vcc, 0, v4\n"
      "s_nop 1\n"
      "s_cbranch_vccnz LAr1\n"
      "s_branch LAo1\n"
      "LAr1_x:\n"
      "s_mov_b32 s67, 0\n"
      "s_branch LAo1\n"
      "LAr2:\n"
      "s_sub_u32 s67, s67, 1\n"
      "s_cbranch_scc1 LAr2_x\n"
      "s_sleep 16\n"
      "s_add_u32 s3, s66, 131072\n"
      "buffer_load_dwordx4 v[160:163], v16, s[12:15], s3 offen sc1\n"
      "s_add_u32 s3, s66, 135168\n"
      "buffer_load_dwordx4 v[164:167], v16, s[12:15], s3 offen sc1\n"
      "s_add_u32 s3, s66, 139264\n"
      "buffer_load_dwordx4 v[168:171], v16, s[12:15], s3 offen sc1\n"
      "s_add_u32 s3, s66, 143360\n"
      "buffer_load_dwordx4 v[172:175], v16, s[12:15], s3 offen sc1\n"
      "buffer_load_dwordx4 v[176:179], v17, s[12:15], s69 offen sc1\n"
      "s_waitcnt vmcnt(0)\n"
      "v_xor_b32 v160, s61, v160\n"
      "v_xor_b32 v161, s62, v161\n"
      "v_xor_b32 v162, s63, v162\n"
      "v_xor_b32 v163, s64, v163\n"
      "v_xor_b32 v164, s61, v164\n"
      "v_xor_b32 v165, s62, v165\n"
      "v_xor_b32 v166, s63, v166\n"
      "v_xor_b32 v167, s64, v167\n"
      "v_xor_b32 v168, s61, v168\n"
      "v_xor_b32 v169, s62, v169\n"
      "v_xor_b32 v170, s63, v170\n"
      "v_xor_b32 v171, s64, v171\n"
      "v_xor_b32 v172, s61, v172\n"
      "v_xor_b32 v173, s62, v173\n"
      "v_xor_b32 v174, s63, v174\n"
      "v_xor_b32 v175, s64, v175\n"
      "v_xor_b32 v176, s61, v176\n"
      "v_xor_b32 v177, s62, v177\n"
      "v_xor_b32 v178, s63, v178\n"
      "v_xor_b32 v179, s64, v179\n"
      "v_or3_b32 v4, v160, v161, v162\n"
      "v_or3_b32 v4, v4, v163, v164\n"
      "v_or3_b32 v4, v4, v165, v166\n"
      "v_or3_b32 v4, v4, v167, v168\n"
      "v_or3_b32 v4, v4, v169, v170\n"
      "v_or3_b32 v4, v4, v171, v172\n"
      "v_or3_b32 v4, v4, v173, v174\n"
      "v_or3_b32 v4, v4, v175, v176\n"
      "v_or3_b32 v4, v4, v177, v178\n"
      "v_or_b32 v4, v4, v179\n"
      "v_and_b32 v4, 0x10001, v4\n"
      "v_cmp_ne_u32 vcc, 0, v4\n"
      "s_nop 1\n"
      "s_cbranch_vccnz LAr2\n"
      "s_branch LAo2\n"
      "LAr2_x:\n"
      "s_mov_b32 s67, 0\n"
      "s_branch LAo2\n"
      "LAr3:\n"
      "s_sub_u32 s67, s67, 1\n"
      "s_cbranch_scc1 LAr3_x\n"
      "s_sleep 16\n"
      "buffer_load_dwordx4 v[128:131], v1, s[12:15], s48 offen sc1\n"
      "s_add_u32 s3, s48, 4096\n"
      "buffer_load_dwordx4 v[132:135], v1, s[12:15], s3 offen sc1\n"
      "s_waitcnt vmcnt(0)\n"
      "v_xor_b32 v128, s57, v128\n"
      "v_xor_b32 v129, s58, v129\n"
      "v_xor_b32 v130, s59, v130\n"
      "v_xor_b32 v131, s60, v131\n"
      "v_xor_b32 v132, s57, v132\n"
      "v_xor_b32 v133, s58, v133\n"
      "v_xor_b32 v134, s59, v134\n"
      "v_xor_b32 v135, s60, v135\n"
      "v_or3_b32 v4, v128, v129, v130\n"
      "v_or3_b32 v4, v4, v131, v132\n"
      "v_or3_b32 v4, v4, v133, v134\n"
      "v_or_b32 v4, v4, v135\n"
      "v_and_b32 v4, 0x80008000, v4\n"
      "v_cmp_ne_u32 vcc, 0, v4\n"
      "s_nop 1\n"
      "s_cbranch_vccnz LAr3\n"
      "s_branch LAo3\n"
      "LAr3_x:\n"
      "s_mov_b32 s67, 0\n"
      "s_branch LAo3\n"
      "LAr4:\n"
      "s_sub_u32 s67, s67, 1\n"
      "s_cbranch_scc1 LAr4_x\n"
      "s_sleep 16\n"
      "s_add_u32 s3, s48, 8192\n"
      "buffer_load_dwordx4 v[136:139], v1, s[12:15], s3 offen sc1\n"
      "s_add_u32 s3, s48, 12288\n"
      "buffer_load_dwordx4 v[140:143], v1, s[12:15], s3 offen sc1\n"
      "s_waitcnt vmcnt(0)\n"
      "v_xor_b32 v136, s57, v136\n"
      "v_xor_b32 v137, s58, v137\n"
      "v_xor_b32 v138, s59, v138\n"
      "v_xor_b32 v139, s60, v139\n"
      "v_xor_b32 v140, s57, v140\n"
      "v_xor_b32 v141, s58, v141\n"
      "v_xor_b32 v142, s59, v142\n"
      "v_xor_b32 v143, s60, v143\n"
      "v_or3_b32 v4, v136, v137, v138\n"
      "v_or3_b32 v4, v4, v139, v140\n"
      "v_or3_b32 v4, v4, v141, v142\n"
      "v_or_b32 v4, v4, v143\n"
      "v_and_b32 v4, 0x80008000, v4\n"
      "v_cmp_ne_u32 vcc, 0, v4\n"
      "s_nop 1\n"
      "s_cbranch_vccnz LAr4\n"
      "s_branch LAo4\n"
      "LAr4_x:\n"
      "s_mov_b32 s67, 0\n"
      "s_branch LAo4\n"
      "LAr5:\n"
      "s_sub_u32 s67, s67, 1\n"
      "s_cbranch_scc1 LAr5_x\n"
      "s_sleep 16\n"
      "s_add_u32 s3, s48, 16384\n"
      "buffer_load_dwordx4 v[144:147], v1, s[12:15], s3 offen sc1\n"
      "s_add_u32 s3, s48, 20480\n"
      "buffer_load_dwordx4 v[148:151], v1, s[12:15], s3 offen sc1\n"
      "s_waitcnt vmcnt(0)\n"
      "v_xor_b32 v144, s57, v144\n"
      "v_xor_b32 v145, s58, v145\n"
      "v_xor_b32 v146, s59, v146\n"
      "v_xor_b32 v147, s60, v147\n"
      "v_xor_b32 v148, s57, v148\n"
      "v_xor_b32 v149, s58, v149\n"
      "v_xor_b32 v150, s59, v150\n"
      "v_xor_b32 v151, s60, v151\n"
      "v_or3_b32 v4, v144, v145, v146\n"
      "v_or3_b32 v4, v4, v147, v148\n"
      "v_or3_b32 v4, v4, v149, v150\n"
      "v_or_b32 v4, v4, v151\n"
      "v_and_b32 v4, 0x80008000, v4\n"
      "v_cmp_ne_u32 vcc, 0, v4\n"
      "s_nop 1\n"
      "s_cbranch_vccnz LAr5\n"
      "s_branch LAo5\n"
      "LAr5_x:\n"
      "s_mov_b32 s67, 0\n"
      "s_branch LAo5\n"
      "LAr6:\n"
      "s_sub_u32 s67, s67, 1\n"
      "s_cbranch_scc1 LAr6_x\n"
      "s_sleep 16\n"
      "s_add_u32 s3, s48, 24576\n"
      "buffer_load_dwordx4 v[152:155], v1, s[12:15], s3 offen sc1\n"
      "s_add_u32 s3, s48, 28672\n"
      "buffer_load_dwordx4 v[156:159], v1, s[12:15], s3 offen sc1\n"
      "s_waitcnt vmcnt(0)\n"
      "v_xor_b32 v152, s57, v152\n"
      "v_xor_b32 v153, s58, v153\n"
      "v_xor_b32 v154, s59, v154\n"
      "v_xor_b32 v155, s60, v155\n"
      "v_xor_b32 v156, s57, v156\n"
      "v_xor_b32 v157, s58, v157\n"
      "v_xor_b32 v158, s59, v158\n"
      "v_xor_b32 v159, s60, v159\n"
      "v_or3_b32 v4, v152, v153, v154\n"
      "v_or3_b32 v4, v4, v155, v156\n"
      "v_or3_b32 v4, v4, v157, v158\n"
      "v_or_b32 v4, v4, v159\n"
      "v_and_b32 v4, 0x80008000, v4\n"
      "v_cmp_ne_u32 vcc, 0, v4\n"
      "s_nop 1\n"
      "s_cbranch_vccnz LAr6\n"
      "s_branch LAo6\n"
      "LAr6_x:\n"
      "s_mov_b32 s67, 0\n"
      "s_branch LAo6\n"
      "LAr7:\n"
      "s_sub_u32 s67, s67, 1\n"
      "s_cbranch_scc1 LAr7_x\n"
      "s_sleep 16\n"
      "s_add_u32 s3, s48, 32768\n"
      "buffer_load_dwordx4 v[160:163], v1, s[12:15], s3 offen sc1\n"
      "s_add_u32 s3, s48, 36864\n"
      "buffer_load_dwordx4 v[164:167], v1, s[12:15], s3 offen sc1\n"
      "s_waitcnt vmcnt(0)\n"
      "v_xor_b32 v160, s57, v160\n"
      "v_xor_b32 v161, s58, v161\n"
      "v_xor_b32 v162, s59, v162\n"
      "v_xor_b32 v163, s60, v163\n"
      "v_xor_b32 v164, s57, v164\n"
      "v_xor_b32 v165, s58, v165\n"
      "v_xor_b32 v166, s59, v166\n"
      "v_xor_b32 v167, s60, v167\n"
      "v_or3_b32 v4, v160, v161, v162\n"
      "v_or3_b32 v4, v4, v163, v164\n"
      "v_or3_b32 v4, v4, v165, v166\n"
      "v_or_b32 v4, v4, v167\n"
      "v_and_b32 v4, 0x80008000, v4\n"
      "v_cmp_ne_u32 vcc, 0, v4\n"
      "s_nop 1\n"
      "s_cbranch_vccnz LAr7\n"
      "s_branch LAo7\n"
      "LAr7_x:\n"
      "s_mov_b32 s67, 0\n"
      "s_branch LAo7\n"
      "LAr8:\n"
      "s_sub_u32 s67, s67, 1\n"
      "s_cbranch_scc1 LAr8_x\n"
      "s_sleep 16\n"
      "s_add_u32 s3, s48, 40960\n"
      "buffer_load_dwordx4 v[168:171], v1, s[12:15], s3 offen sc1\n"
      "s_add_u32 s3, s48, 45056\n"
      "buffer_load_dwordx4 v[172:175], v1, s[12:15], s3 offen sc1\n"
      "s_waitcnt vmcnt(0)\n"
      "v_xor_b32 v168, s57, v168\n"
      "v_xor_b32 v169, s58, v169\n"
      "v_xor_b32 v170, s59, v170\n"
      "v_xor_b32 v171, s60, v171\n"
      "v_xor_b32 v172, s57, v172\n"
      "v_xor_b32 v173, s58, v173\n"
      "v_xor_b32 v174, s59, v174\n"
      "v_xor_b32 v175, s60, v175\n"
      "v_or3_b32 v4, v168, v169, v170\n"
      "v_or3_b32 v4, v4, v171, v172\n"
      "v_or3_b32 v4, v4, v173, v174\n"
      "v_or_b32 v4, v4, v175\n"
      "v_and_b32 v4, 0x80008000, v4\n"
      "v_cmp_ne_u32 vcc, 0, v4\n"
      "s_nop 1\n"
      "s_cbranch_vccnz LAr8\n"
      "s_branch LAo8\n"
      "LAr8_x:\n"
      "s_mov_b32 s67, 0\n"
      "s_branch LAo8\n"
      "LAr9:\n"
      "s_sub_u32 s67, s67, 1\n"
      "s_cbranch_scc1 LAr9_x\n"
      "s_sleep 16\n"
      "s_add_u32 s3, s48, 49152\n"
      "buffer_load_dwordx4 v[176:179], v1, s[12:15], s3 offen sc1\n"
      "s_add_u32 s3, s48, 53248\n"
      "buffer_load_dwordx4 v[180:183], v1, s[12:15], s3 offen sc1\n"
      "s_waitcnt vmcnt(0)\n"
      "v_xor_b32 v176, s57, v176\n"
      "v_xor_b32 v177, s58, v177\n"
      "v_xor_b32 v178, s59, v178\n"
      "v_xor_b32 v179, s60, v179\n"
      "v_xor_b32 v180, s57, v180\n"
      "v_xor_b32 v181, s58, v181\n"
      "v_xor_b32 v182, s59, v182\n"
      "v_xor_b32 v183, s60, v183\n"
      "v_or3_b32 v4, v176, v177, v178\n"
      "v_or3_b32 v4, v4, v179, v180\n"
      "v_or3_b32 v4, v4, v181, v182\n"
      "v_or_b32 v4, v4, v183\n"
      "v_and_b32 v4, 0x80008000, v4\n"
      "v_cmp_ne_u32 vcc, 0, v4\n"
      "s_nop 1\n"
      "s_cbranch_vccnz LAr9\n"
      "s_branch LAo9\n"
      "LAr9_x:\n"
      "s_mov_b32 s67, 0\n"
      "s_branch LAo9\n"
      "LAr10:\n"
      "s_sub_u32 s67, s67, 1\n"
      "s_cbranch_scc1 LAr10_x\n"
      "s_sleep 16\n"
      "s_add_u32 s3, s48, 57344\n"
      "buffer_load_dwordx4 v[184:187], v1, s[12:15], s3 offen sc1\n"
      "s_add_u32 s3, s48, 61440\n"
      "buffer_load_dwordx4 v[188:191], v1, s[12:15], s3 offen sc1\n"
      "s_waitcnt vmcnt(0)\n"
      "v_xor_b32 v184, s57, v184\n"
      "v_xor_b32 v185, s58, v185\n"
      "v_xor_b32 v186, s59, v186\n"
      "v_xor_b32 v187, s60, v187\n"
      "v_xor_b32 v188, s57, v188\n"
      "v_xor_b32 v189, s58, v189\n"
      "v_xor_b32 v190, s59, v190\n"
      "v_xor_b32 v191, s60, v191\n"
      "v_or3_b32 v4, v184, v185, v186\n"
      "v_or3_b32 v4, v4, v187, v188\n"
      "v_or3_b32 v4, v4, v189, v190\n"
      "v_or_b32 v4, v4, v191\n"
      "v_and_b32 v4, 0x80008000, v4\n"
      "v_cmp_ne_u32 vcc, 0, v4\n"
      "s_nop 1\n"
      "s_cbranch_vccnz LAr10\n"
      "s_branch LAo10\n"
      "LAr10_x:\n"
      "s_mov_b32 s67, 0\n"
      "s_branch LAo10\n"
      "LAr11:\n"
      "s_sub_u32 s67, s67, 1\n"
      "s_cbranch_scc1 LAr11_x\n"
      "s_sleep 16\n"
      "s_add_u32 s3, s48, 65536\n"
      "buffer_load_dwordx4 v[192:195], v1, s[12:15], s3 offen sc1\n"
      "s_add_u32 s3, s48, 67584\n"
      "buffer_load_dwordx4 v[196:199], v1, s[12:15], s3 offen sc1\n"
      "s_waitcnt vmcnt(0)\n"
      "v_xor_b32 v192, s57, v192\n"
      "v_xor_b32 v193, s58, v193\n"
      "v_xor_b32 v194, s59, v194\n"
      "v_xor_b32 v195, s60, v195\n"
      "v_xor_b32 v196, s57, v196\n"
      "v_xor_b32 v197, s58, v197\n"
      "v_xor_b32 v198, s59, v198\n"
      "v_xor_b32 v199, s60, v199\n"
      "v_or3_b32 v4, v192, v193, v194\n"
      "v_or3_b32 v4, v4, v195, v196\n"
      "v_or3_b32 v4, v4, v197, v198\n"
      "v_or_b32 v4, v4, v199\n"
      "v_and_b32 v4, 0x80008000, v4\n"
      "v_cmp_ne_u32 vcc, 0, v4\n"
      "s_nop 1\n"
      "s_cbranch_vccnz LAr11\n"
      "s_branch LAo11\n"
      "LAr11_x:\n"
      "s_mov_b32 s67, 0\n"
      "s_branch LAo11\n"
      "LAr12:\n"
      "s_sub_u32 s67, s67, 1\n"
      "s_cbranch_scc1 LAr12_x\n"
      "s_sleep 16\n"
      "buffer_load_dwordx4 v[200:203], v1, s[12:15], s50 offen sc1\n"
      "s_add_u32 s3, s50, 4096\n"
      "buffer_load_dwordx4 v[204:207], v1, s[12:15], s3 offen sc1\n"
      "s_waitcnt vmcnt(0)\n"
      "v_xor_b32 v200, s61, v200\n"
      "v_xor_b32 v201, s62, v201\n"
      "v_xor_b32 v202, s63, v202\n"
      "v_xor_b32 v203, s64, v203\n"
      "v_xor_b32 v204, s61, v204\n"
      "v_xor_b32 v205, s62, v205\n"
      "v_xor_b32 v206, s63, v206\n"
      "v_xor_b32 v207, s64, v207\n"
      "v_or3_b32 v4, v200, v201, v202\n"
      "v_or3_b32 v4, v4, v203, v204\n"
      "v_or3_b32 v4, v4, v205, v206\n"
      "v_or_b32 v4, v4, v207\n"
      "v_and_b32 v4, 0x10001, v4\n"
      "v_cmp_ne_u32 vcc, 0, v4\n"
      "s_nop 1\n"
      "s_cbranch_vccnz LAr12\n"
      "s_branch LAo12\n"
      "LAr12_x:\n"
      "s_mov_b32 s67, 0\n"
      "s_branch LAo12\n"
      "LAr13:\n"
      "s_sub_u32 s67, s67, 1\n"
      "s_cbranch_scc1 LAr13_x\n"
      "s_sleep 16\n"
      "s_add_u32 s3, s50, 8192\n"
      "buffer_load_dwordx4 v[208:211], v1, s[12:15], s3 offen sc1\n"
      "s_add_u32 s3, s50, 12288\n"
      "buffer_load_dwordx4 v[212:215], v1, s[12:15], s3 offen sc1\n"
      "s_waitcnt vmcnt(0)\n"
      "v_xor_b32 v208, s61, v208\n"
      "v_xor_b32 v209, s62, v209\n"
      "v_xor_b32 v210, s63, v210\n"
      "v_xor_b32 v211, s64, v211\n"
      "v_xor_b32 v212, s61, v212\n"
      "v_xor_b32 v213, s62, v213\n"
      "v_xor_b32 v214, s63, v214\n"
      "v_xor_b32 v215, s64, v215\n"
      "v_or3_b32 v4, v208, v209, v210\n"
      "v_or3_b32 v4, v4, v211, v212\n"
      "v_or3_b32 v4, v4, v213, v214\n"
      "v_or_b32 v4, v4, v215\n"
      "v_and_b32 v4, 0x10001, v4\n"
      "v_cmp_ne_u32 vcc, 0, v4\n"
      "s_nop 1\n"
      "s_cbranch_vccnz LAr13\n"
      "s_branch LAo13\n"
      "LAr13_x:\n"
      "s_mov_b32 s67, 0\n"
      "s_branch LAo13\n"
      "LAr14:\n"
      "s_sub_u32 s67, s67, 1\n"
      "s_cbranch_scc1 LAr14_x\n"
      "s_sleep 16\n"
      "s_add_u32 s3, s50, 16384\n"
      "buffer_load_dwordx4 v[216:219], v1, s[12:15], s3 offen sc1\n"
      "s_add_u32 s3, s50, 20480\n"
      "buffer_load_dwordx4 v[220:223], v1, s[12:15], s3 offen sc1\n"
      "s_waitcnt vmcnt(0)\n"
      "v_xor_b32 v216, s61, v216\n"
      "v_xor_b32 v217, s62, v217\n"
      "v_xor_b32 v218, s63, v218\n"
      "v_xor_b32 v219, s64, v219\n"
      "v_xor_b32 v220, s61, v220\n"
      "v_xor_b32 v221, s62, v221\n"
      "v_xor_b32 v222, s63, v222\n"
      "v_xor_b32 v223, s64, v223\n"
      "v_or3_b32 v4, v216, v217, v218\n"
      "v_or3_b32 v4, v4, v219, v220\n"
      "v_or3_b32 v4, v4, v221, v222\n"
      "v_or_b32 v4, v4, v223\n"
      "v_and_b32 v4, 0x10001, v4\n"
      "v_cmp_ne_u32 vcc, 0, v4\n"
      "s_nop 1\n"
      "s_cbranch_vccnz LAr14\n"
      "s_branch LAo14\n"
      "LAr14_x:\n"
      "s_mov_b32 s67, 0\n"
      "s_branch LAo14\n"
      "LAr15:\n"
      "s_sub_u32 s67, s67, 1\n"
      "s_cbranch_scc1 LAr15_x\n"
      "s_sleep 16\n"
      "s_add_u32 s3, s50, 24576\n"
      "buffer_load_dwordx4 v[224:227], v1, s[12:15], s3 offen sc1\n"
      "s_add_u32 s3, s50, 28672\n"
      "buffer_load_dwordx4 v[228:231], v1, s[12:15], s3 offen sc1\n"
      "s_waitcnt vmcnt(0)\n"
      "v_xor_b32 v224, s61, v224\n"
      "v_xor_b32 v225, s62, v225\n"
      "v_xor_b32 v226, s63, v226\n"
      "v_xor_b32 v227, s64, v227\n"
      "v_xor_b32 v228, s61, v228\n"
      "v_xor_b32 v229, s62, v229\n"
      "v_xor_b32 v230, s63, v230\n"
      "v_xor_b32 v231, s64, v231\n"
      "v_or3_b32 v4, v224, v225, v226\n"
      "v_or3_b32 v4, v4, v227, v228\n"
      "v_or3_b32 v4, v4, v229, v230\n"
      "v_or_b32 v4, v4, v231\n"
      "v_and_b32 v4, 0x10001, v4\n"
      "v_cmp_ne_u32 vcc, 0, v4\n"
      "s_nop 1\n"
      "s_cbranch_vccnz LAr15\n"
      "s_branch LAo15\n"
      "LAr15_x:\n"
      "s_mov_b32 s67, 0\n"
      "s_branch LAo15\n"
      "LAr16:\n"
      "s_sub_u32 s67, s67, 1\n"
      "s_cbranch_scc1 LAr16_x\n"
      "s_sleep 2\n"
      "buffer_load_dwordx4 v[248:251], v1, s[12:15], s54 offen sc1\n"
      "s_waitcnt vmcnt(0)\n"
      "v_xor_b32 v248, s61, v248\n"
      "v_xor_b32 v249, s62, v249\n"
      "v_xor_b32 v250, s63, v250\n"
      "v_xor_b32 v251, s64, v251\n"
      "v_or3_b32 v4, v248, v249, v250\n"
      "v_or_b32 v4, v4, v251\n"
      "v_and_b32 v4, 0x10001, v4\n"
      "v_cmp_ne_u32 vcc, 0, v4\n"
      "s_nop 1\n"
      "s_cbranch_vccnz LAr16\n"
      "s_branch LAo16\n"
      "LAr16_x:\n"
      "s_mov_b32 s67, 0\n"
      "s_branch LAo16\n"
      ".p2align 6\n"
      "LBstart:\n"
      "s_load_dwordx2 s[8:9], s[0:1], 0x18\n"
      "s_load_dwordx2 s[92:93], s[0:1], 0x8\n"
      "s_mov_b32 s94, 1572864\n"
      "s_mov_b32 s95, 0x20000\n"
      "v_mul_u32_u24 v44, 5504, v5\n"
      "s_mov_b32 s10, 1409024\n"
      "s_mov_b32 s11, 0x20000\n"
      "v_lshl_add_u32 v44, v4, 4, v44\n"
      "s_mul_i32 s81, s29, 44032\n"
      "s_lshl3_add_u32 s82, s32, s28\n"
      "s_lshl_b32 s82, s82, 10\n"
      "s_add_u32 s55, s82, s33\n"
      "s_add_u32 s55, s55, 65536\n"
      "s_mul_i32 s69, s32, 43\n"
      "s_add_u32 s66, s69, 5\n"
      "s_lshr_b32 s66, s66, 3\n"
      "s_add_u32 s69, s69, 48\n"
      "s_lshr_b32 s69, s69, 3\n"
      "s_sub_u32 s69, s69, s66\n"
      "s_sub_u32 s82, s66, s30\n"
      "s_bitcmp1_b32 s82, 0\n"
      "s_cselect_b32 s83, 2752, 0\n"
      "s_lshl_b32 s82, s82, 6\n"
      "s_add_u32 s81, s81, s83\n"
      "s_add_u32 s36, s81, s82\n"
      "s_add_u32 s37, s36, 128\n"
      "s_bitcmp1_b32 s66, 0\n"
      "s_cselect_b32 s83, 0, 2752\n"
      "s_lshl_b32 s82, s66, 6\n"
      "s_mul_i32 s81, s28, 44032\n"
      "s_add_u32 s82, s82, s83\n"
      "s_add_u32 s81, s81, s82\n"
      "s_add_u32 s97, s81, 192\n"
      "s_waitcnt lgkmcnt(0)\n"
      "buffer_load_dword v3, off, s[12:15], 0 sc1\n"
      "buffer_load_dwordx4 v[32:35], v1, s[4:7], s55 offen\n"
      "s_lshl3_add_u32 s81, s31, s32\n"
      "s_lshl3_add_u32 s81, s81, s28\n"
      "s_mul_i32 s3, s81, 384\n"
      "s_add_u32 s3, s3, 32768\n"
      "v_lshlrev_b32 v48, 7, v0\n"
      "s_mov_b64 exec, 7\n"
      "buffer_load_dword v49, v48, s[8:11], s3 offen\n"
      "s_mov_b64 exec, -1\n"
      "s_andn2_b32 s82, s2, 7\n"
      "s_add_u32 s82, s82, s28\n"
      "s_mul_i32 s82, s82, 6144\n"
      "s_and_b32 s83, s2, 1\n"
      "s_lshl_b32 s83, s83, 24\n"
      "s_add_u32 s3, s82, s83\n"
      "v_lshlrev_b32 v51, 10, v0\n"
      "s_mov_b64 exec, 63\n"
      "buffer_load_dword v51, v51, s[92:95], s3 offen offset:384\n"
      "s_mov_b64 exec, -1\n"
      "s_add_u32 m0, s35, 0\n"
      "s_nop 0\n"
      "buffer_load_dwordx4 v46, s[4:7], s33 offen lds\n"
      "s_add_u32 m0, s35, 1024\n"
      "s_nop 0\n"
      "buffer_load_dwordx4 v47, s[4:7], s33 offen lds\n"
      "s_add_u32 m0, s34, 16384\n"
      "s_nop 0\n"
      "buffer_load_dwordx4 v44, s[8:11], s36 offen lds\n"
      "s_add_u32 m0, s34, 24576\n"
      "s_nop 0\n"
      "buffer_load_dwordx4 v44, s[8:11], s37 offen lds\n"
      "s_add_u32 m0, s34, 131072\n"
      "s_nop 0\n"
      "buffer_load_dwordx4 v44, s[8:11], s97 offen lds\n"
      "s_mul_i32 s3, s81, 960\n"
      "s_add_u32 s3, s3, 425984\n"
      "s_buffer_load_dword s74, s[8:11], s3 offset:0\n"
      "s_buffer_load_dword s74, s[8:11], s3 offset:64\n"
      "s_buffer_load_dword s74, s[8:11], s3 offset:128\n"
      "s_buffer_load_dword s74, s[8:11], s3 offset:192\n"
      "s_buffer_load_dword s74, s[8:11], s3 offset:256\n"
      "s_buffer_load_dword s74, s[8:11], s3 offset:320\n"
      "s_buffer_load_dword s74, s[8:11], s3 offset:384\n"
      "s_buffer_load_dword s74, s[8:11], s3 offset:448\n"
      "s_buffer_load_dword s74, s[8:11], s3 offset:512\n"
      "s_buffer_load_dword s74, s[8:11], s3 offset:576\n"
      "s_buffer_load_dword s74, s[8:11], s3 offset:640\n"
      "s_buffer_load_dword s74, s[8:11], s3 offset:704\n"
      "s_buffer_load_dword s74, s[8:11], s3 offset:768\n"
      "s_buffer_load_dword s74, s[8:11], s3 offset:832\n"
      "s_buffer_load_dword s74, s[8:11], s3 offset:896\n"
      "s_getpc_b64 s[92:93]\n"
      "LBpc:\n"
      "s_sub_u32 s92, s92, LBpc-Lstart\n"
      "s_subb_u32 s93, s93, 0\n"
      "s_mov_b32 s94, Lend-Lstart\n"
      "s_mov_b32 s95, 0x20000\n"
      "s_andn2_b32 s3, s2, 7\n"
      "s_add_u32 s3, s3, s28\n"
      "s_lshl_b32 s3, s3, 7\n"
      "buffer_load_dword v8, off, s[92:95], s3\n"
      "v_bfe_u32 v59, v0, 4, 1\n"
      "v_lshrrev_b32 v56, 5, v0\n"
      "v_bfe_u32 v57, v0, 3, 1\n"
      "v_and_b32 v58, 31, v0\n"
      "v_bfe_u32 v4, v0, 1, 3\n"
      "v_xor_b32 v4, v4, v56\n"
      "v_lshlrev_b32 v4, 4, v4\n"
      "v_lshl_add_u32 v4, v58, 7, v4\n"
      "s_mul_i32 s82, s29, 4128\n"
      "v_xor_b32 v16, s82, v4\n"
      "s_bfe_u32 s89, s2, 0x30003\n"
      "s_and_b32 s81, s2, 1\n"
      "s_and_b32 s82, s28, 1\n"
      "s_lshl1_add_u32 s81, s81, s82\n"
      "s_lshl_b32 s81, s81, 12\n"
      "s_and_b32 s82, s28, 6\n"
      "s_lshl4_add_u32 s81, s82, s81\n"
      "v_xor_b32 v30, s81, v4\n"
      "s_lshl_b32 s81, s89, 1\n"
      "s_lshr_b32 s81, 0xe4e4, s81\n"
      "s_and_b32 s81, s81, 3\n"
      "s_mul_i32 s81, s81, 32768\n"
      "v_add_u32 v30, s81, v30\n"
      "v_xor_b32 v17, 32, v16\n"
      "v_xor_b32 v18, 64, v16\n"
      "v_xor_b32 v19, 96, v16\n"
      "v_lshl_add_u32 v4, v56, 2, v57\n"
      "v_lshlrev_b32 v4, 7, v4\n"
      "v_lshl_add_u32 v4, v59, 5, v4\n"
      "v_and_b32 v5, 3, v0\n"
      "v_lshl_add_u32 v4, v5, 3, v4\n"
      "s_lshl4_add_u32 s82, s29, s30\n"
      "s_lshl_b32 s82, s82, 6\n"
      "s_add_u32 s82, s82, 16384\n"
      "s_xor_b32 s83, s82, 4160\n"
      "s_mov_b32 vcc_lo, 0xf0f0f0f0\n"
      "s_bitcmp1_b32 s66, 0\n"
      "s_cselect_b32 vcc_lo, 0x0f0f0f0f, vcc_lo\n"
      "s_lshl_b32 s81, s30, 13\n"
      "s_add_u32 s83, s83, s81\n"
      "s_mov_b32 vcc_hi, vcc_lo\n"
      "v_add_u32 v5, s82, v4\n"
      "v_add_u32 v4, s83, v4\n"
      "v_cndmask_b32 v20, v5, v4, vcc\n"
      "s_mov_b32 s86, 0xf0f00f0f\n"
      "s_mov_b32 s87, 0xf0f00f0f\n"
      "v_mov_b32 v4, 0x3f803f80\n"
      "v_xor_b32 v21, 1024, v20\n"
      "v_xor_b32 v22, 2048, v20\n"
      "v_xor_b32 v23, 3072, v20\n"
      "v_cndmask_b32 v12, 0, v4, s[86:87]\n"
      "v_cndmask_b32 v13, 0, v4, s[86:87]\n"
      "v_cndmask_b32 v14, 0, v4, s[86:87]\n"
      "v_cndmask_b32 v15, 0, v4, s[86:87]\n"
      "s_cmp_eq_u32 s30, 0\n"
      "s_cselect_b64 s[86:87], 0, vcc\n"
      "v_mov_b32 v4, 8192\n"
      "v_mov_b32 v5, 102400\n"
      "v_cndmask_b32 v4, v4, v5, s[86:87]\n"
      "v_add_u32 v60, v20, v4\n"
      "v_add_u32 v61, v21, v4\n"
      "v_add_u32 v62, v22, v4\n"
      "v_add_u32 v63, v23, v4\n"
      "v_mov_b32 v5, 4096\n"
      "v_mov_b32 v31, 32768\n"
      "v_mov_b32 v7, -98304\n"
      "v_cndmask_b32 v31, v31, v5, s[86:87]\n"
      "v_cndmask_b32 v7, v7, v5, s[86:87]\n"
      "s_add_u32 s88, s2, s28\n"
      "s_sub_u32 s88, s88, 1\n"
      "s_lshl_b32 s81, s30, 7\n"
      "s_add_u32 s81, s81, s36\n"
      "s_add_u32 s81, s81, 256\n"
      "s_sub_u32 s82, s69, s30\n"
      "s_cmp_eq_u32 s82, 4\n"
      "s_cselect_b32 s38, 1073741824, s81\n"
      "s_lshl_b32 s81, s30, 5\n"
      "s_add_u32 s81, s81, s29\n"
      "s_lshl_b32 s96, s81, 10\n"
      "s_cmp_eq_u32 s30, 0\n"
      "s_cselect_b32 s98, -1, 0x0f0f0f0f\n"
      "s_mov_b32 s99, s98\n"
      "s_mul_i32 s81, s30, -106496\n"
      "s_add_u32 s81, s81, 45056\n"
      "s_cmp_eq_u32 s82, 4\n"
      "s_cselect_b32 s82, 32704, 0\n"
      "s_sub_u32 s82, s82, 81920\n"
      "v_mov_b32 v45, s82\n"
      "v_mov_b32 v4, s81\n"
      "v_cndmask_b32 v45, v45, v4, vcc\n"
      "s_lshl2_add_u32 s56, s31, s29\n"
      "s_mul_i32 s0, s56, 122880\n"
      "s_lshl_b32 s56, s56, 9\n"
      "s_add_u32 s56, s56, 98304\n"
      "v_lshlrev_b32 v4, 6, v59\n"
      "v_and_b32 v6, 0x70, v1\n"
      "v_xor_b32 v6, v6, v4\n"
      "v_lshrrev_b32 v4, 3, v0\n"
      "v_lshl_add_u32 v6, v4, 10, v6\n"
      "v_lshrrev_b32 v4, 2, v58\n"
      "v_lshlrev_b32 v4, 7, v4\n"
      "s_add_u32 m0, s35, 32768\n"
      "s_add_u32 s3, s33, 128\n"
      "buffer_load_dwordx4 v46, s[4:7], s3 offen lds\n"
      "s_add_u32 m0, s35, 33792\n"
      "s_add_u32 s3, s33, 128\n"
      "buffer_load_dwordx4 v47, s[4:7], s3 offen lds\n"
      "s_add_u32 m0, s34, 49152\n"
      "s_add_u32 s3, s36, 176128\n"
      "buffer_load_dwordx4 v44, s[8:11], s3 offen lds\n"
      "s_add_u32 m0, s34, 57344\n"
      "s_add_u32 s3, s37, 176128\n"
      "buffer_load_dwordx4 v44, s[8:11], s3 offen lds\n"
      "v_and_b32 v5, 48, v1\n"
      "s_lshl_b32 s82, s30, 10\n"
      "s_add_u32 s82, s82, 32832\n"
      "v_add3_u32 v28, v4, v5, s82\n"
      "v_lshl_add_u32 v29, s29, 2, v28\n"
      "s_mov_b32 s18, 1409024\n"
      "s_mov_b32 s19, 0x20000\n"
      "s_mov_b32 s22, 524288\n"
      "s_mov_b32 s23, 0x20000\n"
      "s_mov_b32 s26, 2097152\n"
      "s_mov_b32 s27, 0x20000\n"
      "s_mov_b32 s67, 0x40000\n"
      "s_lshl1_add_u32 s68, s32, s30\n"
      "s_lshl_b32 s82, s68, 11\n"
      "s_add_u32 s82, s0, s82\n"
      "s_add_u32 s47, s82, 3145728\n"
      "s_add_u32 s68, s47, 32768\n"
      "s_lshl_b32 s81, s32, 2\n"
      "s_sub_u32 s81, s66, s81\n"
      "s_add_u32 s81, s81, s30\n"
      "s_lshl_b32 s81, s81, 11\n"
      "s_add_u32 s81, s81, s0\n"
      "s_add_u32 s81, s81, 3211264\n"
      "s_add_u32 s82, s30, 4\n"
      "s_cmp_lt_u32 s82, s69\n"
      "s_cselect_b32 s69, s81, 1073741824\n"
      "s_lshl2_add_u32 s0, s31, 2\n"
      "s_lshl_b32 s1, s89, 7\n"
      "s_lshl_b32 s81, s29, 13\n"
      "s_add_u32 s81, s81, s1\n"
      "s_lshl_b32 s82, s30, 15\n"
      "s_waitcnt vmcnt(13)\n"
      "v_readfirstlane_b32 s65, v3\n"
      "s_nop 1\n"
      "s_andn2_b32 s65, 0xff, s65\n"
      "s_add_u32 s78, s65, 1\n"
      "s_and_b32 s78, s78, 0xfe\n"
      "s_cselect_b32 s65, s65, 0x55\n"
      "s_mul_i32 s79, s65, 0x8001\n"
      "s_lshr_b32 s78, s79, 0\n"
      "s_and_b32 s61, s78, 0x10001\n"
      "s_lshl_b32 s57, s61, 15\n"
      "s_lshr_b32 s78, s79, 2\n"
      "s_and_b32 s62, s78, 0x10001\n"
      "s_lshl_b32 s58, s62, 15\n"
      "s_lshr_b32 s78, s79, 4\n"
      "s_and_b32 s63, s78, 0x10001\n"
      "s_lshl_b32 s59, s63, 15\n"
      "s_lshr_b32 s78, s79, 6\n"
      "s_and_b32 s64, s78, 0x10001\n"
      "s_lshl_b32 s60, s64, 15\n"
      "s_mul_i32 s65, s65, 0x01010101\n"
      "s_waitcnt vmcnt(12)\n"
      "v_lshlrev_b32 v38, 16, v32\n"
      "v_and_b32 v32, 0xffff0000, v32\n"
      "v_add_f32 v36, v38, v32\n"
      "v_mul_f32 v37, v38, v38\n"
      "v_fmac_f32 v37, v32, v32\n"
      "v_lshlrev_b32 v38, 16, v33\n"
      "v_and_b32 v33, 0xffff0000, v33\n"
      "v_add_f32 v36, v36, v38\n"
      "v_fmac_f32 v37, v38, v38\n"
      "v_add_f32 v36, v36, v33\n"
      "v_fmac_f32 v37, v33, v33\n"
      "v_lshlrev_b32 v38, 16, v34\n"
      "v_and_b32 v34, 0xffff0000, v34\n"
      "v_add_f32 v36, v36, v38\n"
      "v_fmac_f32 v37, v38, v38\n"
      "v_add_f32 v36, v36, v34\n"
      "v_fmac_f32 v37, v34, v34\n"
      "v_lshlrev_b32 v38, 16, v35\n"
      "v_and_b32 v35, 0xffff0000, v35\n"
      "v_add_f32 v36, v36, v38\n"
      "v_fmac_f32 v37, v38, v38\n"
      "v_add_f32 v36, v36, v35\n"
      "v_fmac_f32 v37, v35, v35\n"
      "s_nop 1\n"
      "v_permlane32_swap_b32 v36, v37\n"
      "v_add_f32 v36, v36, v37\n"
      "s_nop 1\n"
      "v_add_f32_dpp v36, v36, v36 quad_perm:[1,0,3,2] row_mask:0xf bank_mask:0xf\n"
      "s_nop 1\n"
      "v_add_f32_dpp v36, v36, v36 quad_perm:[2,3,0,1] row_mask:0xf bank_mask:0xf\n"
      "s_nop 1\n"
      "v_add_f32_dpp v36, v36, v36 row_half_mirror row_mask:0xf bank_mask:0xf\n"
      "s_nop 1\n"
      "v_add_f32_dpp v36, v36, v36 row_mirror row_mask:0xf bank_mask:0xf\n"
      "v_mov_b32 v37, v36\n"
      "s_nop 1\n"
      "v_permlane16_swap_b32 v36, v37\n"
      "v_add_f32 v36, v36, v37\n"
      "v_mov_b32 v37, v36\n"
      "s_nop 1\n"
      "v_permlane32_swap_b32 v36, v37\n"
      "v_mul_f32 v36, 0xbb000000, v36\n"
      "v_mov_b32 v42, s65\n"
      "v_cvt_pk_bf16_f32 v38, v36, v36\n"
      "v_mul_f32 v32, v36, v36\n"
      "v_and_b32 v38, 0xffff0000, v38\n"
      "v_fmamk_f32 v32, v37, 0xbb000000, v32\n"
      "v_sub_f32 v38, v36, v38\n"
      "v_min_f32 v32, 0, v32\n"
      "v_cvt_pk_bf16_f32 v40, v36, v38\n"
      "v_sub_f32 v32, 0x3727c5ac, v32\n"
      "v_mov_b32 v43, s65\n"
      "v_rsq_f32 v41, v32\n"
      "v_lshlrev_b32 v38, 24, v0\n"
      "s_lshr_b32 s55, s55, 6\n"
      "s_add_u32 s55, s55, 98304\n"
      "v_add_u32 v39, s28, v0\n"
      "v_lshlrev_b32 v39, 24, v39\n"
      "buffer_store_dwordx4 v[40:43], v38, s[12:15], s55 offen sc1\n"
      "s_waitcnt vmcnt(6) lgkmcnt(0)\n"
      "s_barrier\n"
      "ds_read_b128 v[128:131], v16 offset:0\n"
      "ds_read_b64_tr_b16 v[64:65], v20 offset:0\n"
      "ds_read_b64_tr_b16 v[66:67], v20 offset:256\n"
      "ds_read_b128 v[132:135], v17 offset:0\n"
      "ds_read_b64_tr_b16 v[68:69], v21 offset:0\n"
      "ds_read_b64_tr_b16 v[70:71], v21 offset:256\n"
      "ds_read_b128 v[136:139], v18 offset:0\n"
      "ds_read_b64_tr_b16 v[72:73], v22 offset:0\n"
      "ds_read_b64_tr_b16 v[74:75], v22 offset:256\n"
      "ds_read_b128 v[140:143], v19 offset:0\n"
      "ds_read_b64_tr_b16 v[76:77], v23 offset:0\n"
      "ds_read_b64_tr_b16 v[78:79], v23 offset:256\n"
      "s_add_u32 m0, s35, 65536\n"
      "s_add_u32 s3, s33, 256\n"
      "buffer_load_dwordx4 v46, s[4:7], s3 offen lds\n"
      "s_add_u32 m0, s35, 66560\n"
      "s_add_u32 s3, s33, 256\n"
      "buffer_load_dwordx4 v47, s[4:7], s3 offen lds\n"
      "s_waitcnt lgkmcnt(9)\n"
      "v_mfma_f32_32x32x16_bf16 v[96:111], v[64:67], v[128:131], 0\n"
      "v_mfma_f32_16x16x32_bf16 v[48:51], v[12:15], v[64:67], 0\n"
      "ds_read_b64_tr_b16 v[80:81], v60 offset:0\n"
      "ds_read_b64_tr_b16 v[82:83], v60 offset:256\n"
      "v_add_u32 v60, v60, v31\n"
      "s_add_u32 m0, s34, 81920\n"
      "s_add_u32 s3, s36, 352256\n"
      "buffer_load_dwordx4 v44, s[8:11], s3 offen lds\n"
      "v_lshlrev_b32 v36, 4, v58\n"
      "v_mul_u32_u24 v37, 0x0202, v56\n"
      "v_add_u32 v37, 0x0100, v37\n"
      "v_lshl_or_b32 v37, v37, 16, v37\n"
      "s_waitcnt lgkmcnt(8)\n"
      "v_mfma_f32_32x32x16_bf16 v[96:111], v[68:71], v[132:135], v[96:111]\n"
      "ds_read_b64_tr_b16 v[84:85], v61 offset:0\n"
      "ds_read_b64_tr_b16 v[86:87], v61 offset:256\n"
      "v_add_u32 v61, v61, v31\n"
      "s_add_u32 m0, s34, 90112\n"
      "s_add_u32 s3, s37, 352256\n"
      "buffer_load_dwordx4 v44, s[8:11], s3 offen lds\n"
      "s_add_u32 s82, s82, s81\n"
      "s_add_u32 s45, s82, 131072\n"
      "s_mul_i32 s82, s30, 163840\n"
      "s_add_u32 s82, s82, s81\n"
      "s_add_u32 s49, s82, 327680\n"
      "s_add_u32 s81, s81, 32768\n"
      "s_cmp_eq_u32 s30, 0\n"
      "s_cselect_b32 s41, 0, 6\n"
      "s_cselect_b32 s42, 11, 16\n"
      "s_cselect_b32 s43, 22, 27\n"
      "s_cselect_b32 s44, 32, 38\n"
      "s_lshl_b32 s41, s41, 15\n"
      "s_waitcnt lgkmcnt(7)\n"
      "v_mfma_f32_32x32x16_bf16 v[96:111], v[72:75], v[136:139], v[96:111]\n"
      "ds_read_b64_tr_b16 v[88:89], v62 offset:0\n"
      "ds_read_b64_tr_b16 v[90:91], v62 offset:256\n"
      "v_add_u32 v62, v62, v31\n"
      "s_add_u32 m0, s34, 139264\n"
      "s_add_u32 s3, s97, 352256\n"
      "buffer_load_dwordx4 v44, s[8:11], s3 offen lds\n"
      "s_add_u32 s41, s41, s81\n"
      "s_lshl_b32 s42, s42, 15\n"
      "s_add_u32 s42, s42, s81\n"
      "s_lshl_b32 s43, s43, 15\n"
      "s_add_u32 s43, s43, s81\n"
      "s_lshl_b32 s44, s44, 15\n"
      "s_add_u32 s44, s44, s81\n"
      "s_lshl_b32 s82, s28, 13\n"
      "s_waitcnt lgkmcnt(6)\n"
      "v_mfma_f32_32x32x16_bf16 v[96:111], v[76:79], v[140:143], v[96:111]\n"
      "ds_read_b64_tr_b16 v[92:93], v63 offset:0\n"
      "ds_read_b64_tr_b16 v[94:95], v63 offset:256\n"
      "v_add_u32 v63, v63, v31\n"
      "s_cmp_eq_u32 s89, 0\n"
      "s_cselect_b64 exec, -1, 0\n"
      "ds_read_b128 v[24:27], v30\n"
      "s_mov_b64 exec, -1\n"
      "s_add_u32 s46, s82, s1\n"
      "s_xor_b32 s81, s0, s29\n"
      "s_mul_i32 s81, s81, 122880\n"
      "s_lshl_b32 s82, s30, 10\n"
      "s_add_u32 s81, s81, s82\n"
      "s_add_u32 s48, s81, 3145728\n"
      "s_and_b32 s81, s28, 1\n"
      "s_add_u32 s0, s0, s81\n"
      "s_waitcnt vmcnt(6)\n"
      "s_barrier\n"
      "s_waitcnt lgkmcnt(7)\n"
      "v_mfma_f32_32x32x16_bf16 v[112:127], v[80:83], v[128:131], 0\n"
      "v_mfma_f32_16x16x32_bf16 v[52:55], v[12:15], v[80:83], 0\n"
      "ds_read_b128 v[144:147], v16 offset:32768\n"
      "ds_read_b64_tr_b16 v[64:65], v20 offset:32768\n"
      "ds_read_b64_tr_b16 v[66:67], v20 offset:33024\n"
      "v_xor_b32 v16, 0x10000, v16\n"
      "v_xor_b32 v20, 0x10000, v20\n"
      "s_add_u32 m0, s35, 98304\n"
      "s_add_u32 s3, s33, 384\n"
      "buffer_load_dwordx4 v46, s[4:7], s3 offen lds\n"
      "s_mul_i32 s82, s0, 122880\n"
      "s_andn2_b32 s83, s28, 1\n"
      "s_lshl_b32 s83, s83, 9\n"
      "s_add_u32 s82, s82, s83\n"
      "s_add_u32 s50, s82, 3235840\n"
      "s_lshl_b32 s82, s0, 15\n"
      "s_lshr_b32 s0, s83, 5\n"
      "s_add_u32 s82, s82, s0\n"
      "s_waitcnt lgkmcnt(8)\n"
      "v_mfma_f32_32x32x16_bf16 v[112:127], v[84:87], v[132:135], v[112:127]\n"
      "ds_read_b128 v[148:151], v17 offset:32768\n"
      "ds_read_b64_tr_b16 v[68:69], v21 offset:32768\n"
      "ds_read_b64_tr_b16 v[70:71], v21 offset:33024\n"
      "v_xor_b32 v17, 0x10000, v17\n"
      "v_xor_b32 v21, 0x10000, v21\n"
      "s_add_u32 m0, s35, 99328\n"
      "s_add_u32 s3, s33, 384\n"
      "buffer_load_dwordx4 v47, s[4:7], s3 offen lds\n"
      "s_add_u32 s51, s82, s1\n"
      "s_lshl1_add_u32 s81, s2, s81\n"
      "s_lshl_b32 s81, s81, 12\n"
      "s_add_u32 s81, s81, s83\n"
      "s_add_u32 s54, s81, 1048576\n"
      "s_xor_b32 s53, s54, 9216\n"
      "s_waitcnt lgkmcnt(9)\n"
      "v_mfma_f32_32x32x16_bf16 v[112:127], v[88:91], v[136:139], v[112:127]\n"
      "ds_read_b128 v[152:155], v18 offset:32768\n"
      "ds_read_b64_tr_b16 v[72:73], v22 offset:32768\n"
      "ds_read_b64_tr_b16 v[74:75], v22 offset:33024\n"
      "v_xor_b32 v18, 0x10000, v18\n"
      "v_xor_b32 v22, 0x10000, v22\n"
      "s_add_u32 m0, s34, 114688\n"
      "s_add_u32 s3, s36, 528384\n"
      "buffer_load_dwordx4 v44, s[8:11], s3 offen lds\n"
      "s_waitcnt lgkmcnt(10)\n"
      "v_mfma_f32_32x32x16_bf16 v[112:127], v[92:95], v[140:143], v[112:127]\n"
      "ds_read_b128 v[156:159], v19 offset:32768\n"
      "ds_read_b64_tr_b16 v[76:77], v23 offset:32768\n"
      "ds_read_b64_tr_b16 v[78:79], v23 offset:33024\n"
      "v_xor_b32 v19, 0x10000, v19\n"
      "v_xor_b32 v23, 0x10000, v23\n"
      "s_add_u32 m0, s34, 122880\n"
      "s_add_u32 s3, s37, 528384\n"
      "buffer_load_dwordx4 v44, s[8:11], s3 offen lds\n"
      "s_waitcnt lgkmcnt(9)\n"
      "v_mfma_f32_32x32x16_bf16 v[96:111], v[64:67], v[144:147], v[96:111]\n"
      "v_mfma_f32_16x16x32_bf16 v[48:51], v[12:15], v[64:67], v[48:51]\n"
      "ds_read_b64_tr_b16 v[80:81], v60 offset:0\n"
      "ds_read_b64_tr_b16 v[82:83], v60 offset:256\n"
      "v_add_u32 v60, v60, v31\n"
      "s_waitcnt lgkmcnt(8)\n"
      "v_mfma_f32_32x32x16_bf16 v[96:111], v[68:71], v[148:151], v[96:111]\n"
      "ds_read_b64_tr_b16 v[84:85], v61 offset:0\n"
      "ds_read_b64_tr_b16 v[86:87], v61 offset:256\n"
      "v_add_u32 v61, v61, v31\n"
      "s_waitcnt lgkmcnt(7)\n"
      "v_mfma_f32_32x32x16_bf16 v[96:111], v[72:75], v[152:155], v[96:111]\n"
      "ds_read_b64_tr_b16 v[88:89], v62 offset:0\n"
      "ds_read_b64_tr_b16 v[90:91], v62 offset:256\n"
      "v_add_u32 v62, v62, v31\n"
      "s_waitcnt lgkmcnt(6)\n"
      "v_mfma_f32_32x32x16_bf16 v[96:111], v[76:79], v[156:159], v[96:111]\n"
      "ds_read_b64_tr_b16 v[92:93], v63 offset:0\n"
      "ds_read_b64_tr_b16 v[94:95], v63 offset:256\n"
      "v_add_u32 v63, v63, v31\n"
      "s_cmp_eq_u32 s89, 1\n"
      "s_cselect_b64 exec, -1, 0\n"
      "ds_read_b128 v[24:27], v30\n"
      "s_mov_b64 exec, -1\n"
      "s_waitcnt vmcnt(4)\n"
      "s_barrier\n"
      "s_waitcnt lgkmcnt(7)\n"
      "v_mfma_f32_32x32x16_bf16 v[112:127], v[80:83], v[144:147], v[112:127]\n"
      "v_mfma_f32_16x16x32_bf16 v[52:55], v[12:15], v[80:83], v[52:55]\n"
      "ds_read_b128 v[160:163], v16 offset:0\n"
      "ds_read_b64_tr_b16 v[64:65], v20 offset:0\n"
      "ds_read_b64_tr_b16 v[66:67], v20 offset:256\n"
      "s_add_u32 m0, s35, 0\n"
      "s_add_u32 s3, s33, 512\n"
      "buffer_load_dwordx4 v46, s[4:7], s3 offen lds\n"
      "s_waitcnt lgkmcnt(8)\n"
      "v_mfma_f32_32x32x16_bf16 v[112:127], v[84:87], v[148:151], v[112:127]\n"
      "ds_read_b128 v[164:167], v17 offset:0\n"
      "ds_read_b64_tr_b16 v[68:69], v21 offset:0\n"
      "ds_read_b64_tr_b16 v[70:71], v21 offset:256\n"
      "s_add_u32 m0, s35, 1024\n"
      "s_add_u32 s3, s33, 512\n"
      "buffer_load_dwordx4 v47, s[4:7], s3 offen lds\n"
      "s_waitcnt lgkmcnt(9)\n"
      "v_mfma_f32_32x32x16_bf16 v[112:127], v[88:91], v[152:155], v[112:127]\n"
      "ds_read_b128 v[168:171], v18 offset:0\n"
      "ds_read_b64_tr_b16 v[72:73], v22 offset:0\n"
      "ds_read_b64_tr_b16 v[74:75], v22 offset:256\n"
      "s_add_u32 m0, s34, 16384\n"
      "s_add_u32 s3, s36, 704512\n"
      "buffer_load_dwordx4 v44, s[8:11], s3 offen lds\n"
      "s_waitcnt lgkmcnt(10)\n"
      "v_mfma_f32_32x32x16_bf16 v[112:127], v[92:95], v[156:159], v[112:127]\n"
      "ds_read_b128 v[172:175], v19 offset:0\n"
      "ds_read_b64_tr_b16 v[76:77], v23 offset:0\n"
      "ds_read_b64_tr_b16 v[78:79], v23 offset:256\n"
      "s_add_u32 m0, s34, 24576\n"
      "s_add_u32 s3, s37, 704512\n"
      "buffer_load_dwordx4 v44, s[8:11], s3 offen lds\n"
      "s_waitcnt lgkmcnt(9)\n"
      "v_mfma_f32_32x32x16_bf16 v[96:111], v[64:67], v[160:163], v[96:111]\n"
      "v_mfma_f32_16x16x32_bf16 v[48:51], v[12:15], v[64:67], v[48:51]\n"
      "ds_read_b64_tr_b16 v[80:81], v60 offset:0\n"
      "ds_read_b64_tr_b16 v[82:83], v60 offset:256\n"
      "v_add_u32 v60, v60, v31\n"
      "s_add_u32 m0, s34, 147456\n"
      "s_add_u32 s3, s97, 704512\n"
      "buffer_load_dwordx4 v44, s[8:11], s3 offen lds\n"
      "s_waitcnt lgkmcnt(8)\n"
      "v_mfma_f32_32x32x16_bf16 v[96:111], v[68:71], v[164:167], v[96:111]\n"
      "ds_read_b64_tr_b16 v[84:85], v61 offset:0\n"
      "ds_read_b64_tr_b16 v[86:87], v61 offset:256\n"
      "v_add_u32 v61, v61, v31\n"
      "s_waitcnt lgkmcnt(7)\n"
      "v_mfma_f32_32x32x16_bf16 v[96:111], v[72:75], v[168:171], v[96:111]\n"
      "ds_read_b64_tr_b16 v[88:89], v62 offset:0\n"
      "ds_read_b64_tr_b16 v[90:91], v62 offset:256\n"
      "v_add_u32 v62, v62, v31\n"
      "s_waitcnt lgkmcnt(6)\n"
      "v_mfma_f32_32x32x16_bf16 v[96:111], v[76:79], v[172:175], v[96:111]\n"
      "ds_read_b64_tr_b16 v[92:93], v63 offset:0\n"
      "ds_read_b64_tr_b16 v[94:95], v63 offset:256\n"
      "v_add_u32 v63, v63, v31\n"
      "s_cmp_eq_u32 s89, 2\n"
      "s_cselect_b64 exec, -1, 0\n"
      "ds_read_b128 v[24:27], v30\n"
      "s_mov_b64 exec, -1\n"
      "s_waitcnt vmcnt(5)\n"
      "s_barrier\n"
      "s_waitcnt lgkmcnt(7)\n"
      "v_mfma_f32_32x32x16_bf16 v[112:127], v[80:83], v[160:163], v[112:127]\n"
      "v_mfma_f32_16x16x32_bf16 v[52:55], v[12:15], v[80:83], v[52:55]\n"
      "ds_read_b128 v[176:179], v16 offset:32768\n"
      "ds_read_b64_tr_b16 v[64:65], v20 offset:32768\n"
      "ds_read_b64_tr_b16 v[66:67], v20 offset:33024\n"
      "v_xor_b32 v16, 0x10000, v16\n"
      "v_xor_b32 v20, 0x10000, v20\n"
      "s_add_u32 m0, s35, 32768\n"
      "s_add_u32 s3, s33, 640\n"
      "buffer_load_dwordx4 v46, s[4:7], s3 offen lds\n"
      "s_waitcnt lgkmcnt(8)\n"
      "v_mfma_f32_32x32x16_bf16 v[112:127], v[84:87], v[164:167], v[112:127]\n"
      "ds_read_b128 v[180:183], v17 offset:32768\n"
      "ds_read_b64_tr_b16 v[68:69], v21 offset:32768\n"
      "ds_read_b64_tr_b16 v[70:71], v21 offset:33024\n"
      "v_xor_b32 v17, 0x10000, v17\n"
      "v_xor_b32 v21, 0x10000, v21\n"
      "s_add_u32 m0, s35, 33792\n"
      "s_add_u32 s3, s33, 640\n"
      "buffer_load_dwordx4 v47, s[4:7], s3 offen lds\n"
      "s_waitcnt lgkmcnt(9)\n"
      "v_mfma_f32_32x32x16_bf16 v[112:127], v[88:91], v[168:171], v[112:127]\n"
      "ds_read_b128 v[184:187], v18 offset:32768\n"
      "ds_read_b64_tr_b16 v[72:73], v22 offset:32768\n"
      "ds_read_b64_tr_b16 v[74:75], v22 offset:33024\n"
      "v_xor_b32 v18, 0x10000, v18\n"
      "v_xor_b32 v22, 0x10000, v22\n"
      "s_add_u32 m0, s34, 49152\n"
      "s_add_u32 s3, s36, 880640\n"
      "buffer_load_dwordx4 v44, s[8:11], s3 offen lds\n"
      "s_waitcnt lgkmcnt(10)\n"
      "v_mfma_f32_32x32x16_bf16 v[112:127], v[92:95], v[172:175], v[112:127]\n"
      "ds_read_b128 v[188:191], v19 offset:32768\n"
      "ds_read_b64_tr_b16 v[76:77], v23 offset:32768\n"
      "ds_read_b64_tr_b16 v[78:79], v23 offset:33024\n"
      "v_xor_b32 v19, 0x10000, v19\n"
      "v_xor_b32 v23, 0x10000, v23\n"
      "s_add_u32 m0, s34, 57344\n"
      "s_add_u32 s3, s37, 880640\n"
      "buffer_load_dwordx4 v44, s[8:11], s3 offen lds\n"
      "s_waitcnt lgkmcnt(9)\n"
      "v_mfma_f32_32x32x16_bf16 v[96:111], v[64:67], v[176:179], v[96:111]\n"
      "v_mfma_f32_16x16x32_bf16 v[48:51], v[12:15], v[64:67], v[48:51]\n"
      "ds_read_b64_tr_b16 v[80:81], v60 offset:0\n"
      "ds_read_b64_tr_b16 v[82:83], v60 offset:256\n"
      "v_add_u32 v60, v60, v7\n"
      "s_waitcnt lgkmcnt(8)\n"
      "v_mfma_f32_32x32x16_bf16 v[96:111], v[68:71], v[180:183], v[96:111]\n"
      "ds_read_b64_tr_b16 v[84:85], v61 offset:0\n"
      "ds_read_b64_tr_b16 v[86:87], v61 offset:256\n"
      "v_add_u32 v61, v61, v7\n"
      "s_waitcnt lgkmcnt(7)\n"
      "v_mfma_f32_32x32x16_bf16 v[96:111], v[72:75], v[184:187], v[96:111]\n"
      "ds_read_b64_tr_b16 v[88:89], v62 offset:0\n"
      "ds_read_b64_tr_b16 v[90:91], v62 offset:256\n"
      "v_add_u32 v62, v62, v7\n"
      "s_waitcnt lgkmcnt(6)\n"
      "v_mfma_f32_32x32x16_bf16 v[96:111], v[76:79], v[188:191], v[96:111]\n"
      "ds_read_b64_tr_b16 v[92:93], v63 offset:0\n"
      "ds_read_b64_tr_b16 v[94:95], v63 offset:256\n"
      "v_add_u32 v63, v63, v7\n"
      "s_cmp_eq_u32 s89, 3\n"
      "s_cselect_b64 exec, -1, 0\n"
      "ds_read_b128 v[24:27], v30\n"
      "s_mov_b64 exec, -1\n"
      "s_waitcnt vmcnt(4)\n"
      "s_barrier\n"
      "s_waitcnt lgkmcnt(7)\n"
      "v_mfma_f32_32x32x16_bf16 v[112:127], v[80:83], v[176:179], v[112:127]\n"
      "v_mfma_f32_16x16x32_bf16 v[52:55], v[12:15], v[80:83], v[52:55]\n"
      "ds_read_b128 v[192:195], v16 offset:0\n"
      "ds_read_b64_tr_b16 v[64:65], v20 offset:0\n"
      "ds_read_b64_tr_b16 v[66:67], v20 offset:256\n"
      "s_add_u32 m0, s35, 65536\n"
      "s_add_u32 s3, s33, 768\n"
      "buffer_load_dwordx4 v46, s[4:7], s3 offen lds\n"
      "s_waitcnt lgkmcnt(8)\n"
      "v_mfma_f32_32x32x16_bf16 v[112:127], v[84:87], v[180:183], v[112:127]\n"
      "ds_read_b128 v[196:199], v17 offset:0\n"
      "ds_read_b64_tr_b16 v[68:69], v21 offset:0\n"
      "ds_read_b64_tr_b16 v[70:71], v21 offset:256\n"
      "s_add_u32 m0, s35, 66560\n"
      "s_add_u32 s3, s33, 768\n"
      "buffer_load_dwordx4 v47, s[4:7], s3 offen lds\n"
      "s_waitcnt lgkmcnt(9)\n"
      "v_mfma_f32_32x32x16_bf16 v[112:127], v[88:91], v[184:187], v[112:127]\n"
      "ds_read_b128 v[200:203], v18 offset:0\n"
      "ds_read_b64_tr_b16 v[72:73], v22 offset:0\n"
      "ds_read_b64_tr_b16 v[74:75], v22 offset:256\n"
      "s_add_u32 m0, s34, 81920\n"
      "s_add_u32 s3, s36, 1056768\n"
      "buffer_load_dwordx4 v44, s[8:11], s3 offen lds\n"
      "s_waitcnt lgkmcnt(10)\n"
      "v_mfma_f32_32x32x16_bf16 v[112:127], v[92:95], v[188:191], v[112:127]\n"
      "ds_read_b128 v[204:207], v19 offset:0\n"
      "ds_read_b64_tr_b16 v[76:77], v23 offset:0\n"
      "ds_read_b64_tr_b16 v[78:79], v23 offset:256\n"
      "s_add_u32 m0, s34, 90112\n"
      "s_add_u32 s3, s37, 1056768\n"
      "buffer_load_dwordx4 v44, s[8:11], s3 offen lds\n"
      "s_waitcnt lgkmcnt(9)\n"
      "v_mfma_f32_32x32x16_bf16 v[96:111], v[64:67], v[192:195], v[96:111]\n"
      "v_mfma_f32_16x16x32_bf16 v[48:51], v[12:15], v[64:67], v[48:51]\n"
      "ds_read_b64_tr_b16 v[80:81], v60 offset:0\n"
      "ds_read_b64_tr_b16 v[82:83], v60 offset:256\n"
      "v_add_u32 v60, v60, v31\n"
      "s_add_u32 m0, s34, 155648\n"
      "s_add_u32 s3, s97, 1056768\n"
      "buffer_load_dwordx4 v44, s[8:11], s3 offen lds\n"
      "s_waitcnt lgkmcnt(8)\n"
      "v_mfma_f32_32x32x16_bf16 v[96:111], v[68:71], v[196:199], v[96:111]\n"
      "ds_read_b64_tr_b16 v[84:85], v61 offset:0\n"
      "ds_read_b64_tr_b16 v[86:87], v61 offset:256\n"
      "v_add_u32 v61, v61, v31\n"
      "s_waitcnt lgkmcnt(7)\n"
      "v_mfma_f32_32x32x16_bf16 v[96:111], v[72:75], v[200:203], v[96:111]\n"
      "ds_read_b64_tr_b16 v[88:89], v62 offset:0\n"
      "ds_read_b64_tr_b16 v[90:91], v62 offset:256\n"
      "v_add_u32 v62, v62, v31\n"
      "s_waitcnt lgkmcnt(6)\n"
      "v_mfma_f32_32x32x16_bf16 v[96:111], v[76:79], v[204:207], v[96:111]\n"
      "ds_read_b64_tr_b16 v[92:93], v63 offset:0\n"
      "ds_read_b64_tr_b16 v[94:95], v63 offset:256\n"
      "v_add_u32 v63, v63, v31\n"
      "s_cmp_eq_u32 s89, 4\n"
      "s_cselect_b64 exec, -1, 0\n"
      "ds_read_b128 v[24:27], v30\n"
      "s_mov_b64 exec, -1\n"
      "s_waitcnt vmcnt(5)\n"
      "s_barrier\n"
      "s_waitcnt lgkmcnt(7)\n"
      "v_mfma_f32_32x32x16_bf16 v[112:127], v[80:83], v[192:195], v[112:127]\n"
      "v_mfma_f32_16x16x32_bf16 v[52:55], v[12:15], v[80:83], v[52:55]\n"
      "ds_read_b128 v[208:211], v16 offset:32768\n"
      "ds_read_b64_tr_b16 v[64:65], v20 offset:32768\n"
      "ds_read_b64_tr_b16 v[66:67], v20 offset:33024\n"
      "v_xor_b32 v16, 0x10000, v16\n"
      "v_xor_b32 v20, 0x10000, v20\n"
      "buffer_load_dwordx4 v[32:35], v36, s[12:15], s56 offen sc1\n"
      "s_waitcnt lgkmcnt(8)\n"
      "v_mfma_f32_32x32x16_bf16 v[112:127], v[84:87], v[196:199], v[112:127]\n"
      "ds_read_b128 v[212:215], v17 offset:32768\n"
      "ds_read_b64_tr_b16 v[68:69], v21 offset:32768\n"
      "ds_read_b64_tr_b16 v[70:71], v21 offset:33024\n"
      "v_xor_b32 v17, 0x10000, v17\n"
      "v_xor_b32 v21, 0x10000, v21\n"
      "s_add_u32 m0, s35, 98304\n"
      "s_add_u32 s3, s33, 896\n"
      "buffer_load_dwordx4 v46, s[4:7], s3 offen lds\n"
      "s_waitcnt lgkmcnt(9)\n"
      "v_mfma_f32_32x32x16_bf16 v[112:127], v[88:91], v[200:203], v[112:127]\n"
      "ds_read_b128 v[216:219], v18 offset:32768\n"
      "ds_read_b64_tr_b16 v[72:73], v22 offset:32768\n"
      "ds_read_b64_tr_b16 v[74:75], v22 offset:33024\n"
      "v_xor_b32 v18, 0x10000, v18\n"
      "v_xor_b32 v22, 0x10000, v22\n"
      "s_add_u32 m0, s35, 99328\n"
      "s_add_u32 s3, s33, 896\n"
      "buffer_load_dwordx4 v47, s[4:7], s3 offen lds\n"
      "s_waitcnt lgkmcnt(10)\n"
      "v_mfma_f32_32x32x16_bf16 v[112:127], v[92:95], v[204:207], v[112:127]\n"
      "ds_read_b128 v[220:223], v19 offset:32768\n"
      "ds_read_b64_tr_b16 v[76:77], v23 offset:32768\n"
      "ds_read_b64_tr_b16 v[78:79], v23 offset:33024\n"
      "v_xor_b32 v19, 0x10000, v19\n"
      "v_xor_b32 v23, 0x10000, v23\n"
      "s_add_u32 m0, s34, 114688\n"
      "s_add_u32 s3, s36, 1232896\n"
      "buffer_load_dwordx4 v44, s[8:11], s3 offen lds\n"
      "s_waitcnt lgkmcnt(9)\n"
      "v_mfma_f32_32x32x16_bf16 v[96:111], v[64:67], v[208:211], v[96:111]\n"
      "v_mfma_f32_16x16x32_bf16 v[48:51], v[12:15], v[64:67], v[48:51]\n"
      "ds_read_b64_tr_b16 v[80:81], v60 offset:0\n"
      "ds_read_b64_tr_b16 v[82:83], v60 offset:256\n"
      "v_add_u32 v60, v60, v31\n"
      "s_add_u32 m0, s34, 122880\n"
      "s_add_u32 s3, s37, 1232896\n"
      "buffer_load_dwordx4 v44, s[8:11], s3 offen lds\n"
      "s_waitcnt lgkmcnt(8)\n"
      "v_mfma_f32_32x32x16_bf16 v[96:111], v[68:71], v[212:215], v[96:111]\n"
      "ds_read_b64_tr_b16 v[84:85], v61 offset:0\n"
      "ds_read_b64_tr_b16 v[86:87], v61 offset:256\n"
      "v_add_u32 v61, v61, v31\n"
      "s_waitcnt lgkmcnt(7)\n"
      "v_mfma_f32_32x32x16_bf16 v[96:111], v[72:75], v[216:219], v[96:111]\n"
      "ds_read_b64_tr_b16 v[88:89], v62 offset:0\n"
      "ds_read_b64_tr_b16 v[90:91], v62 offset:256\n"
      "v_add_u32 v62, v62, v31\n"
      "s_waitcnt lgkmcnt(6)\n"
      "v_mfma_f32_32x32x16_bf16 v[96:111], v[76:79], v[220:223], v[96:111]\n"
      "ds_read_b64_tr_b16 v[92:93], v63 offset:0\n"
      "ds_read_b64_tr_b16 v[94:95], v63 offset:256\n"
      "v_add_u32 v63, v63, v31\n"
      "s_cmp_eq_u32 s89, 5\n"
      "s_cselect_b64 exec, -1, 0\n"
      "ds_read_b128 v[24:27], v30\n"
      "s_mov_b64 exec, -1\n"
      "s_waitcnt vmcnt(5) lgkmcnt(0)\n"
      "s_barrier\n"
      "s_memrealtime s[90:91]\n"
      "s_waitcnt lgkmcnt(0)\n"
      "v_mfma_f32_32x32x16_bf16 v[112:127], v[80:83], v[208:211], v[112:127]\n"
      "v_mfma_f32_16x16x32_bf16 v[52:55], v[12:15], v[80:83], v[52:55]\n"
      "ds_read_b128 v[224:227], v16 offset:0\n"
      "ds_read_b64_tr_b16 v[64:65], v20 offset:0\n"
      "ds_read_b64_tr_b16 v[66:67], v20 offset:256\n"
      "v_mov_b32 v42, s90\n"
      "s_lshl3_add_u32 s3, s2, 32768\n"
      "buffer_store_dwordx2 v[42:43], v39, s[12:15], s3 offen sc1\n"
      "v_mfma_f32_32x32x16_bf16 v[112:127], v[84:87], v[212:215], v[112:127]\n"
      "ds_read_b128 v[228:231], v17 offset:0\n"
      "ds_read_b64_tr_b16 v[68:69], v21 offset:0\n"
      "ds_read_b64_tr_b16 v[70:71], v21 offset:256\n"
      "s_add_u32 m0, s96, 0\n"
      "s_mov_b64 exec, s[98:99]\n"
      "buffer_load_dwordx4 v44, s[8:11], s38 offen lds\n"
      "s_mov_b64 exec, -1\n"
      "v_mfma_f32_32x32x16_bf16 v[112:127], v[88:91], v[216:219], v[112:127]\n"
      "ds_read_b128 v[232:235], v18 offset:0\n"
      "ds_read_b64_tr_b16 v[72:73], v22 offset:0\n"
      "ds_read_b64_tr_b16 v[74:75], v22 offset:256\n"
      "s_add_u32 m0, s96, 4096\n"
      "s_add_u32 s3, s38, 176128\n"
      "s_mov_b64 exec, s[98:99]\n"
      "buffer_load_dwordx4 v44, s[8:11], s3 offen lds\n"
      "s_mov_b64 exec, -1\n"
      "v_mfma_f32_32x32x16_bf16 v[112:127], v[92:95], v[220:223], v[112:127]\n"
      "ds_read_b128 v[236:239], v19 offset:0\n"
      "ds_read_b64_tr_b16 v[76:77], v23 offset:0\n"
      "ds_read_b64_tr_b16 v[78:79], v23 offset:256\n"
      "s_add_u32 m0, s96, 8192\n"
      "s_add_u32 s3, s38, 352256\n"
      "s_mov_b64 exec, s[98:99]\n"
      "buffer_load_dwordx4 v44, s[8:11], s3 offen lds\n"
      "s_mov_b64 exec, -1\n"
      "s_waitcnt vmcnt(8)\n"
      "v_xor_b32 v4, s65, v34\n"
      "v_bitop3_b32 v4, v35, s65, v4 bitop3:0xbe\n"
      "v_cmp_ne_u32 vcc, 0, v4\n"
      "s_nop 1\n"
      "s_cbranch_vccnz LBr1\n"
      "LBo1:\n"
      "s_waitcnt lgkmcnt(9)\n"
      "v_mfma_f32_32x32x16_bf16 v[96:111], v[64:67], v[224:227], v[96:111]\n"
      "v_mfma_f32_16x16x32_bf16 v[48:51], v[12:15], v[64:67], v[48:51]\n"
      "ds_read_b64_tr_b16 v[80:81], v60 offset:0\n"
      "ds_read_b64_tr_b16 v[82:83], v60 offset:256\n"
      "v_add_u32 v60, v60, v31\n"
      "s_add_u32 m0, s96, 12288\n"
      "s_add_u32 s3, s38, 528384\n"
      "s_mov_b64 exec, s[98:99]\n"
      "buffer_load_dwordx4 v44, s[8:11], s3 offen lds\n"
      "s_mov_b64 exec, -1\n"
      "v_perm_b32 v8, v32, v32, v37\n"
      "v_mov_b32 v3, v33\n"
      "v_mov_b32 v9, v8\n"
      "v_mov_b32 v10, v8\n"
      "s_waitcnt lgkmcnt(8)\n"
      "v_mfma_f32_32x32x16_bf16 v[96:111], v[68:71], v[228:231], v[96:111]\n"
      "ds_read_b64_tr_b16 v[84:85], v61 offset:0\n"
      "ds_read_b64_tr_b16 v[86:87], v61 offset:256\n"
      "v_add_u32 v61, v61, v31\n"
      "s_add_u32 m0, s96, 16384\n"
      "s_add_u32 s3, s38, 704512\n"
      "s_mov_b64 exec, s[98:99]\n"
      "buffer_load_dwordx4 v44, s[8:11], s3 offen lds\n"
      "s_mov_b64 exec, -1\n"
      "v_mov_b32 v11, v8\n"
      "s_waitcnt lgkmcnt(7)\n"
      "v_mfma_f32_32x32x16_bf16 v[96:111], v[72:75], v[232:235], v[96:111]\n"
      "ds_read_b64_tr_b16 v[88:89], v62 offset:0\n"
      "ds_read_b64_tr_b16 v[90:91], v62 offset:256\n"
      "v_add_u32 v62, v62, v31\n"
      "s_add_u32 m0, s96, 20480\n"
      "s_add_u32 s3, s38, 880640\n"
      "s_mov_b64 exec, s[98:99]\n"
      "buffer_load_dwordx4 v44, s[8:11], s3 offen lds\n"
      "s_mov_b64 exec, -1\n"
      "s_waitcnt lgkmcnt(6)\n"
      "v_mfma_f32_32x32x16_bf16 v[96:111], v[76:79], v[236:239], v[96:111]\n"
      "ds_read_b64_tr_b16 v[92:93], v63 offset:0\n"
      "ds_read_b64_tr_b16 v[94:95], v63 offset:256\n"
      "v_add_u32 v63, v63, v31\n"
      "s_cmp_eq_u32 s89, 6\n"
      "s_cselect_b64 exec, -1, 0\n"
      "ds_read_b128 v[24:27], v30\n"
      "s_mov_b64 exec, -1\n"
      "s_waitcnt vmcnt(7)\n"
      "s_barrier\n"
      "s_waitcnt lgkmcnt(7)\n"
      "v_mfma_f32_32x32x16_bf16 v[112:127], v[80:83], v[224:227], v[112:127]\n"
      "v_mfma_f32_16x16x32_bf16 v[52:55], v[12:15], v[80:83], v[52:55]\n"
      "ds_read_b128 v[240:243], v16 offset:32768\n"
      "ds_read_b64_tr_b16 v[64:65], v20 offset:32768\n"
      "ds_read_b64_tr_b16 v[66:67], v20 offset:33024\n"
      "ds_read_b64_tr_b16 v[80:81], v60 offset:0\n"
      "ds_read_b64_tr_b16 v[82:83], v60 offset:256\n"
      "s_lshl3_add_u32 s3, s31, s32\n"
      "s_lshl3_add_u32 s3, s3, s28\n"
      "s_mul_i32 s3, s3, 1408\n"
      "v_lshlrev_b32 v57, 7, v0\n"
      "s_mov_b64 exec, 2047\n"
      "buffer_load_dword v57, v57, s[16:19], s3 offen\n"
      "s_mov_b64 exec, -1\n"
      "s_waitcnt lgkmcnt(10)\n"
      "v_mfma_f32_32x32x16_bf16 v[112:127], v[84:87], v[228:231], v[112:127]\n"
      "ds_read_b128 v[244:247], v17 offset:32768\n"
      "ds_read_b64_tr_b16 v[68:69], v21 offset:32768\n"
      "ds_read_b64_tr_b16 v[70:71], v21 offset:33024\n"
      "ds_read_b64_tr_b16 v[84:85], v61 offset:0\n"
      "ds_read_b64_tr_b16 v[86:87], v61 offset:256\n"
      "s_andn2_b32 s78, s2, 7\n"
      "s_add_u32 s78, s78, s28\n"
      "s_mul_i32 s3, s78, 6144\n"
      "v_lshlrev_b32 v56, 10, v0\n"
      "s_mov_b64 exec, 63\n"
      "buffer_load_dword v58, v56, s[16:19], s3 offen offset:384\n"
      "s_lshl_b32 s3, s78, 11\n"
      "s_mov_b64 exec, 3\n"
      "buffer_load_dword v59, v56, s[20:23], s3 offen offset:384\n"
      "s_mov_b64 exec, -1\n"
      "s_waitcnt lgkmcnt(13)\n"
      "v_mfma_f32_32x32x16_bf16 v[112:127], v[88:91], v[232:235], v[112:127]\n"
      "ds_read_b128 v[248:251], v18 offset:32768\n"
      "ds_read_b64_tr_b16 v[72:73], v22 offset:32768\n"
      "ds_read_b64_tr_b16 v[74:75], v22 offset:33024\n"
      "ds_read_b64_tr_b16 v[88:89], v62 offset:0\n"
      "ds_read_b64_tr_b16 v[90:91], v62 offset:256\n"
      "s_waitcnt lgkmcnt(15)\n"
      "v_mfma_f32_32x32x16_bf16 v[112:127], v[92:95], v[236:239], v[112:127]\n"
      "ds_read_b128 v[252:255], v19 offset:32768\n"
      "ds_read_b64_tr_b16 v[76:77], v23 offset:32768\n"
      "ds_read_b64_tr_b16 v[78:79], v23 offset:33024\n"
      "ds_read_b64_tr_b16 v[92:93], v63 offset:0\n"
      "ds_read_b64_tr_b16 v[94:95], v63 offset:256\n"
      "s_cmp_eq_u32 s89, 7\n"
      "s_cselect_b64 exec, -1, 0\n"
      "ds_read_b128 v[24:27], v30\n"
      "s_mov_b64 exec, -1\n"
      "s_waitcnt lgkmcnt(15)\n"
      "v_mfma_f32_16x16x32_bf16 v[48:51], v[12:15], v[64:67], v[48:51]\n"
      "v_mfma_f32_16x16x32_bf16 v[52:55], v[12:15], v[80:83], v[52:55]\n"
      "v_mfma_f32_32x32x16_bf16 v[96:111], v[64:67], v[240:243], v[96:111]\n"
      "v_add_u32 v20, v20, v45\n"
      "v_add_u32 v21, v21, v45\n"
      "v_add_u32 v22, v22, v45\n"
      "v_add_u32 v23, v23, v45\n"
      "s_nop 1\n"
      "v_cvt_pk_bf16_f32 v49, v48, v48\n"
      "v_cvt_pk_bf16_f32 v53, v52, v52\n"
      "v_lshlrev_b32 v49, 16, v49\n"
      "v_lshlrev_b32 v53, 16, v53\n"
      "v_sub_f32 v49, v48, v49\n"
      "v_sub_f32 v53, v52, v53\n"
      "v_cvt_pk_bf16_f32 v49, v48, v49\n"
      "v_cvt_pk_bf16_f32 v53, v52, v53\n"
      "ds_write_b32 v29, v49 offset:0\n"
      "ds_write_b32 v29, v53 offset:2048\n"
      "s_waitcnt lgkmcnt(0)\n"
      "v_mfma_f32_32x32x16_bf16 v[96:111], v[68:71], v[244:247], v[96:111]\n"
      "s_waitcnt vmcnt(5) lgkmcnt(0)\n"
      "s_barrier\n"
      "v_mfma_f32_32x32x16_bf16 v[96:111], v[72:75], v[248:251], v[96:111]\n"
      "ds_read_b128 v[48:51], v28 offset:0\n"
      "ds_read_b128 v[52:55], v28 offset:2048\n"
      "ds_read_b64_tr_b16 v[64:65], v20 offset:0\n"
      "ds_read_b64_tr_b16 v[66:67], v20 offset:256\n"
      "ds_read_b64_tr_b16 v[68:69], v21 offset:0\n"
      "ds_read_b64_tr_b16 v[70:71], v21 offset:256\n"
      "s_add_u32 m0, s96, 24576\n"
      "s_add_u32 s3, s38, 1056768\n"
      "s_mov_b64 exec, s[98:99]\n"
      "buffer_load_dwordx4 v44, s[8:11], s3 offen lds\n"
      "s_mov_b64 exec, -1\n"
      "v_mfma_f32_32x32x16_bf16 v[96:111], v[76:79], v[252:255], v[96:111]\n"
      "ds_read_b64_tr_b16 v[72:73], v22 offset:0\n"
      "ds_read_b64_tr_b16 v[74:75], v22 offset:256\n"
      "ds_read_b64_tr_b16 v[76:77], v23 offset:0\n"
      "ds_read_b64_tr_b16 v[78:79], v23 offset:256\n"
      "s_add_u32 m0, s96, 28672\n"
      "s_add_u32 s3, s38, 1232896\n"
      "s_mov_b64 exec, s[98:99]\n"
      "buffer_load_dwordx4 v44, s[8:11], s3 offen lds\n"
      "s_mov_b64 exec, -1\n"
      "v_mfma_f32_32x32x16_bf16 v[112:127], v[80:83], v[240:243], v[112:127]\n"
      "ds_read_b64_tr_b16 v[80:81], v20 offset:4096\n"
      "ds_read_b64_tr_b16 v[82:83], v20 offset:4352\n"
      "s_add_u32 m0, s34, 98304\n"
      "s_nop 0\n"
      "buffer_load_dwordx4 v6, s[16:19], s41 offen lds\n"
      "s_waitcnt lgkmcnt(11)\n"
      "v_mfma_f32_32x32x16_bf16 v[96:111], v[48:51], v[8:11], v[96:111]\n"
      "v_mfma_f32_32x32x16_bf16 v[112:127], v[84:87], v[244:247], v[112:127]\n"
      "ds_read_b64_tr_b16 v[84:85], v21 offset:4096\n"
      "ds_read_b64_tr_b16 v[86:87], v21 offset:4352\n"
      "s_add_u32 m0, s34, 106496\n"
      "s_nop 0\n"
      "buffer_load_dwordx4 v6, s[16:19], s42 offen lds\n"
      "s_nop 5\n"
      "v_mul_f32 v96, v96, v3\n"
      "v_mul_f32 v97, v97, v3\n"
      "v_cvt_pk_bf16_f32 v96, v96, v97\n"
      "v_pk_max_i16 v96, v96, 0\n"
      "v_mfma_f32_32x32x16_bf16 v[112:127], v[88:91], v[248:251], v[112:127]\n"
      "ds_read_b64_tr_b16 v[88:89], v22 offset:4096\n"
      "ds_read_b64_tr_b16 v[90:91], v22 offset:4352\n"
      "s_add_u32 m0, s34, 114688\n"
      "s_nop 0\n"
      "buffer_load_dwordx4 v6, s[16:19], s43 offen lds\n"
      "v_or_b32 v96, s57, v96\n"
      "v_mul_f32 v98, v98, v3\n"
      "v_mul_f32 v99, v99, v3\n"
      "v_cvt_pk_bf16_f32 v97, v98, v99\n"
      "v_mfma_f32_32x32x16_bf16 v[112:127], v[92:95], v[252:255], v[112:127]\n"
      "ds_read_b64_tr_b16 v[92:93], v23 offset:4096\n"
      "ds_read_b64_tr_b16 v[94:95], v23 offset:4352\n"
      "s_add_u32 m0, s34, 122880\n"
      "s_nop 0\n"
      "buffer_load_dwordx4 v6, s[16:19], s44 offen lds\n"
      "s_waitcnt lgkmcnt(15)\n"
      "v_mfma_f32_32x32x16_bf16 v[112:127], v[52:55], v[8:11], v[112:127]\n"
      "v_pk_max_i16 v97, v97, 0\n"
      "v_or_b32 v97, s58, v97\n"
      "v_mul_f32 v100, v100, v3\n"
      "v_mul_f32 v101, v101, v3\n"
      "s_waitcnt vmcnt(6) lgkmcnt(14)\n"
      "v_mfma_f32_16x16x32_bf16 v[56:59], v[12:15], v[64:67], 0\n"
      "v_mfma_f32_32x32x16_bf16 v[32:47], v[64:67], v[128:131], 0\n"
      "ds_read_b64_tr_b16 v[64:65], v20 offset:8192\n"
      "ds_read_b64_tr_b16 v[66:67], v20 offset:8448\n"
      "s_mov_b32 s3, 32768\n"
      "buffer_load_dwordx4 v[48:51], v1, s[12:15], s3 offen offset:0 sc1\n"
      "v_cvt_pk_bf16_f32 v98, v100, v101\n"
      "v_pk_max_i16 v98, v98, 0\n"
      "v_or_b32 v98, s59, v98\n"
      "v_mul_f32 v102, v102, v3\n"
      "s_waitcnt lgkmcnt(14)\n"
      "v_mfma_f32_32x32x16_bf16 v[32:47], v[68:71], v[132:135], v[32:47]\n"
      "ds_read_b64_tr_b16 v[68:69], v21 offset:8192\n"
      "ds_read_b64_tr_b16 v[70:71], v21 offset:8448\n"
      "s_mov_b32 s3, 32768\n"
      "buffer_load_dwordx4 v[52:55], v1, s[12:15], s3 offen offset:1024 sc1\n"
      "v_mul_f32 v103, v103, v3\n"
      "v_cvt_pk_bf16_f32 v99, v102, v103\n"
      "v_pk_max_i16 v99, v99, 0\n"
      "v_or_b32 v99, s60, v99\n"
      "s_waitcnt lgkmcnt(14)\n"
      "v_mfma_f32_32x32x16_bf16 v[32:47], v[72:75], v[136:139], v[32:47]\n"
      "ds_read_b64_tr_b16 v[72:73], v22 offset:8192\n"
      "ds_read_b64_tr_b16 v[74:75], v22 offset:8448\n"
      "buffer_store_dwordx4 v[96:99], v1, s[12:15], s47 offen offset:0 sc1\n"
      "v_mul_f32 v104, v104, v3\n"
      "v_mul_f32 v105, v105, v3\n"
      "v_cvt_pk_bf16_f32 v100, v104, v105\n"
      "v_pk_max_i16 v100, v100, 0\n"
      "s_waitcnt lgkmcnt(14)\n"
      "v_mfma_f32_32x32x16_bf16 v[32:47], v[76:79], v[140:143], v[32:47]\n"
      "ds_read_b64_tr_b16 v[76:77], v23 offset:8192\n"
      "ds_read_b64_tr_b16 v[78:79], v23 offset:8448\n"
      "v_or_b32 v100, s57, v100\n"
      "v_mul_f32 v106, v106, v3\n"
      "v_mul_f32 v107, v107, v3\n"
      "v_cvt_pk_bf16_f32 v101, v106, v107\n"
      "s_waitcnt lgkmcnt(14)\n"
      "v_mfma_f32_16x16x32_bf16 v[56:59], v[12:15], v[80:83], v[56:59]\n"
      "v_mfma_f32_32x32x16_bf16 v[32:47], v[80:83], v[144:147], v[32:47]\n"
      "ds_read_b64_tr_b16 v[80:81], v20 offset:12288\n"
      "ds_read_b64_tr_b16 v[82:83], v20 offset:12544\n"
      "v_pk_max_i16 v101, v101, 0\n"
      "v_or_b32 v101, s58, v101\n"
      "v_mul_f32 v108, v108, v3\n"
      "v_mul_f32 v109, v109, v3\n"
      "s_waitcnt lgkmcnt(14)\n"
      "v_mfma_f32_32x32x16_bf16 v[32:47], v[84:87], v[148:151], v[32:47]\n"
      "ds_read_b64_tr_b16 v[84:85], v21 offset:12288\n"
      "ds_read_b64_tr_b16 v[86:87], v21 offset:12544\n"
      "v_cvt_pk_bf16_f32 v102, v108, v109\n"
      "v_pk_max_i16 v102, v102, 0\n"
      "v_or_b32 v102, s59, v102\n"
      "v_mul_f32 v110, v110, v3\n"
      "s_waitcnt lgkmcnt(14)\n"
      "v_mfma_f32_32x32x16_bf16 v[32:47], v[88:91], v[152:155], v[32:47]\n"
      "ds_read_b64_tr_b16 v[88:89], v22 offset:12288\n"
      "ds_read_b64_tr_b16 v[90:91], v22 offset:12544\n"
      "v_mul_f32 v111, v111, v3\n"
      "v_cvt_pk_bf16_f32 v103, v110, v111\n"
      "v_pk_max_i16 v103, v103, 0\n"
      "v_or_b32 v103, s60, v103\n"
      "s_waitcnt lgkmcnt(14)\n"
      "v_mfma_f32_32x32x16_bf16 v[32:47], v[92:95], v[156:159], v[32:47]\n"
      "ds_read_b64_tr_b16 v[92:93], v23 offset:12288\n"
      "ds_read_b64_tr_b16 v[94:95], v23 offset:12544\n"
      "buffer_store_dwordx4 v[100:103], v1, s[12:15], s47 offen offset:1024 sc1\n"
      "v_mul_f32 v112, v112, v3\n"
      "v_mul_f32 v113, v113, v3\n"
      "v_cvt_pk_bf16_f32 v112, v112, v113\n"
      "v_pk_max_i16 v112, v112, 0\n"
      "s_waitcnt lgkmcnt(14)\n"
      "v_mfma_f32_16x16x32_bf16 v[56:59], v[12:15], v[64:67], v[56:59]\n"
      "v_mfma_f32_32x32x16_bf16 v[32:47], v[64:67], v[160:163], v[32:47]\n"
      "v_or_b32 v112, s57, v112\n"
      "v_mul_f32 v114, v114, v3\n"
      "v_mul_f32 v115, v115, v3\n"
      "v_cvt_pk_bf16_f32 v113, v114, v115\n"
      "s_waitcnt lgkmcnt(12)\n"
      "v_mfma_f32_32x32x16_bf16 v[32:47], v[68:71], v[164:167], v[32:47]\n"
      "s_waitcnt vmcnt(8) lgkmcnt(0)\n"
      "s_barrier\n"
      "v_pk_max_i16 v113, v113, 0\n"
      "v_or_b32 v113, s58, v113\n"
      "v_mul_f32 v116, v116, v3\n"
      "v_mul_f32 v117, v117, v3\n"
      "v_mfma_f32_32x32x16_bf16 v[32:47], v[72:75], v[168:171], v[32:47]\n"
      "ds_read_b64_tr_b16 v[64:65], v20 offset:16384\n"
      "ds_read_b64_tr_b16 v[66:67], v20 offset:16640\n"
      "ds_read_b64_tr_b16 v[68:69], v21 offset:16384\n"
      "ds_read_b64_tr_b16 v[70:71], v21 offset:16640\n"
      "v_cvt_pk_bf16_f32 v114, v116, v117\n"
      "v_pk_max_i16 v114, v114, 0\n"
      "v_or_b32 v114, s59, v114\n"
      "v_mul_f32 v118, v118, v3\n"
      "v_mfma_f32_32x32x16_bf16 v[32:47], v[76:79], v[172:175], v[32:47]\n"
      "ds_read_b64_tr_b16 v[72:73], v22 offset:16384\n"
      "ds_read_b64_tr_b16 v[74:75], v22 offset:16640\n"
      "ds_read_b64_tr_b16 v[76:77], v23 offset:16384\n"
      "ds_read_b64_tr_b16 v[78:79], v23 offset:16640\n"
      "v_mul_f32 v119, v119, v3\n"
      "v_cvt_pk_bf16_f32 v115, v118, v119\n"
      "v_pk_max_i16 v115, v115, 0\n"
      "v_or_b32 v115, s60, v115\n"
      "v_mfma_f32_16x16x32_bf16 v[56:59], v[12:15], v[80:83], v[56:59]\n"
      "v_mfma_f32_32x32x16_bf16 v[32:47], v[80:83], v[176:179], v[32:47]\n"
      "ds_read_b64_tr_b16 v[80:81], v20 offset:20480\n"
      "ds_read_b64_tr_b16 v[82:83], v20 offset:20736\n"
      "s_add_u32 m0, s34, 65536\n"
      "s_add_u32 s3, s41, 65536\n"
      "buffer_load_dwordx4 v6, s[16:19], s3 offen lds\n"
      "buffer_store_dwordx4 v[112:115], v1, s[12:15], s68 offen offset:0 sc1\n"
      "v_mul_f32 v120, v120, v3\n"
      "v_mul_f32 v121, v121, v3\n"
      "v_cvt_pk_bf16_f32 v116, v120, v121\n"
      "v_pk_max_i16 v116, v116, 0\n"
      "v_mfma_f32_32x32x16_bf16 v[32:47], v[84:87], v[180:183], v[32:47]\n"
      "ds_read_b64_tr_b16 v[84:85], v21 offset:20480\n"
      "ds_read_b64_tr_b16 v[86:87], v21 offset:20736\n"
      "v_or_b32 v116, s57, v116\n"
      "v_mul_f32 v122, v122, v3\n"
      "v_mul_f32 v123, v123, v3\n"
      "v_cvt_pk_bf16_f32 v117, v122, v123\n"
      "v_mfma_f32_32x32x16_bf16 v[32:47], v[88:91], v[184:187], v[32:47]\n"
      "ds_read_b64_tr_b16 v[88:89], v22 offset:20480\n"
      "ds_read_b64_tr_b16 v[90:91], v22 offset:20736\n"
      "s_add_u32 m0, s34, 73728\n"
      "s_add_u32 s3, s42, 65536\n"
      "buffer_load_dwordx4 v6, s[16:19], s3 offen lds\n"
      "v_pk_max_i16 v117, v117, 0\n"
      "v_or_b32 v117, s58, v117\n"
      "v_mul_f32 v124, v124, v3\n"
      "v_mul_f32 v125, v125, v3\n"
      "v_mfma_f32_32x32x16_bf16 v[32:47], v[92:95], v[188:191], v[32:47]\n"
      "ds_read_b64_tr_b16 v[92:93], v23 offset:20480\n"
      "ds_read_b64_tr_b16 v[94:95], v23 offset:20736\n"
      "v_cvt_pk_bf16_f32 v118, v124, v125\n"
      "v_pk_max_i16 v118, v118, 0\n"
      "v_or_b32 v118, s59, v118\n"
      "v_mul_f32 v126, v126, v3\n"
      "s_waitcnt lgkmcnt(14)\n"
      "v_mfma_f32_16x16x32_bf16 v[56:59], v[12:15], v[64:67], v[56:59]\n"
      "v_mfma_f32_32x32x16_bf16 v[32:47], v[64:67], v[192:195], v[32:47]\n"
      "ds_read_b64_tr_b16 v[64:65], v20 offset:24576\n"
      "ds_read_b64_tr_b16 v[66:67], v20 offset:24832\n"
      "s_add_u32 m0, s34, 81920\n"
      "s_add_u32 s3, s43, 65536\n"
      "buffer_load_dwordx4 v6, s[16:19], s3 offen lds\n"
      "v_mul_f32 v127, v127, v3\n"
      "v_cvt_pk_bf16_f32 v119, v126, v127\n"
      "v_pk_max_i16 v119, v119, 0\n"
      "v_or_b32 v119, s60, v119\n"
      "s_waitcnt lgkmcnt(14)\n"
      "v_mfma_f32_32x32x16_bf16 v[32:47], v[68:71], v[196:199], v[32:47]\n"
      "ds_read_b64_tr_b16 v[68:69], v21 offset:24576\n"
      "ds_read_b64_tr_b16 v[70:71], v21 offset:24832\n"
      "buffer_store_dwordx4 v[116:119], v1, s[12:15], s68 offen offset:1024 sc1\n"
      "s_waitcnt lgkmcnt(14)\n"
      "v_mfma_f32_32x32x16_bf16 v[32:47], v[72:75], v[200:203], v[32:47]\n"
      "ds_read_b64_tr_b16 v[72:73], v22 offset:24576\n"
      "ds_read_b64_tr_b16 v[74:75], v22 offset:24832\n"
      "s_add_u32 m0, s34, 90112\n"
      "s_add_u32 s3, s44, 65536\n"
      "buffer_load_dwordx4 v6, s[16:19], s3 offen lds\n"
      "s_waitcnt vmcnt(9)\n"
      "v_xor_b32 v4, s65, v49\n"
      "v_bitop3_b32 v4, v51, s65, v4 bitop3:0xbe\n"
      "s_waitcnt vmcnt(8)\n"
      "v_bitop3_b32 v4, v53, s65, v4 bitop3:0xbe\n"
      "v_bitop3_b32 v4, v55, s65, v4 bitop3:0xbe\n"
      "v_cmp_ne_u32 vcc, 0, v4\n"
      "s_nop 1\n"
      "s_cbranch_vccnz LBr2\n"
      "LBo2:\n"
      "v_subrev_u32 v48, s90, v48\n"
      "v_subrev_u32 v50, s90, v50\n"
      "v_subrev_u32 v52, s90, v52\n"
      "v_subrev_u32 v54, s90, v54\n"
      "s_waitcnt lgkmcnt(14)\n"
      "v_mfma_f32_32x32x16_bf16 v[32:47], v[76:79], v[204:207], v[32:47]\n"
      "ds_read_b64_tr_b16 v[76:77], v23 offset:24576\n"
      "ds_read_b64_tr_b16 v[78:79], v23 offset:24832\n"
      "v_max_i32 v48, v50, v54\n"
      "s_nop 1\n"
      "v_max_i32_dpp v48, v48, v48 quad_perm:[1,0,3,2] row_mask:0xf bank_mask:0xf\n"
      "s_nop 1\n"
      "v_max_i32_dpp v48, v48, v48 quad_perm:[2,3,0,1] row_mask:0xf bank_mask:0xf\n"
      "s_nop 1\n"
      "v_max_i32_dpp v48, v48, v48 row_half_mirror row_mask:0xf bank_mask:0xf\n"
      "s_waitcnt lgkmcnt(14)\n"
      "v_mfma_f32_16x16x32_bf16 v[56:59], v[12:15], v[80:83], v[56:59]\n"
      "v_mfma_f32_32x32x16_bf16 v[32:47], v[80:83], v[208:211], v[32:47]\n"
      "ds_read_b64_tr_b16 v[80:81], v20 offset:28672\n"
      "ds_read_b64_tr_b16 v[82:83], v20 offset:28928\n"
      "v_max_i32_dpp v48, v48, v48 row_mirror row_mask:0xf bank_mask:0xf\n"
      "v_min_i32 v48, 0x400, v48\n"
      "s_nop 0\n"
      "v_readlane_b32 s81, v48, 0\n"
      "v_readlane_b32 s82, v48, 32\n"
      "s_waitcnt lgkmcnt(14)\n"
      "v_mfma_f32_32x32x16_bf16 v[32:47], v[84:87], v[212:215], v[32:47]\n"
      "ds_read_b64_tr_b16 v[84:85], v21 offset:28672\n"
      "ds_read_b64_tr_b16 v[86:87], v21 offset:28928\n"
      "v_readlane_b32 s83, v48, 16\n"
      "v_readlane_b32 s78, v48, 48\n"
      "s_max_i32 s81, s81, s82\n"
      "s_nop 0\n"
      "s_max_i32 s83, s83, s78\n"
      "s_max_i32 s83, s83, s81\n"
      "s_add_u32 s91, s90, s83\n"
      "s_waitcnt lgkmcnt(14)\n"
      "v_mfma_f32_32x32x16_bf16 v[32:47], v[88:91], v[216:219], v[32:47]\n"
      "ds_read_b64_tr_b16 v[88:89], v22 offset:28672\n"
      "ds_read_b64_tr_b16 v[90:91], v22 offset:28928\n"
      "s_add_u32 s90, s90, s81\n"
      "v_mov_b32 v4, s65\n"
      "v_add_u32 v5, s88, v0\n"
      "v_min_u32 v5, 64, v5\n"
      "v_lshlrev_b32 v5, 24, v5\n"
      "s_waitcnt lgkmcnt(14)\n"
      "v_mfma_f32_32x32x16_bf16 v[32:47], v[92:95], v[220:223], v[32:47]\n"
      "ds_read_b64_tr_b16 v[92:93], v23 offset:28672\n"
      "ds_read_b64_tr_b16 v[94:95], v23 offset:28928\n"
      "buffer_store_dword v4, v5, s[12:15], 0 offen sc1\n"
      "s_waitcnt lgkmcnt(14)\n"
      "v_mfma_f32_16x16x32_bf16 v[56:59], v[12:15], v[64:67], v[56:59]\n"
      "s_waitcnt lgkmcnt(6)\n"
      "v_mfma_f32_16x16x32_bf16 v[56:59], v[12:15], v[80:83], v[56:59]\n"
      "v_mfma_f32_32x32x16_bf16 v[32:47], v[64:67], v[224:227], v[32:47]\n"
      "s_nop 6\n"
      "v_cvt_pk_bf16_f32 v57, v56, v56\n"
      "v_lshlrev_b32 v57, 16, v57\n"
      "v_sub_f32 v57, v56, v57\n"
      "v_cvt_pk_bf16_f32 v57, v56, v57\n"
      "ds_write_b32 v29, v57 offset:4096\n"
      "s_waitcnt lgkmcnt(0)\n"
      "v_mfma_f32_32x32x16_bf16 v[32:47], v[68:71], v[228:231], v[32:47]\n"
      "s_waitcnt lgkmcnt(0)\n"
      "s_barrier\n"
      "v_mfma_f32_32x32x16_bf16 v[32:47], v[72:75], v[232:235], v[32:47]\n"
      "ds_read_b128 v[56:59], v28 offset:4096\n"
      "LBg3:\n"
      "s_memrealtime s[84:85]\n"
      "s_waitcnt lgkmcnt(0)\n"
      "s_sub_u32 s78, s84, s91\n"
      "s_cmp_lt_i32 s78, 270\n"
      "s_cbranch_scc1 LBg3\n"
      "v_lshrrev_b32 v220, 5, v0\n"
      "v_and_b32 v222, 31, v0\n"
      "v_bfe_u32 v223, v0, 4, 1\n"
      "v_bfe_u32 v4, v0, 2, 2\n"
      "v_mfma_f32_32x32x16_bf16 v[32:47], v[76:79], v[236:239], v[32:47]\n"
      "s_add_u32 s3, s48, 2048\n"
      "buffer_load_dwordx4 v[128:131], v1, s[12:15], s3 offen sc1\n"
      "v_lshl_add_u32 v4, v220, 2, v4\n"
      "v_lshlrev_b32 v4, 7, v4\n"
      "v_lshl_add_u32 v4, v223, 5, v4\n"
      "v_and_b32 v5, 11, v0\n"
      "v_mfma_f32_32x32x16_bf16 v[32:47], v[80:83], v[240:243], v[32:47]\n"
      "s_add_u32 s3, s48, 6144\n"
      "buffer_load_dwordx4 v[132:135], v1, s[12:15], s3 offen sc1\n"
      "v_lshl_add_u32 v4, v5, 3, v4\n"
      "s_mul_i32 s81, s30, 4160\n"
      "s_lshr_b32 s82, s29, 1\n"
      "s_mul_i32 s82, s82, 2080\n"
      "s_or_b32 s82, s82, s81\n"
      "s_or_b32 s82, s82, 98304\n"
      "v_xor_b32 v20, s82, v4\n"
      "v_mfma_f32_32x32x16_bf16 v[32:47], v[84:87], v[244:247], v[32:47]\n"
      "s_add_u32 s3, s48, 10240\n"
      "buffer_load_dwordx4 v[136:139], v1, s[12:15], s3 offen sc1\n"
      "v_xor_b32 v21, 64, v20\n"
      "s_mul_i32 s81, s30, 2112\n"
      "s_or_b32 s81, s81, 65536\n"
      "v_xor_b32 v22, s81, v4\n"
      "v_xor_b32 v23, 64, v22\n"
      "v_mfma_f32_32x32x16_bf16 v[32:47], v[88:91], v[248:251], v[32:47]\n"
      "s_add_u32 s3, s48, 14336\n"
      "buffer_load_dwordx4 v[140:143], v1, s[12:15], s3 offen sc1\n"
      "v_xor_b32 v28, 65536, v22\n"
      "v_xor_b32 v29, 65536, v23\n"
      "v_mov_b32 v7, 0xfffefffe\n"
      "s_lshl_b32 s81, s28, 12\n"
      "v_mfma_f32_32x32x16_bf16 v[32:47], v[92:95], v[252:255], v[32:47]\n"
      "v_mfma_f32_32x32x16_bf16 v[32:47], v[56:59], v[8:11], v[32:47]\n"
      "ds_read_b64_tr_b16 v[64:65], v22 offset:32768\n"
      "ds_read_b64_tr_b16 v[66:67], v22 offset:33792\n"
      "ds_read_b64_tr_b16 v[68:69], v23 offset:32768\n"
      "ds_read_b64_tr_b16 v[70:71], v23 offset:33792\n"
      "ds_read_b64_tr_b16 v[72:73], v22 offset:36864\n"
      "ds_read_b64_tr_b16 v[74:75], v22 offset:37888\n"
      "ds_read_b64_tr_b16 v[76:77], v23 offset:36864\n"
      "ds_read_b64_tr_b16 v[78:79], v23 offset:37888\n"
      "s_nop 3\n"
      "v_mul_f32 v32, v32, v3\n"
      "v_mul_f32 v33, v33, v3\n"
      "v_cvt_pk_bf16_f32 v32, v32, v33\n"
      "v_pk_max_i16 v32, v32, 0\n"
      "v_or_b32 v32, s57, v32\n"
      "v_mul_f32 v34, v34, v3\n"
      "v_mul_f32 v35, v35, v3\n"
      "v_cvt_pk_bf16_f32 v33, v34, v35\n"
      "v_pk_max_i16 v33, v33, 0\n"
      "v_or_b32 v33, s58, v33\n"
      "v_mul_f32 v36, v36, v3\n"
      "v_mul_f32 v37, v37, v3\n"
      "v_cvt_pk_bf16_f32 v34, v36, v37\n"
      "v_pk_max_i16 v34, v34, 0\n"
      "v_or_b32 v34, s59, v34\n"
      "v_mul_f32 v38, v38, v3\n"
      "v_mul_f32 v39, v39, v3\n"
      "v_cvt_pk_bf16_f32 v35, v38, v39\n"
      "v_pk_max_i16 v35, v35, 0\n"
      "v_or_b32 v35, s60, v35\n"
      "buffer_store_dwordx4 v[32:35], v1, s[12:15], s69 offen offset:0 sc1\n"
      "v_mul_f32 v40, v40, v3\n"
      "v_mul_f32 v41, v41, v3\n"
      "v_cvt_pk_bf16_f32 v36, v40, v41\n"
      "v_pk_max_i16 v36, v36, 0\n"
      "v_or_b32 v36, s57, v36\n"
      "v_mul_f32 v42, v42, v3\n"
      "v_mul_f32 v43, v43, v3\n"
      "v_cvt_pk_bf16_f32 v37, v42, v43\n"
      "v_pk_max_i16 v37, v37, 0\n"
      "v_or_b32 v37, s58, v37\n"
      "v_mul_f32 v44, v44, v3\n"
      "v_mul_f32 v45, v45, v3\n"
      "v_cvt_pk_bf16_f32 v38, v44, v45\n"
      "v_pk_max_i16 v38, v38, 0\n"
      "v_or_b32 v38, s59, v38\n"
      "v_mul_f32 v46, v46, v3\n"
      "v_mul_f32 v47, v47, v3\n"
      "v_cvt_pk_bf16_f32 v39, v46, v47\n"
      "v_pk_max_i16 v39, v39, 0\n"
      "v_or_b32 v39, s60, v39\n"
      "buffer_store_dwordx4 v[36:39], v1, s[12:15], s69 offen offset:1024 sc1\n"
      "s_add_u32 s3, s48, 18432\n"
      "buffer_load_dwordx4 v[144:147], v1, s[12:15], s3 offen sc1\n"
      "s_add_u32 s3, s48, 22528\n"
      "buffer_load_dwordx4 v[148:151], v1, s[12:15], s3 offen sc1\n"
      "s_add_u32 s3, s48, 26624\n"
      "buffer_load_dwordx4 v[152:155], v1, s[12:15], s3 offen sc1\n"
      "s_add_u32 s3, s48, 30720\n"
      "buffer_load_dwordx4 v[156:159], v1, s[12:15], s3 offen sc1\n"
      "s_waitcnt vmcnt(9)\n"
      "v_xor_b32 v128, s57, v128\n"
      "v_xor_b32 v129, s58, v129\n"
      "v_xor_b32 v130, s59, v130\n"
      "v_xor_b32 v131, s60, v131\n"
      "s_waitcnt vmcnt(8)\n"
      "v_xor_b32 v132, s57, v132\n"
      "v_xor_b32 v133, s58, v133\n"
      "v_xor_b32 v134, s59, v134\n"
      "v_xor_b32 v135, s60, v135\n"
      "v_or3_b32 v4, v128, v129, v130\n"
      "v_or3_b32 v4, v4, v131, v132\n"
      "v_or3_b32 v4, v4, v133, v134\n"
      "v_or_b32 v4, v4, v135\n"
      "v_and_b32 v4, 0x80008000, v4\n"
      "v_cmp_ne_u32 vcc, 0, v4\n"
      "s_nop 1\n"
      "s_cbranch_vccnz LBr4\n"
      "LBo4:\n"
      "s_waitcnt lgkmcnt(6)\n"
      "v_mfma_f32_32x32x16_bf16 v[112:127], v[64:67], v[128:131], 0\n"
      "ds_read_b64_tr_b16 v[80:81], v22 offset:40960\n"
      "ds_read_b64_tr_b16 v[82:83], v22 offset:41984\n"
      "s_add_u32 s3, s48, 34816\n"
      "buffer_load_dwordx4 v[160:163], v1, s[12:15], s3 offen sc1\n"
      "s_add_u32 s81, s81, 65536\n"
      "v_add_u32 v30, s81, v1\n"
      "v_xor_b32 v31, 16384, v30\n"
      "v_lshlrev_b32 v5, 4, v220\n"
      "v_lshl_add_u32 v13, v222, 10, v5\n"
      "s_add_u32 s81, s35, 0\n"
      "s_waitcnt vmcnt(8)\n"
      "v_xor_b32 v136, s57, v136\n"
      "s_waitcnt lgkmcnt(6)\n"
      "v_mfma_f32_32x32x16_bf16 v[96:111], v[68:71], v[128:131], 0\n"
      "ds_read_b64_tr_b16 v[84:85], v23 offset:40960\n"
      "ds_read_b64_tr_b16 v[86:87], v23 offset:41984\n"
      "s_add_u32 s3, s48, 38912\n"
      "buffer_load_dwordx4 v[164:167], v1, s[12:15], s3 offen sc1\n"
      "v_xor_b32 v137, s58, v137\n"
      "v_xor_b32 v138, s59, v138\n"
      "v_xor_b32 v139, s60, v139\n"
      "s_waitcnt vmcnt(8)\n"
      "v_xor_b32 v140, s57, v140\n"
      "v_xor_b32 v141, s58, v141\n"
      "v_xor_b32 v142, s59, v142\n"
      "s_waitcnt lgkmcnt(6)\n"
      "v_mfma_f32_32x32x16_bf16 v[112:127], v[72:75], v[132:135], v[112:127]\n"
      "ds_read_b64_tr_b16 v[88:89], v22 offset:45056\n"
      "ds_read_b64_tr_b16 v[90:91], v22 offset:46080\n"
      "s_add_u32 m0, s34, 8192\n"
      "s_nop 0\n"
      "buffer_load_dwordx4 v6, s[16:19], s49 offen lds\n"
      "v_xor_b32 v143, s60, v143\n"
      "v_or3_b32 v4, v136, v137, v138\n"
      "v_or3_b32 v4, v4, v139, v140\n"
      "v_or3_b32 v4, v4, v141, v142\n"
      "v_or_b32 v4, v4, v143\n"
      "v_and_b32 v4, 0x80008000, v4\n"
      "s_waitcnt lgkmcnt(6)\n"
      "v_mfma_f32_32x32x16_bf16 v[96:111], v[76:79], v[132:135], v[96:111]\n"
      "ds_read_b64_tr_b16 v[92:93], v23 offset:45056\n"
      "ds_read_b64_tr_b16 v[94:95], v23 offset:46080\n"
      "v_cmp_ne_u32 vcc, 0, v4\n"
      "s_nop 1\n"
      "s_cbranch_vccnz LBr5\n"
      "LBo5:\n"
      "s_waitcnt lgkmcnt(6)\n"
      "v_mfma_f32_32x32x16_bf16 v[112:127], v[80:83], v[136:139], v[112:127]\n"
      "ds_read_b64_tr_b16 v[64:65], v22 offset:49152\n"
      "ds_read_b64_tr_b16 v[66:67], v22 offset:50176\n"
      "s_add_u32 s3, s48, 43008\n"
      "buffer_load_dwordx4 v[168:171], v1, s[12:15], s3 offen sc1\n"
      "v_add_u32 v8, s81, v1\n"
      "v_xor_b32 v9, 4096, v8\n"
      "v_xor_b32 v10, 12288, v8\n"
      "s_lshl3_add_u32 s81, s30, s29\n"
      "s_lshl_b32 s81, s81, 11\n"
      "s_add_u32 s81, s81, 65536\n"
      "s_waitcnt vmcnt(7)\n"
      "v_xor_b32 v144, s57, v144\n"
      "v_xor_b32 v145, s58, v145\n"
      "s_waitcnt lgkmcnt(6)\n"
      "v_mfma_f32_32x32x16_bf16 v[96:111], v[84:87], v[136:139], v[96:111]\n"
      "ds_read_b64_tr_b16 v[68:69], v23 offset:49152\n"
      "ds_read_b64_tr_b16 v[70:71], v23 offset:50176\n"
      "s_add_u32 s3, s48, 47104\n"
      "buffer_load_dwordx4 v[172:175], v1, s[12:15], s3 offen sc1\n"
      "v_xor_b32 v146, s59, v146\n"
      "v_xor_b32 v147, s60, v147\n"
      "s_waitcnt vmcnt(7)\n"
      "v_xor_b32 v148, s57, v148\n"
      "v_xor_b32 v149, s58, v149\n"
      "v_xor_b32 v150, s59, v150\n"
      "v_xor_b32 v151, s60, v151\n"
      "s_waitcnt lgkmcnt(6)\n"
      "v_mfma_f32_32x32x16_bf16 v[112:127], v[88:91], v[140:143], v[112:127]\n"
      "ds_read_b64_tr_b16 v[72:73], v22 offset:53248\n"
      "ds_read_b64_tr_b16 v[74:75], v22 offset:54272\n"
      "s_add_u32 m0, s34, 16384\n"
      "s_add_u32 s3, s45, 524288\n"
      "buffer_load_dwordx4 v6, s[16:19], s3 offen lds\n"
      "v_or3_b32 v4, v144, v145, v146\n"
      "v_or3_b32 v4, v4, v147, v148\n"
      "v_or3_b32 v4, v4, v149, v150\n"
      "v_or_b32 v4, v4, v151\n"
      "v_and_b32 v4, 0x80008000, v4\n"
      "v_cmp_ne_u32 vcc, 0, v4\n"
      "s_waitcnt lgkmcnt(6)\n"
      "v_mfma_f32_32x32x16_bf16 v[96:111], v[92:95], v[140:143], v[96:111]\n"
      "ds_read_b64_tr_b16 v[76:77], v23 offset:53248\n"
      "ds_read_b64_tr_b16 v[78:79], v23 offset:54272\n"
      "s_cbranch_vccnz LBr6\n"
      "LBo6:\n"
      "s_waitcnt lgkmcnt(6)\n"
      "v_mfma_f32_32x32x16_bf16 v[112:127], v[64:67], v[144:147], v[112:127]\n"
      "ds_read_b64_tr_b16 v[80:81], v22 offset:57344\n"
      "ds_read_b64_tr_b16 v[82:83], v22 offset:58368\n"
      "s_add_u32 s3, s48, 51200\n"
      "buffer_load_dwordx4 v[176:179], v1, s[12:15], s3 offen sc1\n"
      "v_add_u32 v11, s81, v1\n"
      "v_xor_b32 v12, 16384, v11\n"
      "v_permlane32_swap_b32 v24, v26\n"
      "v_permlane32_swap_b32 v25, v27\n"
      "v_lshlrev_b32 v48, 16, v24\n"
      "v_and_b32 v49, 0xffff0000, v24\n"
      "s_waitcnt lgkmcnt(6)\n"
      "v_mfma_f32_32x32x16_bf16 v[96:111], v[68:71], v[144:147], v[96:111]\n"
      "ds_read_b64_tr_b16 v[84:85], v23 offset:57344\n"
      "ds_read_b64_tr_b16 v[86:87], v23 offset:58368\n"
      "s_add_u32 s3, s48, 55296\n"
      "buffer_load_dwordx4 v[180:183], v1, s[12:15], s3 offen sc1\n"
      "s_waitcnt vmcnt(9)\n"
      "v_xor_b32 v152, s57, v152\n"
      "v_xor_b32 v153, s58, v153\n"
      "v_xor_b32 v154, s59, v154\n"
      "v_xor_b32 v155, s60, v155\n"
      "s_waitcnt vmcnt(8)\n"
      "v_xor_b32 v156, s57, v156\n"
      "v_xor_b32 v157, s58, v157\n"
      "s_waitcnt lgkmcnt(6)\n"
      "v_mfma_f32_32x32x16_bf16 v[112:127], v[72:75], v[148:151], v[112:127]\n"
      "ds_read_b64_tr_b16 v[88:89], v22 offset:61440\n"
      "ds_read_b64_tr_b16 v[90:91], v22 offset:62464\n"
      "s_add_u32 m0, s34, 24576\n"
      "s_add_u32 s3, s49, 524288\n"
      "buffer_load_dwordx4 v6, s[16:19], s3 offen lds\n"
      "v_xor_b32 v158, s59, v158\n"
      "v_xor_b32 v159, s60, v159\n"
      "v_or3_b32 v4, v152, v153, v154\n"
      "v_or3_b32 v4, v4, v155, v156\n"
      "v_or3_b32 v4, v4, v157, v158\n"
      "v_or_b32 v4, v4, v159\n"
      "s_waitcnt lgkmcnt(6)\n"
      "v_mfma_f32_32x32x16_bf16 v[96:111], v[76:79], v[148:151], v[96:111]\n"
      "ds_read_b64_tr_b16 v[92:93], v23 offset:61440\n"
      "ds_read_b64_tr_b16 v[94:95], v23 offset:62464\n"
      "v_and_b32 v4, 0x80008000, v4\n"
      "v_cmp_ne_u32 vcc, 0, v4\n"
      "s_nop 1\n"
      "s_cbranch_vccnz LBr7\n"
      "LBo7:\n"
      "s_waitcnt lgkmcnt(0)\n"
      "s_barrier\n"
      "v_mfma_f32_32x32x16_bf16 v[112:127], v[80:83], v[152:155], v[112:127]\n"
      "ds_read_b64_tr_b16 v[64:65], v22 offset:0\n"
      "ds_read_b64_tr_b16 v[66:67], v22 offset:1024\n"
      "s_add_u32 s3, s48, 59392\n"
      "buffer_load_dwordx4 v[184:187], v1, s[12:15], s3 offen sc1\n"
      "v_lshlrev_b32 v50, 16, v25\n"
      "v_and_b32 v51, 0xffff0000, v25\n"
      "v_lshlrev_b32 v52, 16, v26\n"
      "v_and_b32 v53, 0xffff0000, v26\n"
      "v_lshlrev_b32 v54, 16, v27\n"
      "v_and_b32 v55, 0xffff0000, v27\n"
      "v_mfma_f32_32x32x16_bf16 v[96:111], v[84:87], v[152:155], v[96:111]\n"
      "ds_read_b64_tr_b16 v[68:69], v23 offset:0\n"
      "ds_read_b64_tr_b16 v[70:71], v23 offset:1024\n"
      "s_add_u32 s3, s48, 63488\n"
      "buffer_load_dwordx4 v[188:191], v1, s[12:15], s3 offen sc1\n"
      "s_waitcnt vmcnt(10)\n"
      "v_xor_b32 v160, s57, v160\n"
      "v_xor_b32 v161, s58, v161\n"
      "v_xor_b32 v162, s59, v162\n"
      "v_xor_b32 v163, s60, v163\n"
      "s_waitcnt vmcnt(9)\n"
      "v_xor_b32 v164, s57, v164\n"
      "v_xor_b32 v165, s58, v165\n"
      "v_mfma_f32_32x32x16_bf16 v[112:127], v[88:91], v[156:159], v[112:127]\n"
      "ds_read_b64_tr_b16 v[72:73], v22 offset:4096\n"
      "ds_read_b64_tr_b16 v[74:75], v22 offset:5120\n"
      "s_add_u32 m0, s34, 32768\n"
      "s_add_u32 s3, s45, 1048576\n"
      "buffer_load_dwordx4 v6, s[16:19], s3 offen lds\n"
      "v_xor_b32 v166, s59, v166\n"
      "v_xor_b32 v167, s60, v167\n"
      "v_or3_b32 v4, v160, v161, v162\n"
      "v_or3_b32 v4, v4, v163, v164\n"
      "v_or3_b32 v4, v4, v165, v166\n"
      "v_or_b32 v4, v4, v167\n"
      "v_mfma_f32_32x32x16_bf16 v[96:111], v[92:95], v[156:159], v[96:111]\n"
      "ds_read_b64_tr_b16 v[76:77], v23 offset:4096\n"
      "ds_read_b64_tr_b16 v[78:79], v23 offset:5120\n"
      "s_add_u32 m0, s34, 40960\n"
      "s_add_u32 s3, s49, 1048576\n"
      "buffer_load_dwordx4 v6, s[16:19], s3 offen lds\n"
      "v_and_b32 v4, 0x80008000, v4\n"
      "v_cmp_ne_u32 vcc, 0, v4\n"
      "s_nop 1\n"
      "s_cbranch_vccnz LBr8\n"
      "LBo8:\n"
      "s_waitcnt lgkmcnt(6)\n"
      "v_mfma_f32_32x32x16_bf16 v[112:127], v[64:67], v[160:163], v[112:127]\n"
      "ds_read_b64_tr_b16 v[80:81], v22 offset:8192\n"
      "ds_read_b64_tr_b16 v[82:83], v22 offset:9216\n"
      "LBg9:\n"
      "s_memrealtime s[84:85]\n"
      "s_waitcnt lgkmcnt(0)\n"
      "s_sub_u32 s78, s84, s91\n"
      "s_cmp_lt_i32 s78, 400\n"
      "s_cbranch_scc1 LBg9\n"
      "v_mfma_f32_32x32x16_bf16 v[96:111], v[68:71], v[160:163], v[96:111]\n"
      "ds_read_b64_tr_b16 v[84:85], v23 offset:8192\n"
      "ds_read_b64_tr_b16 v[86:87], v23 offset:9216\n"
      "s_add_u32 s3, s48, 69632\n"
      "buffer_load_dwordx4 v[192:195], v1, s[12:15], s3 offen sc1\n"
      "s_waitcnt vmcnt(10)\n"
      "v_xor_b32 v168, s57, v168\n"
      "v_xor_b32 v169, s58, v169\n"
      "v_xor_b32 v170, s59, v170\n"
      "v_xor_b32 v171, s60, v171\n"
      "s_waitcnt vmcnt(9)\n"
      "v_xor_b32 v172, s57, v172\n"
      "v_xor_b32 v173, s58, v173\n"
      "v_mfma_f32_32x32x16_bf16 v[112:127], v[72:75], v[164:167], v[112:127]\n"
      "ds_read_b64_tr_b16 v[88:89], v22 offset:12288\n"
      "ds_read_b64_tr_b16 v[90:91], v22 offset:13312\n"
      "s_add_u32 s3, s48, 71680\n"
      "buffer_load_dwordx4 v[196:199], v1, s[12:15], s3 offen sc1\n"
      "v_xor_b32 v174, s59, v174\n"
      "v_xor_b32 v175, s60, v175\n"
      "v_or3_b32 v4, v168, v169, v170\n"
      "v_or3_b32 v4, v4, v171, v172\n"
      "v_or3_b32 v4, v4, v173, v174\n"
      "v_or_b32 v4, v4, v175\n"
      "v_mfma_f32_32x32x16_bf16 v[96:111], v[76:79], v[164:167], v[96:111]\n"
      "ds_read_b64_tr_b16 v[92:93], v23 offset:12288\n"
      "ds_read_b64_tr_b16 v[94:95], v23 offset:13312\n"
      "v_and_b32 v4, 0x80008000, v4\n"
      "v_cmp_ne_u32 vcc, 0, v4\n"
      "s_nop 1\n"
      "s_cbranch_vccnz LBr10\n"
      "LBo10:\n"
      "v_mfma_f32_32x32x16_bf16 v[112:127], v[80:83], v[168:171], v[112:127]\n"
      "ds_read_b64_tr_b16 v[64:65], v22 offset:16384\n"
      "ds_read_b64_tr_b16 v[66:67], v22 offset:17408\n"
      "s_add_u32 s3, s48, 73728\n"
      "buffer_load_dwordx4 v[200:203], v1, s[12:15], s3 offen sc1\n"
      "s_waitcnt lgkmcnt(6)\n"
      "v_mfma_f32_32x32x16_bf16 v[96:111], v[84:87], v[168:171], v[96:111]\n"
      "ds_read_b64_tr_b16 v[68:69], v23 offset:16384\n"
      "ds_read_b64_tr_b16 v[70:71], v23 offset:17408\n"
      "s_add_u32 s3, s48, 75776\n"
      "buffer_load_dwordx4 v[204:207], v1, s[12:15], s3 offen sc1\n"
      "s_waitcnt vmcnt(10)\n"
      "v_xor_b32 v176, s57, v176\n"
      "v_xor_b32 v177, s58, v177\n"
      "v_xor_b32 v178, s59, v178\n"
      "v_xor_b32 v179, s60, v179\n"
      "s_waitcnt vmcnt(9)\n"
      "v_xor_b32 v180, s57, v180\n"
      "v_xor_b32 v181, s58, v181\n"
      "s_waitcnt lgkmcnt(6)\n"
      "v_mfma_f32_32x32x16_bf16 v[112:127], v[88:91], v[172:175], v[112:127]\n"
      "ds_read_b64_tr_b16 v[72:73], v22 offset:20480\n"
      "ds_read_b64_tr_b16 v[74:75], v22 offset:21504\n"
      "v_xor_b32 v182, s59, v182\n"
      "v_xor_b32 v183, s60, v183\n"
      "v_or3_b32 v4, v176, v177, v178\n"
      "v_or3_b32 v4, v4, v179, v180\n"
      "v_or3_b32 v4, v4, v181, v182\n"
      "v_or_b32 v4, v4, v183\n"
      "s_waitcnt lgkmcnt(6)\n"
      "v_mfma_f32_32x32x16_bf16 v[96:111], v[92:95], v[172:175], v[96:111]\n"
      "ds_read_b64_tr_b16 v[76:77], v23 offset:20480\n"
      "ds_read_b64_tr_b16 v[78:79], v23 offset:21504\n"
      "v_and_b32 v4, 0x80008000, v4\n"
      "v_cmp_ne_u32 vcc, 0, v4\n"
      "s_nop 1\n"
      "s_cbranch_vccnz LBr11\n"
      "LBo11:\n"
      "s_waitcnt lgkmcnt(6)\n"
      "v_mfma_f32_32x32x16_bf16 v[112:127], v[64:67], v[176:179], v[112:127]\n"
      "ds_read_b64_tr_b16 v[80:81], v22 offset:24576\n"
      "ds_read_b64_tr_b16 v[82:83], v22 offset:25600\n"
      "s_add_u32 s3, s48, 77824\n"
      "buffer_load_dwordx4 v[208:211], v1, s[12:15], s3 offen sc1\n"
      "s_waitcnt lgkmcnt(6)\n"
      "v_mfma_f32_32x32x16_bf16 v[96:111], v[68:71], v[176:179], v[96:111]\n"
      "ds_read_b64_tr_b16 v[84:85], v23 offset:24576\n"
      "ds_read_b64_tr_b16 v[86:87], v23 offset:25600\n"
      "s_add_u32 s3, s48, 79872\n"
      "buffer_load_dwordx4 v[212:215], v1, s[12:15], s3 offen sc1\n"
      "s_waitcnt vmcnt(9)\n"
      "v_xor_b32 v184, s57, v184\n"
      "v_xor_b32 v185, s58, v185\n"
      "v_xor_b32 v186, s59, v186\n"
      "v_xor_b32 v187, s60, v187\n"
      "s_waitcnt vmcnt(8)\n"
      "v_xor_b32 v188, s57, v188\n"
      "v_xor_b32 v189, s58, v189\n"
      "s_waitcnt lgkmcnt(6)\n"
      "v_mfma_f32_32x32x16_bf16 v[112:127], v[72:75], v[180:183], v[112:127]\n"
      "ds_read_b64_tr_b16 v[88:89], v22 offset:28672\n"
      "ds_read_b64_tr_b16 v[90:91], v22 offset:29696\n"
      "v_xor_b32 v190, s59, v190\n"
      "v_xor_b32 v191, s60, v191\n"
      "v_or3_b32 v4, v184, v185, v186\n"
      "v_or3_b32 v4, v4, v187, v188\n"
      "v_or3_b32 v4, v4, v189, v190\n"
      "v_or_b32 v4, v4, v191\n"
      "s_waitcnt lgkmcnt(6)\n"
      "v_mfma_f32_32x32x16_bf16 v[96:111], v[76:79], v[180:183], v[96:111]\n"
      "ds_read_b64_tr_b16 v[92:93], v23 offset:28672\n"
      "ds_read_b64_tr_b16 v[94:95], v23 offset:29696\n"
      "v_and_b32 v4, 0x80008000, v4\n"
      "v_cmp_ne_u32 vcc, 0, v4\n"
      "s_nop 1\n"
      "s_cbranch_vccnz LBr12\n"
      "LBo12:\n"
      "s_waitcnt vmcnt(6) lgkmcnt(0)\n"
      "s_barrier\n"
      "v_mfma_f32_32x32x16_bf16 v[112:127], v[80:83], v[184:187], v[112:127]\n"
      "ds_read_b64_tr_b16 v[64:65], v28 offset:8192\n"
      "ds_read_b64_tr_b16 v[66:67], v28 offset:9216\n"
      "s_add_u32 s3, s48, 81920\n"
      "buffer_load_dwordx4 v[216:219], v1, s[12:15], s3 offen sc1\n"
      "v_mfma_f32_32x32x16_bf16 v[96:111], v[84:87], v[184:187], v[96:111]\n"
      "ds_read_b64_tr_b16 v[68:69], v29 offset:8192\n"
      "ds_read_b64_tr_b16 v[70:71], v29 offset:9216\n"
      "s_add_u32 s3, s48, 83968\n"
      "buffer_load_dwordx4 v[220:223], v1, s[12:15], s3 offen sc1\n"
      "s_waitcnt vmcnt(7)\n"
      "v_xor_b32 v192, s57, v192\n"
      "v_xor_b32 v193, s58, v193\n"
      "v_xor_b32 v194, s59, v194\n"
      "v_xor_b32 v195, s60, v195\n"
      "s_waitcnt vmcnt(6)\n"
      "v_xor_b32 v196, s57, v196\n"
      "v_xor_b32 v197, s58, v197\n"
      "v_mfma_f32_32x32x16_bf16 v[112:127], v[88:91], v[188:191], v[112:127]\n"
      "ds_read_b64_tr_b16 v[72:73], v28 offset:12288\n"
      "ds_read_b64_tr_b16 v[74:75], v28 offset:13312\n"
      "v_xor_b32 v198, s59, v198\n"
      "v_xor_b32 v199, s60, v199\n"
      "v_or3_b32 v4, v192, v193, v194\n"
      "v_or3_b32 v4, v4, v195, v196\n"
      "v_or3_b32 v4, v4, v197, v198\n"
      "v_or_b32 v4, v4, v199\n"
      "v_mfma_f32_32x32x16_bf16 v[96:111], v[92:95], v[188:191], v[96:111]\n"
      "ds_read_b64_tr_b16 v[76:77], v29 offset:12288\n"
      "ds_read_b64_tr_b16 v[78:79], v29 offset:13312\n"
      "v_and_b32 v4, 0x80008000, v4\n"
      "v_cmp_ne_u32 vcc, 0, v4\n"
      "s_nop 1\n"
      "s_cbranch_vccnz LBr13\n"
      "LBo13:\n"
      "s_waitcnt lgkmcnt(6)\n"
      "v_mfma_f32_32x32x16_bf16 v[112:127], v[64:67], v[192:195], v[112:127]\n"
      "ds_read_b64_tr_b16 v[80:81], v28 offset:16384\n"
      "ds_read_b64_tr_b16 v[82:83], v28 offset:17408\n"
      "s_add_u32 s3, s48, 86016\n"
      "buffer_load_dwordx4 v[224:227], v1, s[12:15], s3 offen sc1\n"
      "s_waitcnt lgkmcnt(6)\n"
      "v_mfma_f32_32x32x16_bf16 v[96:111], v[68:71], v[192:195], v[96:111]\n"
      "ds_read_b64_tr_b16 v[84:85], v29 offset:16384\n"
      "ds_read_b64_tr_b16 v[86:87], v29 offset:17408\n"
      "s_add_u32 s3, s48, 86016\n"
      "buffer_load_dwordx4 v[228:231], v1, s[12:15], s3 offen sc1\n"
      "s_waitcnt vmcnt(7)\n"
      "v_xor_b32 v200, s57, v200\n"
      "v_xor_b32 v201, s58, v201\n"
      "v_xor_b32 v202, s59, v202\n"
      "v_xor_b32 v203, s60, v203\n"
      "s_waitcnt vmcnt(6)\n"
      "v_xor_b32 v204, s57, v204\n"
      "v_xor_b32 v205, s58, v205\n"
      "s_waitcnt lgkmcnt(6)\n"
      "v_mfma_f32_32x32x16_bf16 v[112:127], v[72:75], v[196:199], v[112:127]\n"
      "ds_read_b64_tr_b16 v[88:89], v28 offset:20480\n"
      "ds_read_b64_tr_b16 v[90:91], v28 offset:21504\n"
      "v_xor_b32 v206, s59, v206\n"
      "v_xor_b32 v207, s60, v207\n"
      "v_or3_b32 v4, v200, v201, v202\n"
      "v_or3_b32 v4, v4, v203, v204\n"
      "v_or3_b32 v4, v4, v205, v206\n"
      "v_or_b32 v4, v4, v207\n"
      "s_waitcnt lgkmcnt(6)\n"
      "v_mfma_f32_32x32x16_bf16 v[96:111], v[76:79], v[196:199], v[96:111]\n"
      "ds_read_b64_tr_b16 v[92:93], v29 offset:20480\n"
      "ds_read_b64_tr_b16 v[94:95], v29 offset:21504\n"
      "v_and_b32 v4, 0x80008000, v4\n"
      "v_cmp_ne_u32 vcc, 0, v4\n"
      "s_nop 1\n"
      "s_cbranch_vccnz LBr14\n"
      "LBo14:\n"
      "s_waitcnt lgkmcnt(6)\n"
      "v_mfma_f32_32x32x16_bf16 v[112:127], v[80:83], v[200:203], v[112:127]\n"
      "ds_read_b64_tr_b16 v[64:65], v28 offset:24576\n"
      "ds_read_b64_tr_b16 v[66:67], v28 offset:25600\n"
      "s_add_u32 m0, s34, 131072\n"
      "s_nop 0\n"
      "buffer_load_dwordx4 v6, s[20:23], s46 offen lds\n"
      "s_waitcnt lgkmcnt(6)\n"
      "v_mfma_f32_32x32x16_bf16 v[96:111], v[84:87], v[200:203], v[96:111]\n"
      "ds_read_b64_tr_b16 v[68:69], v29 offset:24576\n"
      "ds_read_b64_tr_b16 v[70:71], v29 offset:25600\n"
      "s_add_u32 m0, s34, 139264\n"
      "s_add_u32 s3, s46, 65536\n"
      "buffer_load_dwordx4 v6, s[20:23], s3 offen lds\n"
      "s_waitcnt vmcnt(7)\n"
      "v_xor_b32 v208, s57, v208\n"
      "v_xor_b32 v209, s58, v209\n"
      "v_xor_b32 v210, s59, v210\n"
      "v_xor_b32 v211, s60, v211\n"
      "s_waitcnt vmcnt(6)\n"
      "v_xor_b32 v212, s57, v212\n"
      "v_xor_b32 v213, s58, v213\n"
      "s_waitcnt lgkmcnt(6)\n"
      "v_mfma_f32_32x32x16_bf16 v[112:127], v[88:91], v[204:207], v[112:127]\n"
      "ds_read_b64_tr_b16 v[72:73], v28 offset:28672\n"
      "ds_read_b64_tr_b16 v[74:75], v28 offset:29696\n"
      "s_add_u32 m0, s34, 147456\n"
      "s_add_u32 s3, s46, 131072\n"
      "buffer_load_dwordx4 v6, s[20:23], s3 offen lds\n"
      "v_xor_b32 v214, s59, v214\n"
      "v_xor_b32 v215, s60, v215\n"
      "v_or3_b32 v4, v208, v209, v210\n"
      "v_or3_b32 v4, v4, v211, v212\n"
      "v_or3_b32 v4, v4, v213, v214\n"
      "v_or_b32 v4, v4, v215\n"
      "s_waitcnt lgkmcnt(6)\n"
      "v_mfma_f32_32x32x16_bf16 v[96:111], v[92:95], v[204:207], v[96:111]\n"
      "ds_read_b64_tr_b16 v[76:77], v29 offset:28672\n"
      "ds_read_b64_tr_b16 v[78:79], v29 offset:29696\n"
      "v_and_b32 v4, 0x80008000, v4\n"
      "v_cmp_ne_u32 vcc, 0, v4\n"
      "s_nop 1\n"
      "s_cbranch_vccnz LBr15\n"
      "LBo15:\n"
      "s_waitcnt lgkmcnt(6)\n"
      "v_mfma_f32_32x32x16_bf16 v[112:127], v[64:67], v[208:211], v[112:127]\n"
      "ds_read_b64_tr_b16 v[80:81], v28 offset:32768\n"
      "ds_read_b64_tr_b16 v[82:83], v28 offset:33792\n"
      "s_add_u32 m0, s34, 155648\n"
      "s_add_u32 s3, s46, 196608\n"
      "buffer_load_dwordx4 v6, s[20:23], s3 offen lds\n"
      "s_waitcnt lgkmcnt(6)\n"
      "v_mfma_f32_32x32x16_bf16 v[96:111], v[68:71], v[208:211], v[96:111]\n"
      "ds_read_b64_tr_b16 v[84:85], v29 offset:32768\n"
      "ds_read_b64_tr_b16 v[86:87], v29 offset:33792\n"
      "s_add_u32 m0, s34, 98304\n"
      "s_add_u32 s3, s46, 262144\n"
      "buffer_load_dwordx4 v6, s[20:23], s3 offen lds\n"
      "s_waitcnt vmcnt(8)\n"
      "v_xor_b32 v216, s57, v216\n"
      "v_xor_b32 v217, s58, v217\n"
      "v_xor_b32 v218, s59, v218\n"
      "v_xor_b32 v219, s60, v219\n"
      "s_waitcnt vmcnt(7)\n"
      "v_xor_b32 v220, s57, v220\n"
      "v_xor_b32 v221, s58, v221\n"
      "s_waitcnt lgkmcnt(6)\n"
      "v_mfma_f32_32x32x16_bf16 v[112:127], v[72:75], v[212:215], v[112:127]\n"
      "ds_read_b64_tr_b16 v[88:89], v28 offset:36864\n"
      "ds_read_b64_tr_b16 v[90:91], v28 offset:37888\n"
      "s_add_u32 m0, s34, 106496\n"
      "s_add_u32 s3, s46, 327680\n"
      "buffer_load_dwordx4 v6, s[20:23], s3 offen lds\n"
      "v_xor_b32 v222, s59, v222\n"
      "v_xor_b32 v223, s60, v223\n"
      "v_or3_b32 v4, v216, v217, v218\n"
      "v_or3_b32 v4, v4, v219, v220\n"
      "v_or3_b32 v4, v4, v221, v222\n"
      "v_or_b32 v4, v4, v223\n"
      "s_waitcnt lgkmcnt(6)\n"
      "v_mfma_f32_32x32x16_bf16 v[96:111], v[76:79], v[212:215], v[96:111]\n"
      "ds_read_b64_tr_b16 v[92:93], v29 offset:36864\n"
      "ds_read_b64_tr_b16 v[94:95], v29 offset:37888\n"
      "v_and_b32 v4, 0x80008000, v4\n"
      "v_cmp_ne_u32 vcc, 0, v4\n"
      "s_nop 1\n"
      "s_cbranch_vccnz LBr16\n"
      "LBo16:\n"
      "s_waitcnt lgkmcnt(6)\n"
      "v_mfma_f32_32x32x16_bf16 v[112:127], v[80:83], v[216:219], v[112:127]\n"
      "ds_read_b64_tr_b16 v[64:65], v28 offset:40960\n"
      "ds_read_b64_tr_b16 v[66:67], v28 offset:41984\n"
      "s_add_u32 m0, s34, 114688\n"
      "s_add_u32 s3, s46, 393216\n"
      "buffer_load_dwordx4 v6, s[20:23], s3 offen lds\n"
      "s_waitcnt lgkmcnt(6)\n"
      "v_mfma_f32_32x32x16_bf16 v[96:111], v[84:87], v[216:219], v[96:111]\n"
      "ds_read_b64_tr_b16 v[68:69], v29 offset:40960\n"
      "ds_read_b64_tr_b16 v[70:71], v29 offset:41984\n"
      "s_add_u32 m0, s34, 122880\n"
      "s_add_u32 s3, s46, 458752\n"
      "buffer_load_dwordx4 v6, s[20:23], s3 offen lds\n"
      "s_waitcnt vmcnt(9)\n"
      "v_xor_b32 v224, s57, v224\n"
      "v_xor_b32 v225, s58, v225\n"
      "v_xor_b32 v226, s59, v226\n"
      "v_xor_b32 v227, s60, v227\n"
      "s_waitcnt vmcnt(8)\n"
      "v_xor_b32 v228, s57, v228\n"
      "v_xor_b32 v229, s58, v229\n"
      "s_waitcnt lgkmcnt(6)\n"
      "v_mfma_f32_32x32x16_bf16 v[112:127], v[88:91], v[220:223], v[112:127]\n"
      "ds_read_b64_tr_b16 v[72:73], v28 offset:45056\n"
      "ds_read_b64_tr_b16 v[74:75], v28 offset:46080\n"
      "v_xor_b32 v230, s59, v230\n"
      "v_xor_b32 v231, s60, v231\n"
      "v_or3_b32 v4, v224, v225, v226\n"
      "v_or3_b32 v4, v4, v227, v228\n"
      "v_or3_b32 v4, v4, v229, v230\n"
      "v_or_b32 v4, v4, v231\n"
      "s_waitcnt lgkmcnt(6)\n"
      "v_mfma_f32_32x32x16_bf16 v[96:111], v[92:95], v[220:223], v[96:111]\n"
      "ds_read_b64_tr_b16 v[76:77], v29 offset:45056\n"
      "ds_read_b64_tr_b16 v[78:79], v29 offset:46080\n"
      "v_and_b32 v4, 0x80008000, v4\n"
      "v_cmp_ne_u32 vcc, 0, v4\n"
      "s_nop 1\n"
      "s_cbranch_vccnz LBr17\n"
      "LBo17:\n"
      "s_waitcnt lgkmcnt(4)\n"
      "v_mfma_f32_32x32x16_bf16 v[96:111], v[68:71], v[224:227], v[96:111]\n"
      "s_waitcnt lgkmcnt(0)\n"
      "v_mfma_f32_32x32x16_bf16 v[96:111], v[76:79], v[228:231], v[96:111]\n"
      "buffer_load_dwordx4 v[232:235], v1, s[12:15], s50 offen sc1\n"
      "s_add_u32 s3, s50, 4096\n"
      "buffer_load_dwordx4 v[236:239], v1, s[12:15], s3 offen sc1\n"
      "v_mfma_f32_32x32x16_bf16 v[112:127], v[64:67], v[224:227], v[112:127]\n"
      "v_mfma_f32_32x32x16_bf16 v[112:127], v[72:75], v[228:231], v[112:127]\n"
      "s_cmp_lt_u32 s29, 2\n"
      "s_cbranch_scc1 LBf0\n"
      "s_nop 4\n"
      "ds_write_b128 v30, v[96:99] offset:0\n"
      "ds_write_b128 v30, v[100:103] offset:1024\n"
      "ds_write_b128 v30, v[104:107] offset:2048\n"
      "ds_write_b128 v30, v[108:111] offset:3072\n"
      "LBf0:\n"
      "s_add_u32 s3, s50, 8192\n"
      "buffer_load_dwordx4 v[240:243], v1, s[12:15], s3 offen sc1\n"
      "s_add_u32 s3, s50, 12288\n"
      "buffer_load_dwordx4 v[244:247], v1, s[12:15], s3 offen sc1\n"
      "s_cmp_lt_u32 s29, 2\n"
      "s_waitcnt lgkmcnt(0)\n"
      "s_barrier\n"
      "s_cbranch_scc1 LBf1\n"
      "ds_read_b128 v[160:163], v31 offset:0\n"
      "ds_read_b128 v[164:167], v31 offset:1024\n"
      "ds_read_b128 v[168:171], v31 offset:2048\n"
      "ds_read_b128 v[172:175], v31 offset:3072\n"
      "s_waitcnt lgkmcnt(3)\n"
      "v_add_f32 v112, v112, v160\n"
      "v_add_f32 v113, v113, v161\n"
      "v_cvt_pk_bf16_f32 v160, v112, v113\n"
      "v_and_or_b32 v160, v160, v7, s61\n"
      "v_add_f32 v114, v114, v162\n"
      "v_add_f32 v115, v115, v163\n"
      "v_cvt_pk_bf16_f32 v161, v114, v115\n"
      "v_and_or_b32 v161, v161, v7, s62\n"
      "s_waitcnt lgkmcnt(2)\n"
      "v_add_f32 v116, v116, v164\n"
      "v_add_f32 v117, v117, v165\n"
      "v_cvt_pk_bf16_f32 v162, v116, v117\n"
      "v_and_or_b32 v162, v162, v7, s63\n"
      "v_add_f32 v118, v118, v166\n"
      "v_add_f32 v119, v119, v167\n"
      "v_cvt_pk_bf16_f32 v163, v118, v119\n"
      "v_and_or_b32 v163, v163, v7, s64\n"
      "buffer_store_dwordx4 v[160:163], v1, s[12:15], s53 offen offset:0 sc1\n"
      "s_waitcnt lgkmcnt(1)\n"
      "v_add_f32 v120, v120, v168\n"
      "v_add_f32 v121, v121, v169\n"
      "v_cvt_pk_bf16_f32 v164, v120, v121\n"
      "v_and_or_b32 v164, v164, v7, s61\n"
      "v_add_f32 v122, v122, v170\n"
      "v_add_f32 v123, v123, v171\n"
      "v_cvt_pk_bf16_f32 v165, v122, v123\n"
      "v_and_or_b32 v165, v165, v7, s62\n"
      "s_waitcnt lgkmcnt(0)\n"
      "v_add_f32 v124, v124, v172\n"
      "v_add_f32 v125, v125, v173\n"
      "v_cvt_pk_bf16_f32 v166, v124, v125\n"
      "v_and_or_b32 v166, v166, v7, s63\n"
      "v_add_f32 v126, v126, v174\n"
      "v_add_f32 v127, v127, v175\n"
      "v_cvt_pk_bf16_f32 v167, v126, v127\n"
      "v_and_or_b32 v167, v167, v7, s64\n"
      "buffer_store_dwordx4 v[164:167], v1, s[12:15], s53 offen offset:1024 sc1\n"
      "LBf1:\n"
      "s_add_u32 s3, s50, 16384\n"
      "buffer_load_dwordx4 v[128:131], v1, s[12:15], s3 offen sc1\n"
      "s_add_u32 s3, s50, 20480\n"
      "buffer_load_dwordx4 v[132:135], v1, s[12:15], s3 offen sc1\n"
      "s_add_u32 s3, s50, 24576\n"
      "buffer_load_dwordx4 v[136:139], v1, s[12:15], s3 offen sc1\n"
      "s_add_u32 s3, s50, 28672\n"
      "buffer_load_dwordx4 v[140:143], v1, s[12:15], s3 offen sc1\n"
      "s_waitcnt vmcnt(8) lgkmcnt(0)\n"
      "s_barrier\n"
      "ds_read_b64_tr_b16 v[80:81], v20 offset:32768\n"
      "ds_read_b64_tr_b16 v[82:83], v20 offset:33792\n"
      "ds_read_b64_tr_b16 v[84:85], v21 offset:32768\n"
      "ds_read_b64_tr_b16 v[86:87], v21 offset:33792\n"
      "ds_read_b64_tr_b16 v[88:89], v20 offset:40960\n"
      "ds_read_b64_tr_b16 v[90:91], v20 offset:41984\n"
      "ds_read_b64_tr_b16 v[92:93], v21 offset:40960\n"
      "ds_read_b64_tr_b16 v[94:95], v21 offset:41984\n"
      "s_waitcnt vmcnt(7)\n"
      "v_xor_b32 v232, s61, v232\n"
      "v_xor_b32 v233, s62, v233\n"
      "v_xor_b32 v234, s63, v234\n"
      "v_xor_b32 v235, s64, v235\n"
      "s_waitcnt vmcnt(6)\n"
      "v_xor_b32 v236, s61, v236\n"
      "v_xor_b32 v237, s62, v237\n"
      "v_xor_b32 v238, s63, v238\n"
      "v_xor_b32 v239, s64, v239\n"
      "v_or3_b32 v4, v232, v233, v234\n"
      "v_or3_b32 v4, v4, v235, v236\n"
      "v_or3_b32 v4, v4, v237, v238\n"
      "v_or_b32 v4, v4, v239\n"
      "v_and_b32 v4, 0x10001, v4\n"
      "v_cmp_ne_u32 vcc, 0, v4\n"
      "s_nop 1\n"
      "s_cbranch_vccnz LBr18\n"
      "LBo18:\n"
      "s_cmp_lt_u32 s29, 2\n"
      "s_waitcnt lgkmcnt(0)\n"
      "s_cbranch_scc1 LBz0\n"
      "v_mfma_f32_32x32x16_bf16 v[112:127], v[80:83], v[232:235], 0\n"
      "v_mfma_f32_32x32x16_bf16 v[96:111], v[84:87], v[232:235], 0\n"
      "LBz0:\n"
      "s_cbranch_scc0 LBz1\n"
      "v_mfma_f32_32x32x16_bf16 v[112:127], v[80:83], v[232:235], v[112:127]\n"
      "v_mfma_f32_32x32x16_bf16 v[96:111], v[84:87], v[232:235], v[96:111]\n"
      "LBz1:\n"
      "v_mfma_f32_32x32x16_bf16 v[112:127], v[88:91], v[236:239], v[112:127]\n"
      "ds_read_b64_tr_b16 v[64:65], v20 offset:49152\n"
      "ds_read_b64_tr_b16 v[66:67], v20 offset:50176\n"
      "ds_read_b64_tr_b16 v[68:69], v21 offset:49152\n"
      "ds_read_b64_tr_b16 v[70:71], v21 offset:50176\n"
      "v_mfma_f32_32x32x16_bf16 v[96:111], v[92:95], v[236:239], v[96:111]\n"
      "ds_read_b64_tr_b16 v[72:73], v20 offset:57344\n"
      "ds_read_b64_tr_b16 v[74:75], v20 offset:58368\n"
      "ds_read_b64_tr_b16 v[76:77], v21 offset:57344\n"
      "ds_read_b64_tr_b16 v[78:79], v21 offset:58368\n"
      "s_waitcnt vmcnt(5)\n"
      "v_xor_b32 v240, s61, v240\n"
      "v_xor_b32 v241, s62, v241\n"
      "v_xor_b32 v242, s63, v242\n"
      "v_xor_b32 v243, s64, v243\n"
      "s_waitcnt vmcnt(4)\n"
      "v_xor_b32 v244, s61, v244\n"
      "v_xor_b32 v245, s62, v245\n"
      "v_xor_b32 v246, s63, v246\n"
      "v_xor_b32 v247, s64, v247\n"
      "v_or3_b32 v4, v240, v241, v242\n"
      "v_or3_b32 v4, v4, v243, v244\n"
      "v_or3_b32 v4, v4, v245, v246\n"
      "v_or_b32 v4, v4, v247\n"
      "v_and_b32 v4, 0x10001, v4\n"
      "v_cmp_ne_u32 vcc, 0, v4\n"
      "s_nop 1\n"
      "s_cbranch_vccnz LBr19\n"
      "LBo19:\n"
      "s_waitcnt lgkmcnt(6)\n"
      "v_mfma_f32_32x32x16_bf16 v[112:127], v[64:67], v[240:243], v[112:127]\n"
      "ds_read_b64_tr_b16 v[80:81], v20 offset:0\n"
      "ds_read_b64_tr_b16 v[82:83], v20 offset:1024\n"
      "s_waitcnt lgkmcnt(6)\n"
      "v_mfma_f32_32x32x16_bf16 v[96:111], v[68:71], v[240:243], v[96:111]\n"
      "ds_read_b64_tr_b16 v[84:85], v21 offset:0\n"
      "ds_read_b64_tr_b16 v[86:87], v21 offset:1024\n"
      "s_waitcnt vmcnt(3)\n"
      "v_xor_b32 v128, s61, v128\n"
      "v_xor_b32 v129, s62, v129\n"
      "v_xor_b32 v130, s63, v130\n"
      "v_xor_b32 v131, s64, v131\n"
      "s_waitcnt vmcnt(2)\n"
      "v_xor_b32 v132, s61, v132\n"
      "v_xor_b32 v133, s62, v133\n"
      "s_waitcnt lgkmcnt(6)\n"
      "v_mfma_f32_32x32x16_bf16 v[112:127], v[72:75], v[244:247], v[112:127]\n"
      "ds_read_b64_tr_b16 v[88:89], v20 offset:8192\n"
      "ds_read_b64_tr_b16 v[90:91], v20 offset:9216\n"
      "v_xor_b32 v134, s63, v134\n"
      "v_xor_b32 v135, s64, v135\n"
      "v_or3_b32 v4, v128, v129, v130\n"
      "v_or3_b32 v4, v4, v131, v132\n"
      "v_or3_b32 v4, v4, v133, v134\n"
      "v_or_b32 v4, v4, v135\n"
      "s_waitcnt lgkmcnt(6)\n"
      "v_mfma_f32_32x32x16_bf16 v[96:111], v[76:79], v[244:247], v[96:111]\n"
      "ds_read_b64_tr_b16 v[92:93], v21 offset:8192\n"
      "ds_read_b64_tr_b16 v[94:95], v21 offset:9216\n"
      "v_and_b32 v4, 0x10001, v4\n"
      "v_cmp_ne_u32 vcc, 0, v4\n"
      "s_nop 1\n"
      "s_cbranch_vccnz LBr20\n"
      "LBo20:\n"
      "s_waitcnt lgkmcnt(6)\n"
      "v_mfma_f32_32x32x16_bf16 v[112:127], v[80:83], v[128:131], v[112:127]\n"
      "ds_read_b64_tr_b16 v[64:65], v20 offset:16384\n"
      "ds_read_b64_tr_b16 v[66:67], v20 offset:17408\n"
      "s_waitcnt lgkmcnt(6)\n"
      "v_mfma_f32_32x32x16_bf16 v[96:111], v[84:87], v[128:131], v[96:111]\n"
      "ds_read_b64_tr_b16 v[68:69], v21 offset:16384\n"
      "ds_read_b64_tr_b16 v[70:71], v21 offset:17408\n"
      "s_waitcnt vmcnt(1)\n"
      "v_xor_b32 v136, s61, v136\n"
      "v_xor_b32 v137, s62, v137\n"
      "v_xor_b32 v138, s63, v138\n"
      "v_xor_b32 v139, s64, v139\n"
      "s_waitcnt vmcnt(0)\n"
      "v_xor_b32 v140, s61, v140\n"
      "v_xor_b32 v141, s62, v141\n"
      "s_waitcnt lgkmcnt(6)\n"
      "v_mfma_f32_32x32x16_bf16 v[112:127], v[88:91], v[132:135], v[112:127]\n"
      "ds_read_b64_tr_b16 v[72:73], v20 offset:24576\n"
      "ds_read_b64_tr_b16 v[74:75], v20 offset:25600\n"
      "v_xor_b32 v142, s63, v142\n"
      "v_xor_b32 v143, s64, v143\n"
      "v_or3_b32 v4, v136, v137, v138\n"
      "v_or3_b32 v4, v4, v139, v140\n"
      "v_or3_b32 v4, v4, v141, v142\n"
      "v_or_b32 v4, v4, v143\n"
      "s_waitcnt lgkmcnt(6)\n"
      "v_mfma_f32_32x32x16_bf16 v[96:111], v[92:95], v[132:135], v[96:111]\n"
      "ds_read_b64_tr_b16 v[76:77], v21 offset:24576\n"
      "ds_read_b64_tr_b16 v[78:79], v21 offset:25600\n"
      "v_and_b32 v4, 0x10001, v4\n"
      "v_cmp_ne_u32 vcc, 0, v4\n"
      "s_nop 1\n"
      "s_cbranch_vccnz LBr21\n"
      "LBo21:\n"
      "s_waitcnt lgkmcnt(4)\n"
      "v_mfma_f32_32x32x16_bf16 v[96:111], v[68:71], v[136:139], v[96:111]\n"
      "s_waitcnt lgkmcnt(0)\n"
      "v_mfma_f32_32x32x16_bf16 v[96:111], v[76:79], v[140:143], v[96:111]\n"
      "buffer_load_dwordx4 v[248:251], v1, s[12:15], s54 offen sc1\n"
      "v_mfma_f32_32x32x16_bf16 v[112:127], v[64:67], v[136:139], v[112:127]\n"
      "v_mfma_f32_32x32x16_bf16 v[112:127], v[72:75], v[140:143], v[112:127]\n"
      "s_nop 8\n"
      "ds_write_b128 v11, v[96:99] offset:0\n"
      "ds_write_b128 v11, v[100:103] offset:1024\n"
      "ds_write_b128 v8, v[104:107] offset:16384\n"
      "ds_write_b128 v8, v[108:111] offset:17408\n"
      "ds_write_b128 v8, v[120:123] offset:0\n"
      "ds_write_b128 v8, v[124:127] offset:1024\n"
      "v_add_f32 v48, v48, v112\n"
      "v_add_f32 v49, v49, v113\n"
      "v_add_f32 v50, v50, v114\n"
      "v_add_f32 v51, v51, v115\n"
      "v_add_f32 v52, v52, v116\n"
      "v_add_f32 v53, v53, v117\n"
      "v_add_f32 v54, v54, v118\n"
      "v_add_f32 v55, v55, v119\n"
      "s_waitcnt lgkmcnt(0)\n"
      "s_barrier\n"
      "ds_read_b128 v[64:67], v9 offset:0\n"
      "ds_read_b128 v[68:71], v9 offset:1024\n"
      "ds_read_b128 v[72:75], v12 offset:0\n"
      "ds_read_b128 v[76:79], v12 offset:1024\n"
      "ds_read_b128 v[80:83], v10 offset:16384\n"
      "ds_read_b128 v[84:87], v10 offset:17408\n"
      "s_waitcnt lgkmcnt(5)\n"
      "v_add_f32 v48, v48, v64\n"
      "v_add_f32 v49, v49, v65\n"
      "v_add_f32 v50, v50, v66\n"
      "v_add_f32 v51, v51, v67\n"
      "s_waitcnt lgkmcnt(4)\n"
      "v_add_f32 v52, v52, v68\n"
      "v_add_f32 v53, v53, v69\n"
      "v_add_f32 v54, v54, v70\n"
      "v_add_f32 v55, v55, v71\n"
      "s_waitcnt lgkmcnt(3)\n"
      "v_add_f32 v48, v48, v72\n"
      "v_add_f32 v49, v49, v73\n"
      "v_add_f32 v50, v50, v74\n"
      "v_add_f32 v51, v51, v75\n"
      "s_waitcnt lgkmcnt(2)\n"
      "v_add_f32 v52, v52, v76\n"
      "v_add_f32 v53, v53, v77\n"
      "v_add_f32 v54, v54, v78\n"
      "v_add_f32 v55, v55, v79\n"
      "s_waitcnt lgkmcnt(1)\n"
      "v_add_f32 v48, v48, v80\n"
      "v_add_f32 v49, v49, v81\n"
      "v_add_f32 v50, v50, v82\n"
      "v_add_f32 v51, v51, v83\n"
      "s_waitcnt lgkmcnt(0)\n"
      "v_add_f32 v52, v52, v84\n"
      "v_add_f32 v53, v53, v85\n"
      "v_add_f32 v54, v54, v86\n"
      "v_add_f32 v55, v55, v87\n"
      "s_waitcnt vmcnt(0)\n"
      "v_xor_b32 v248, s61, v248\n"
      "v_xor_b32 v249, s62, v249\n"
      "v_xor_b32 v250, s63, v250\n"
      "v_xor_b32 v251, s64, v251\n"
      "v_or3_b32 v4, v248, v249, v250\n"
      "v_or_b32 v4, v4, v251\n"
      "v_and_b32 v4, 0x10001, v4\n"
      "v_cmp_ne_u32 vcc, 0, v4\n"
      "s_nop 1\n"
      "s_cbranch_vccnz LBr22\n"
      "LBo22:\n"
      "v_lshlrev_b32 v4, 16, v248\n"
      "v_and_b32 v5, 0xffff0000, v248\n"
      "v_add_f32 v48, v48, v4\n"
      "v_add_f32 v49, v49, v5\n"
      "v_lshlrev_b32 v4, 16, v249\n"
      "v_and_b32 v5, 0xffff0000, v249\n"
      "v_add_f32 v50, v50, v4\n"
      "v_add_f32 v51, v51, v5\n"
      "v_lshlrev_b32 v4, 16, v250\n"
      "v_and_b32 v5, 0xffff0000, v250\n"
      "v_add_f32 v52, v52, v4\n"
      "v_add_f32 v53, v53, v5\n"
      "v_lshlrev_b32 v4, 16, v251\n"
      "v_and_b32 v5, 0xffff0000, v251\n"
      "v_add_f32 v54, v54, v4\n"
      "v_add_f32 v55, v55, v5\n"
      "v_cvt_pk_bf16_f32 v48, v48, v49\n"
      "v_cvt_pk_bf16_f32 v49, v50, v51\n"
      "v_cvt_pk_bf16_f32 v50, v52, v53\n"
      "v_cvt_pk_bf16_f32 v51, v54, v55\n"
      "s_nop 0\n"
      "v_permlane32_swap_b32 v48, v50\n"
      "v_permlane32_swap_b32 v49, v51\n"
      "buffer_store_dwordx4 v[48:51], v13, s[24:27], s51 offen\n"
      "s_endpgm\n"
      "LBr1:\n"
      "s_sub_u32 s67, s67, 1\n"
      "s_cbranch_scc1 LBr1_x\n"
      "s_sleep 16\n"
      "buffer_load_dwordx4 v[32:35], v36, s[12:15], s56 offen sc1\n"
      "s_waitcnt vmcnt(0)\n"
      "v_xor_b32 v4, s65, v34\n"
      "v_bitop3_b32 v4, v35, s65, v4 bitop3:0xbe\n"
      "v_cmp_ne_u32 vcc, 0, v4\n"
      "s_nop 1\n"
      "s_cbranch_vccnz LBr1\n"
      "s_branch LBo1\n"
      "LBr1_x:\n"
      "s_mov_b32 s67, 0\n"
      "s_branch LBo1\n"
      "LBr2:\n"
      "s_sub_u32 s67, s67, 1\n"
      "s_cbranch_scc1 LBr2_x\n"
      "s_sleep 16\n"
      "s_mov_b32 s3, 32768\n"
      "buffer_load_dwordx4 v[48:51], v1, s[12:15], s3 offen offset:0 sc1\n"
      "s_mov_b32 s3, 32768\n"
      "buffer_load_dwordx4 v[52:55], v1, s[12:15], s3 offen offset:1024 sc1\n"
      "s_waitcnt vmcnt(0)\n"
      "v_xor_b32 v4, s65, v49\n"
      "v_bitop3_b32 v4, v51, s65, v4 bitop3:0xbe\n"
      "v_bitop3_b32 v4, v53, s65, v4 bitop3:0xbe\n"
      "v_bitop3_b32 v4, v55, s65, v4 bitop3:0xbe\n"
      "v_cmp_ne_u32 vcc, 0, v4\n"
      "s_nop 1\n"
      "s_cbranch_vccnz LBr2\n"
      "s_branch LBo2\n"
      "LBr2_x:\n"
      "s_mov_b32 s67, 0\n"
      "s_branch LBo2\n"
      "LBr4:\n"
      "s_sub_u32 s67, s67, 1\n"
      "s_cbranch_scc1 LBr4_x\n"
      "s_sleep 16\n"
      "s_add_u32 s3, s48, 2048\n"
      "buffer_load_dwordx4 v[128:131], v1, s[12:15], s3 offen sc1\n"
      "s_add_u32 s3, s48, 6144\n"
      "buffer_load_dwordx4 v[132:135], v1, s[12:15], s3 offen sc1\n"
      "s_waitcnt vmcnt(0)\n"
      "v_xor_b32 v128, s57, v128\n"
      "v_xor_b32 v129, s58, v129\n"
      "v_xor_b32 v130, s59, v130\n"
      "v_xor_b32 v131, s60, v131\n"
      "v_xor_b32 v132, s57, v132\n"
      "v_xor_b32 v133, s58, v133\n"
      "v_xor_b32 v134, s59, v134\n"
      "v_xor_b32 v135, s60, v135\n"
      "v_or3_b32 v4, v128, v129, v130\n"
      "v_or3_b32 v4, v4, v131, v132\n"
      "v_or3_b32 v4, v4, v133, v134\n"
      "v_or_b32 v4, v4, v135\n"
      "v_and_b32 v4, 0x80008000, v4\n"
      "v_cmp_ne_u32 vcc, 0, v4\n"
      "s_nop 1\n"
      "s_cbranch_vccnz LBr4\n"
      "s_branch LBo4\n"
      "LBr4_x:\n"
      "s_mov_b32 s67, 0\n"
      "s_branch LBo4\n"
      "LBr5:\n"
      "s_sub_u32 s67, s67, 1\n"
      "s_cbranch_scc1 LBr5_x\n"
      "s_sleep 16\n"
      "s_add_u32 s3, s48, 10240\n"
      "buffer_load_dwordx4 v[136:139], v1, s[12:15], s3 offen sc1\n"
      "s_add_u32 s3, s48, 14336\n"
      "buffer_load_dwordx4 v[140:143], v1, s[12:15], s3 offen sc1\n"
      "s_waitcnt vmcnt(0)\n"
      "v_xor_b32 v136, s57, v136\n"
      "v_xor_b32 v137, s58, v137\n"
      "v_xor_b32 v138, s59, v138\n"
      "v_xor_b32 v139, s60, v139\n"
      "v_xor_b32 v140, s57, v140\n"
      "v_xor_b32 v141, s58, v141\n"
      "v_xor_b32 v142, s59, v142\n"
      "v_xor_b32 v143, s60, v143\n"
      "v_or3_b32 v4, v136, v137, v138\n"
      "v_or3_b32 v4, v4, v139, v140\n"
      "v_or3_b32 v4, v4, v141, v142\n"
      "v_or_b32 v4, v4, v143\n"
      "v_and_b32 v4, 0x80008000, v4\n"
      "v_cmp_ne_u32 vcc, 0, v4\n"
      "s_nop 1\n"
      "s_cbranch_vccnz LBr5\n"
      "s_branch LBo5\n"
      "LBr5_x:\n"
      "s_mov_b32 s67, 0\n"
      "s_branch LBo5\n"
      "LBr6:\n"
      "s_sub_u32 s67, s67, 1\n"
      "s_cbranch_scc1 LBr6_x\n"
      "s_sleep 16\n"
      "s_add_u32 s3, s48, 18432\n"
      "buffer_load_dwordx4 v[144:147], v1, s[12:15], s3 offen sc1\n"
      "s_add_u32 s3, s48, 22528\n"
      "buffer_load_dwordx4 v[148:151], v1, s[12:15], s3 offen sc1\n"
      "s_waitcnt vmcnt(0)\n"
      "v_xor_b32 v144, s57, v144\n"
      "v_xor_b32 v145, s58, v145\n"
      "v_xor_b32 v146, s59, v146\n"
      "v_xor_b32 v147, s60, v147\n"
      "v_xor_b32 v148, s57, v148\n"
      "v_xor_b32 v149, s58, v149\n"
      "v_xor_b32 v150, s59, v150\n"
      "v_xor_b32 v151, s60, v151\n"
      "v_or3_b32 v4, v144, v145, v146\n"
      "v_or3_b32 v4, v4, v147, v148\n"
      "v_or3_b32 v4, v4, v149, v150\n"
      "v_or_b32 v4, v4, v151\n"
      "v_and_b32 v4, 0x80008000, v4\n"
      "v_cmp_ne_u32 vcc, 0, v4\n"
      "s_nop 1\n"
      "s_cbranch_vccnz LBr6\n"
      "s_branch LBo6\n"
      "LBr6_x:\n"
      "s_mov_b32 s67, 0\n"
      "s_branch LBo6\n"
      "LBr7:\n"
      "s_sub_u32 s67, s67, 1\n"
      "s_cbranch_scc1 LBr7_x\n"
      "s_sleep 16\n"
      "s_add_u32 s3, s48, 26624\n"
      "buffer_load_dwordx4 v[152:155], v1, s[12:15], s3 offen sc1\n"
      "s_add_u32 s3, s48, 30720\n"
      "buffer_load_dwordx4 v[156:159], v1, s[12:15], s3 offen sc1\n"
      "s_waitcnt vmcnt(0)\n"
      "v_xor_b32 v152, s57, v152\n"
      "v_xor_b32 v153, s58, v153\n"
      "v_xor_b32 v154, s59, v154\n"
      "v_xor_b32 v155, s60, v155\n"
      "v_xor_b32 v156, s57, v156\n"
      "v_xor_b32 v157, s58, v157\n"
      "v_xor_b32 v158, s59, v158\n"
      "v_xor_b32 v159, s60, v159\n"
      "v_or3_b32 v4, v152, v153, v154\n"
      "v_or3_b32 v4, v4, v155, v156\n"
      "v_or3_b32 v4, v4, v157, v158\n"
      "v_or_b32 v4, v4, v159\n"
      "v_and_b32 v4, 0x80008000, v4\n"
      "v_cmp_ne_u32 vcc, 0, v4\n"
      "s_nop 1\n"
      "s_cbranch_vccnz LBr7\n"
      "s_branch LBo7\n"
      "LBr7_x:\n"
      "s_mov_b32 s67, 0\n"
      "s_branch LBo7\n"
      "LBr8:\n"
      "s_sub_u32 s67, s67, 1\n"
      "s_cbranch_scc1 LBr8_x\n"
      "s_sleep 16\n"
      "s_add_u32 s3, s48, 34816\n"
      "buffer_load_dwordx4 v[160:163], v1, s[12:15], s3 offen sc1\n"
      "s_add_u32 s3, s48, 38912\n"
      "buffer_load_dwordx4 v[164:167], v1, s[12:15], s3 offen sc1\n"
      "s_waitcnt vmcnt(0)\n"
      "v_xor_b32 v160, s57, v160\n"
      "v_xor_b32 v161, s58, v161\n"
      "v_xor_b32 v162, s59, v162\n"
      "v_xor_b32 v163, s60, v163\n"
      "v_xor_b32 v164, s57, v164\n"
      "v_xor_b32 v165, s58, v165\n"
      "v_xor_b32 v166, s59, v166\n"
      "v_xor_b32 v167, s60, v167\n"
      "v_or3_b32 v4, v160, v161, v162\n"
      "v_or3_b32 v4, v4, v163, v164\n"
      "v_or3_b32 v4, v4, v165, v166\n"
      "v_or_b32 v4, v4, v167\n"
      "v_and_b32 v4, 0x80008000, v4\n"
      "v_cmp_ne_u32 vcc, 0, v4\n"
      "s_nop 1\n"
      "s_cbranch_vccnz LBr8\n"
      "s_branch LBo8\n"
      "LBr8_x:\n"
      "s_mov_b32 s67, 0\n"
      "s_branch LBo8\n"
      "LBr10:\n"
      "s_sub_u32 s67, s67, 1\n"
      "s_cbranch_scc1 LBr10_x\n"
      "s_sleep 16\n"
      "s_add_u32 s3, s48, 43008\n"
      "buffer_load_dwordx4 v[168:171], v1, s[12:15], s3 offen sc1\n"
      "s_add_u32 s3, s48, 47104\n"
      "buffer_load_dwordx4 v[172:175], v1, s[12:15], s3 offen sc1\n"
      "s_waitcnt vmcnt(0)\n"
      "v_xor_b32 v168, s57, v168\n"
      "v_xor_b32 v169, s58, v169\n"
      "v_xor_b32 v170, s59, v170\n"
      "v_xor_b32 v171, s60, v171\n"
      "v_xor_b32 v172, s57, v172\n"
      "v_xor_b32 v173, s58, v173\n"
      "v_xor_b32 v174, s59, v174\n"
      "v_xor_b32 v175, s60, v175\n"
      "v_or3_b32 v4, v168, v169, v170\n"
      "v_or3_b32 v4, v4, v171, v172\n"
      "v_or3_b32 v4, v4, v173, v174\n"
      "v_or_b32 v4, v4, v175\n"
      "v_and_b32 v4, 0x80008000, v4\n"
      "v_cmp_ne_u32 vcc, 0, v4\n"
      "s_nop 1\n"
      "s_cbranch_vccnz LBr10\n"
      "s_branch LBo10\n"
      "LBr10_x:\n"
      "s_mov_b32 s67, 0\n"
      "s_branch LBo10\n"
      "LBr11:\n"
      "s_sub_u32 s67, s67, 1\n"
      "s_cbranch_scc1 LBr11_x\n"
      "s_sleep 16\n"
      "s_add_u32 s3, s48, 51200\n"
      "buffer_load_dwordx4 v[176:179], v1, s[12:15], s3 offen sc1\n"
      "s_add_u32 s3, s48, 55296\n"
      "buffer_load_dwordx4 v[180:183], v1, s[12:15], s3 offen sc1\n"
      "s_waitcnt vmcnt(0)\n"
      "v_xor_b32 v176, s57, v176\n"
      "v_xor_b32 v177, s58, v177\n"
      "v_xor_b32 v178, s59, v178\n"
      "v_xor_b32 v179, s60, v179\n"
      "v_xor_b32 v180, s57, v180\n"
      "v_xor_b32 v181, s58, v181\n"
      "v_xor_b32 v182, s59, v182\n"
      "v_xor_b32 v183, s60, v183\n"
      "v_or3_b32 v4, v176, v177, v178\n"
      "v_or3_b32 v4, v4, v179, v180\n"
      "v_or3_b32 v4, v4, v181, v182\n"
      "v_or_b32 v4, v4, v183\n"
      "v_and_b32 v4, 0x80008000, v4\n"
      "v_cmp_ne_u32 vcc, 0, v4\n"
      "s_nop 1\n"
      "s_cbranch_vccnz LBr11\n"
      "s_branch LBo11\n"
      "LBr11_x:\n"
      "s_mov_b32 s67, 0\n"
      "s_branch LBo11\n"
      "LBr12:\n"
      "s_sub_u32 s67, s67, 1\n"
      "s_cbranch_scc1 LBr12_x\n"
      "s_sleep 16\n"
      "s_add_u32 s3, s48, 59392\n"
      "buffer_load_dwordx4 v[184:187], v1, s[12:15], s3 offen sc1\n"
      "s_add_u32 s3, s48, 63488\n"
      "buffer_load_dwordx4 v[188:191], v1, s[12:15], s3 offen sc1\n"
      "s_waitcnt vmcnt(0)\n"
      "v_xor_b32 v184, s57, v184\n"
      "v_xor_b32 v185, s58, v185\n"
      "v_xor_b32 v186, s59, v186\n"
      "v_xor_b32 v187, s60, v187\n"
      "v_xor_b32 v188, s57, v188\n"
      "v_xor_b32 v189, s58, v189\n"
      "v_xor_b32 v190, s59, v190\n"
      "v_xor_b32 v191, s60, v191\n"
      "v_or3_b32 v4, v184, v185, v186\n"
      "v_or3_b32 v4, v4, v187, v188\n"
      "v_or3_b32 v4, v4, v189, v190\n"
      "v_or_b32 v4, v4, v191\n"
      "v_and_b32 v4, 0x80008000, v4\n"
      "v_cmp_ne_u32 vcc, 0, v4\n"
      "s_nop 1\n"
      "s_cbranch_vccnz LBr12\n"
      "s_branch LBo12\n"
      "LBr12_x:\n"
      "s_mov_b32 s67, 0\n"
      "s_branch LBo12\n"
      "LBr13:\n"
      "s_sub_u32 s67, s67, 1\n"
      "s_cbranch_scc1 LBr13_x\n"
      "s_sleep 16\n"
      "s_add_u32 s3, s48, 69632\n"
      "buffer_load_dwordx4 v[192:195], v1, s[12:15], s3 offen sc1\n"
      "s_add_u32 s3, s48, 71680\n"
      "buffer_load_dwordx4 v[196:199], v1, s[12:15], s3 offen sc1\n"
      "s_waitcnt vmcnt(0)\n"
      "v_xor_b32 v192, s57, v192\n"
      "v_xor_b32 v193, s58, v193\n"
      "v_xor_b32 v194, s59, v194\n"
      "v_xor_b32 v195, s60, v195\n"
      "v_xor_b32 v196, s57, v196\n"
      "v_xor_b32 v197, s58, v197\n"
      "v_xor_b32 v198, s59, v198\n"
      "v_xor_b32 v199, s60, v199\n"
      "v_or3_b32 v4, v192, v193, v194\n"
      "v_or3_b32 v4, v4, v195, v196\n"
      "v_or3_b32 v4, v4, v197, v198\n"
      "v_or_b32 v4, v4, v199\n"
      "v_and_b32 v4, 0x80008000, v4\n"
      "v_cmp_ne_u32 vcc, 0, v4\n"
      "s_nop 1\n"
      "s_cbranch_vccnz LBr13\n"
      "s_branch LBo13\n"
      "LBr13_x:\n"
      "s_mov_b32 s67, 0\n"
      "s_branch LBo13\n"
      "LBr14:\n"
      "s_sub_u32 s67, s67, 1\n"
      "s_cbranch_scc1 LBr14_x\n"
      "s_sleep 16\n"
      "s_add_u32 s3, s48, 73728\n"
      "buffer_load_dwordx4 v[200:203], v1, s[12:15], s3 offen sc1\n"
      "s_add_u32 s3, s48, 75776\n"
      "buffer_load_dwordx4 v[204:207], v1, s[12:15], s3 offen sc1\n"
      "s_waitcnt vmcnt(0)\n"
      "v_xor_b32 v200, s57, v200\n"
      "v_xor_b32 v201, s58, v201\n"
      "v_xor_b32 v202, s59, v202\n"
      "v_xor_b32 v203, s60, v203\n"
      "v_xor_b32 v204, s57, v204\n"
      "v_xor_b32 v205, s58, v205\n"
      "v_xor_b32 v206, s59, v206\n"
      "v_xor_b32 v207, s60, v207\n"
      "v_or3_b32 v4, v200, v201, v202\n"
      "v_or3_b32 v4, v4, v203, v204\n"
      "v_or3_b32 v4, v4, v205, v206\n"
      "v_or_b32 v4, v4, v207\n"
      "v_and_b32 v4, 0x80008000, v4\n"
      "v_cmp_ne_u32 vcc, 0, v4\n"
      "s_nop 1\n"
      "s_cbranch_vccnz LBr14\n"
      "s_branch LBo14\n"
      "LBr14_x:\n"
      "s_mov_b32 s67, 0\n"
      "s_branch LBo14\n"
      "LBr15:\n"
      "s_sub_u32 s67, s67, 1\n"
      "s_cbranch_scc1 LBr15_x\n"
      "s_sleep 16\n"
      "s_add_u32 s3, s48, 77824\n"
      "buffer_load_dwordx4 v[208:211], v1, s[12:15], s3 offen sc1\n"
      "s_add_u32 s3, s48, 79872\n"
      "buffer_load_dwordx4 v[212:215], v1, s[12:15], s3 offen sc1\n"
      "s_waitcnt vmcnt(0)\n"
      "v_xor_b32 v208, s57, v208\n"
      "v_xor_b32 v209, s58, v209\n"
      "v_xor_b32 v210, s59, v210\n"
      "v_xor_b32 v211, s60, v211\n"
      "v_xor_b32 v212, s57, v212\n"
      "v_xor_b32 v213, s58, v213\n"
      "v_xor_b32 v214, s59, v214\n"
      "v_xor_b32 v215, s60, v215\n"
      "v_or3_b32 v4, v208, v209, v210\n"
      "v_or3_b32 v4, v4, v211, v212\n"
      "v_or3_b32 v4, v4, v213, v214\n"
      "v_or_b32 v4, v4, v215\n"
      "v_and_b32 v4, 0x80008000, v4\n"
      "v_cmp_ne_u32 vcc, 0, v4\n"
      "s_nop 1\n"
      "s_cbranch_vccnz LBr15\n"
      "s_branch LBo15\n"
      "LBr15_x:\n"
      "s_mov_b32 s67, 0\n"
      "s_branch LBo15\n"
      "LBr16:\n"
      "s_sub_u32 s67, s67, 1\n"
      "s_cbranch_scc1 LBr16_x\n"
      "s_sleep 16\n"
      "s_add_u32 s3, s48, 81920\n"
      "buffer_load_dwordx4 v[216:219], v1, s[12:15], s3 offen sc1\n"
      "s_add_u32 s3, s48, 83968\n"
      "buffer_load_dwordx4 v[220:223], v1, s[12:15], s3 offen sc1\n"
      "s_waitcnt vmcnt(0)\n"
      "v_xor_b32 v216, s57, v216\n"
      "v_xor_b32 v217, s58, v217\n"
      "v_xor_b32 v218, s59, v218\n"
      "v_xor_b32 v219, s60, v219\n"
      "v_xor_b32 v220, s57, v220\n"
      "v_xor_b32 v221, s58, v221\n"
      "v_xor_b32 v222, s59, v222\n"
      "v_xor_b32 v223, s60, v223\n"
      "v_or3_b32 v4, v216, v217, v218\n"
      "v_or3_b32 v4, v4, v219, v220\n"
      "v_or3_b32 v4, v4, v221, v222\n"
      "v_or_b32 v4, v4, v223\n"
      "v_and_b32 v4, 0x80008000, v4\n"
      "v_cmp_ne_u32 vcc, 0, v4\n"
      "s_nop 1\n"
      "s_cbranch_vccnz LBr16\n"
      "s_branch LBo16\n"
      "LBr16_x:\n"
      "s_mov_b32 s67, 0\n"
      "s_branch LBo16\n"
      "LBr17:\n"
      "s_sub_u32 s67, s67, 1\n"
      "s_cbranch_scc1 LBr17_x\n"
      "s_sleep 16\n"
      "s_add_u32 s3, s48, 86016\n"
      "buffer_load_dwordx4 v[224:227], v1, s[12:15], s3 offen sc1\n"
      "s_add_u32 s3, s48, 86016\n"
      "buffer_load_dwordx4 v[228:231], v1, s[12:15], s3 offen sc1\n"
      "s_waitcnt vmcnt(0)\n"
      "v_xor_b32 v224, s57, v224\n"
      "v_xor_b32 v225, s58, v225\n"
      "v_xor_b32 v226, s59, v226\n"
      "v_xor_b32 v227, s60, v227\n"
      "v_xor_b32 v228, s57, v228\n"
      "v_xor_b32 v229, s58, v229\n"
      "v_xor_b32 v230, s59, v230\n"
      "v_xor_b32 v231, s60, v231\n"
      "v_or3_b32 v4, v224, v225, v226\n"
      "v_or3_b32 v4, v4, v227, v228\n"
      "v_or3_b32 v4, v4, v229, v230\n"
      "v_or_b32 v4, v4, v231\n"
      "v_and_b32 v4, 0x80008000, v4\n"
      "v_cmp_ne_u32 vcc, 0, v4\n"
      "s_nop 1\n"
      "s_cbranch_vccnz LBr17\n"
      "s_branch LBo17\n"
      "LBr17_x:\n"
      "s_mov_b32 s67, 0\n"
      "s_branch LBo17\n"
      "LBr18:\n"
      "s_sub_u32 s67, s67, 1\n"
      "s_cbranch_scc1 LBr18_x\n"
      "s_sleep 16\n"
      "buffer_load_dwordx4 v[232:235], v1, s[12:15], s50 offen sc1\n"
      "s_add_u32 s3, s50, 4096\n"
      "buffer_load_dwordx4 v[236:239], v1, s[12:15], s3 offen sc1\n"
      "s_waitcnt vmcnt(0)\n"
      "v_xor_b32 v232, s61, v232\n"
      "v_xor_b32 v233, s62, v233\n"
      "v_xor_b32 v234, s63, v234\n"
      "v_xor_b32 v235, s64, v235\n"
      "v_xor_b32 v236, s61, v236\n"
      "v_xor_b32 v237, s62, v237\n"
      "v_xor_b32 v238, s63, v238\n"
      "v_xor_b32 v239, s64, v239\n"
      "v_or3_b32 v4, v232, v233, v234\n"
      "v_or3_b32 v4, v4, v235, v236\n"
      "v_or3_b32 v4, v4, v237, v238\n"
      "v_or_b32 v4, v4, v239\n"
      "v_and_b32 v4, 0x10001, v4\n"
      "v_cmp_ne_u32 vcc, 0, v4\n"
      "s_nop 1\n"
      "s_cbranch_vccnz LBr18\n"
      "s_branch LBo18\n"
      "LBr18_x:\n"
      "s_mov_b32 s67, 0\n"
      "s_branch LBo18\n"
      "LBr19:\n"
      "s_sub_u32 s67, s67, 1\n"
      "s_cbranch_scc1 LBr19_x\n"
      "s_sleep 16\n"
      "s_add_u32 s3, s50, 8192\n"
      "buffer_load_dwordx4 v[240:243], v1, s[12:15], s3 offen sc1\n"
      "s_add_u32 s3, s50, 12288\n"
      "buffer_load_dwordx4 v[244:247], v1, s[12:15], s3 offen sc1\n"
      "s_waitcnt vmcnt(0)\n"
      "v_xor_b32 v240, s61, v240\n"
      "v_xor_b32 v241, s62, v241\n"
      "v_xor_b32 v242, s63, v242\n"
      "v_xor_b32 v243, s64, v243\n"
      "v_xor_b32 v244, s61, v244\n"
      "v_xor_b32 v245, s62, v245\n"
      "v_xor_b32 v246, s63, v246\n"
      "v_xor_b32 v247, s64, v247\n"
      "v_or3_b32 v4, v240, v241, v242\n"
      "v_or3_b32 v4, v4, v243, v244\n"
      "v_or3_b32 v4, v4, v245, v246\n"
      "v_or_b32 v4, v4, v247\n"
      "v_and_b32 v4, 0x10001, v4\n"
      "v_cmp_ne_u32 vcc, 0, v4\n"
      "s_nop 1\n"
      "s_cbranch_vccnz LBr19\n"
      "s_branch LBo19\n"
      "LBr19_x:\n"
      "s_mov_b32 s67, 0\n"
      "s_branch LBo19\n"
      "LBr20:\n"
      "s_sub_u32 s67, s67, 1\n"
      "s_cbranch_scc1 LBr20_x\n"
      "s_sleep 16\n"
      "s_add_u32 s3, s50, 16384\n"
      "buffer_load_dwordx4 v[128:131], v1, s[12:15], s3 offen sc1\n"
      "s_add_u32 s3, s50, 20480\n"
      "buffer_load_dwordx4 v[132:135], v1, s[12:15], s3 offen sc1\n"
      "s_waitcnt vmcnt(0)\n"
      "v_xor_b32 v128, s61, v128\n"
      "v_xor_b32 v129, s62, v129\n"
      "v_xor_b32 v130, s63, v130\n"
      "v_xor_b32 v131, s64, v131\n"
      "v_xor_b32 v132, s61, v132\n"
      "v_xor_b32 v133, s62, v133\n"
      "v_xor_b32 v134, s63, v134\n"
      "v_xor_b32 v135, s64, v135\n"
      "v_or3_b32 v4, v128, v129, v130\n"
      "v_or3_b32 v4, v4, v131, v132\n"
      "v_or3_b32 v4, v4, v133, v134\n"
      "v_or_b32 v4, v4, v135\n"
      "v_and_b32 v4, 0x10001, v4\n"
      "v_cmp_ne_u32 vcc, 0, v4\n"
      "s_nop 1\n"
      "s_cbranch_vccnz LBr20\n"
      "s_branch LBo20\n"
      "LBr20_x:\n"
      "s_mov_b32 s67, 0\n"
      "s_branch LBo20\n"
      "LBr21:\n"
      "s_sub_u32 s67, s67, 1\n"
      "s_cbranch_scc1 LBr21_x\n"
      "s_sleep 16\n"
      "s_add_u32 s3, s50, 24576\n"
      "buffer_load_dwordx4 v[136:139], v1, s[12:15], s3 offen sc1\n"
      "s_add_u32 s3, s50, 28672\n"
      "buffer_load_dwordx4 v[140:143], v1, s[12:15], s3 offen sc1\n"
      "s_waitcnt vmcnt(0)\n"
      "v_xor_b32 v136, s61, v136\n"
      "v_xor_b32 v137, s62, v137\n"
      "v_xor_b32 v138, s63, v138\n"
      "v_xor_b32 v139, s64, v139\n"
      "v_xor_b32 v140, s61, v140\n"
      "v_xor_b32 v141, s62, v141\n"
      "v_xor_b32 v142, s63, v142\n"
      "v_xor_b32 v143, s64, v143\n"
      "v_or3_b32 v4, v136, v137, v138\n"
      "v_or3_b32 v4, v4, v139, v140\n"
      "v_or3_b32 v4, v4, v141, v142\n"
      "v_or_b32 v4, v4, v143\n"
      "v_and_b32 v4, 0x10001, v4\n"
      "v_cmp_ne_u32 vcc, 0, v4\n"
      "s_nop 1\n"
      "s_cbranch_vccnz LBr21\n"
      "s_branch LBo21\n"
      "LBr21_x:\n"
      "s_mov_b32 s67, 0\n"
      "s_branch LBo21\n"
      "LBr22:\n"
      "s_sub_u32 s67, s67, 1\n"
      "s_cbranch_scc1 LBr22_x\n"
      "s_sleep 2\n"
      "buffer_load_dwordx4 v[248:251], v1, s[12:15], s54 offen sc1\n"
      "s_waitcnt vmcnt(0)\n"
      "v_xor_b32 v248, s61, v248\n"
      "v_xor_b32 v249, s62, v249\n"
      "v_xor_b32 v250, s63, v250\n"
      "v_xor_b32 v251, s64, v251\n"
      "v_or3_b32 v4, v248, v249, v250\n"
      "v_or_b32 v4, v4, v251\n"
      "v_and_b32 v4, 0x10001, v4\n"
      "v_cmp_ne_u32 vcc, 0, v4\n"
      "s_nop 1\n"
      "s_cbranch_vccnz LBr22\n"
      "s_branch LBo22\n"
      "LBr22_x:\n"
      "s_mov_b32 s67, 0\n"
      "s_branch LBo22\n"
      "Lend:\n"
      :
      : "{s[0:1]}"(__builtin_amdgcn_kernarg_segment_ptr()), "{s2}"(blockIdx.x), "{v0}"(threadIdx.x),
        "i"((__attribute__((address_space(3))) char*)smem)
      : "v1", "v2", "v3", "v4", "v5", "v6", "v7", "v8", "v9", "v10", "v11", "v12", "v13", "v14", "v15", "v16", "v17", "v18", "v19", "v20", "v21", "v22", "v23", "v24", "v25", "v26", "v27", "v28", "v29", "v30", "v31", "v32", "v33", "v34", "v35", "v36", "v37", "v38", "v39", "v40", "v41", "v42", "v43", "v44", "v45", "v46", "v47", "v48", "v49", "v50", "v51", "v52", "v53", "v54", "v55", "v56", "v57", "v58", "v59", "v60", "v61", "v62", "v63", "v64", "v65", "v66", "v67", "v68", "v69", "v70", "v71", "v72", "v73", "v74", "v75", "v76", "v77", "v78", "v79", "v80", "v81", "v82", "v83", "v84", "v85", "v86", "v87", "v88", "v89", "v90", "v91", "v92", "v93", "v94", "v95", "v96", "v97", "v98", "v99", "v100", "v101", "v102", "v103", "v104", "v105", "v106", "v107", "v108", "v109", "v110", "v111", "v112", "v113", "v114", "v115", "v116", "v117", "v118", "v119", "v120", "v121", "v122", "v123", "v124", "v125", "v126", "v127", "v128", "v129", "v130", "v131", "v132", "v133", "v134", "v135", "v136", "v137", "v138", "v139", "v140", "v141", "v142", "v143", "v144", "v145", "v146", "v147", "v148", "v149", "v150", "v151", "v152", "v153", "v154", "v155", "v156", "v157", "v158", "v159", "v160", "v161", "v162", "v163", "v164", "v165", "v166", "v167", "v168", "v169", "v170", "v171", "v172", "v173", "v174", "v175", "v176", "v177", "v178", "v179", "v180", "v181", "v182", "v183", "v184", "v185", "v186", "v187", "v188", "v189", "v190", "v191", "v192", "v193", "v194", "v195", "v196", "v197", "v198", "v199", "v200", "v201", "v202", "v203", "v204", "v205", "v206", "v207", "v208", "v209", "v210", "v211", "v212", "v213", "v214", "v215", "v216", "v217", "v218", "v219", "v220", "v221", "v222", "v223", "v224", "v225", "v226", "v227", "v228", "v229", "v230", "v231", "v232", "v233", "v234", "v235", "v236", "v237", "v238", "v239", "v240", "v241", "v242", "v243", "v244", "v245", "v246", "v247", "v248", "v249", "v250", "v251", "v252", "v253", "v254", "v255", "s3", "s4", "s5", "s6", "s7", "s8", "s9", "s10", "s11", "s12", "s13", "s14", "s15", "s16", "s17", "s18", "s19", "s20", "s21", "s22", "s23", "s24", "s25", "s26", "s27", "s28", "s29", "s30", "s31", "s32", "s33", "s34", "s35", "s36", "s37", "s38", "s39", "s40", "s41", "s42", "s43", "s44", "s45", "s46", "s47", "s48", "s49", "s50", "s51", "s52", "s53", "s54", "s55", "s56", "s57", "s58", "s59", "s60", "s61", "s62", "s63", "s64", "s65", "s66", "s67", "s68", "s69", "s70", "s71", "s72", "s73", "s74", "s75", "s76", "s77", "s78", "s79", "s80", "s81", "s82", "s83", "s84", "s85", "s86", "s87", "s88", "s89", "s90", "s91", "s92", "s93", "s94", "s95", "s96", "s97", "s98", "s99", "s100", "s101", "vcc", "scc", "memory");
#endif
}

extern "C" void kernel_launch(void* const* d_in, const int* in_sizes, int n_in, void* d_out, int out_size, void* d_ws,
                              size_t ws_size, hipStream_t stream) {
  fused_block<<<256, 512, 0, stream>>>(d_in[0], d_in[1], d_in[2], d_in[4], d_in[5], d_out, d_ws);
}
```
